```python
import torch

def _softmax(x, axis=-1):
    return torch.softmax(x, dim=axis)
def _gelu(x):
    return torch.nn.functional.gelu(x, approximate="tanh")
def _silu(x):
    return torch.nn.functional.silu(x)
def _sigmoid(x):
    return torch.sigmoid(x)
def _layernorm(x, axis=-1, eps=1e-5):
    m = x.mean(dim=axis, keepdim=True)
    v = x.var(dim=axis, keepdim=True, unbiased=False)
    return (x - m) / torch.sqrt(v + eps)
def _scatter_add(tgt, idx, src, axis=0):
    return tgt.clone().index_add_(axis, idx, src)
def _segment_sum(src, seg, n):
    out = torch.zeros((n,) + src.shape[1:], dtype=src.dtype)
    return out.index_add_(0, seg, src)
def _pad(x, pw):
    flat = tuple(v for pair in reversed(pw) for v in pair)
    return torch.nn.functional.pad(x, flat)

kernel_name = 'standalone__gqa_softmax__near_ridge'

def setup_inputs():

    _w = (lambda a: (lambda t: t.to(torch.bfloat16) if t.is_floating_point() else t)(torch.as_tensor(a)))
    B, Hq, Hkv, Sq, Sk, D = 16, 32, 16, 288, 512, 128
    return {
        "q": _w((lambda *s: torch.randn(*s, dtype=torch.bfloat16) * 0.8)(B, Hq, Sq, D)),
        "k": _w((lambda *s: torch.randn(*s, dtype=torch.bfloat16) * 0.8)(B, Hkv, Sk, D)),
        "v": _w((lambda *s: torch.randn(*s, dtype=torch.bfloat16) * 0.05)(B, Hkv, Sk, D)),
    }

def reference(q, k, v):
    B, Hq, Sq, D = q.shape
    Hkv = k.shape[1]
    rep = Hq // Hkv
    k = (lambda x, n, axis: torch.repeat_interleave(x, n, dim=axis))(k, rep, axis=1)
    v = (lambda x, n, axis: torch.repeat_interleave(x, n, dim=axis))(v, rep, axis=1)
    scale = D ** -0.5
    scores = torch.einsum("bhqd,bhkd->bhqk", q, k) * scale
    p = _softmax(scores, axis=-1)
    return torch.einsum("bhqk,bhkd->bhqd", p, v)
```

```cpp
#include <hip/hip_runtime.h>
#include <hip/hip_bf16.h>

typedef float    f32x16 __attribute__((ext_vector_type(16)));
typedef short    bf16x8 __attribute__((ext_vector_type(8)));
typedef short    s16x4  __attribute__((ext_vector_type(4)));
typedef unsigned u32x4  __attribute__((ext_vector_type(4)));
typedef unsigned u32x2  __attribute__((ext_vector_type(2)));

constexpr int NQ = 576, NK = 512, D = 128;

__device__ __forceinline__ unsigned cvtpk(float lo, float hi) {
  unsigned r; asm volatile("v_cvt_pk_bf16_f32 %0, %1, %2" : "=v"(r) : "v"(lo), "v"(hi)); return r;
}
template <int OFF> __device__ __forceinline__ s16x4 tr_read(int vb) {
  s16x4 r; asm volatile("ds_read_b64_tr_b16 %0, %1 offset:%2" : "=&v"(r) : "v"(vb), "i"(OFF) : "memory"); return r;
}
__device__ __forceinline__ int koff(int row, int ch) { return 1024 * (row >> 2) + 256 * (ch >> 2) + 64 * (row & 3) + 16 * ((ch & 3) ^ ((row >> 2) & 3)); }
__device__ __forceinline__ int voff(int row, int ch) { return 1024 * (row >> 2) + 256 * (ch >> 2) + 64 * (row & 3) + 16 * (ch & 3); }

template <int S, int DB> __device__ __forceinline__ bf16x8 vfrag(int vb) {
  s16x4 l = tr_read<4096 * S + 256 * DB>(vb), h = tr_read<4096 * S + 2048 + 256 * DB>(vb);
  asm volatile("s_waitcnt lgkmcnt(0)" : "+v"(l), "+v"(h) :: "memory");
  return (bf16x8){l[0], l[1], l[2], l[3], h[0], h[1], h[2], h[3]};
}

__global__ __launch_bounds__(256, 1) void attn_base(const unsigned short* __restrict__ Q, const unsigned short* __restrict__ K,
                                                    const unsigned short* __restrict__ V, unsigned short* __restrict__ O) {
  __shared__ __attribute__((aligned(16))) char lds[32768];
  const int tid = threadIdx.x, lane = tid & 63, wid = tid >> 6, r32 = lane & 31, hi = lane >> 5;
  const long bh = blockIdx.x;
  const unsigned short* Qb = Q + bh * NQ * D; const unsigned short* Kb = K + bh * NK * D; const unsigned short* Vb = V + bh * NK * D;
  unsigned short* Ob = O + bh * NQ * D;
  const float C = 0.12751743082f;
  const int vb = (int)(size_t)(lds + 16384) + 1024 * hi + 64 * ((lane >> 2) & 3) + 32 * ((lane >> 4) & 1) + 8 * (lane & 3);
  for (int it = 0; it < 5; ++it) {
    const int t = wid + 4 * it; const bool act = t < 18; const int tt = act ? t : 0;
    bf16x8 qr[8];
#pragma unroll
    for (int s = 0; s < 8; ++s) qr[s] = *(const bf16x8*)(Qb + (tt * 32 + r32) * D + 16 * s + 8 * hi);
    float m = -1e30f, l = 0.f; f32x16 o[4] = {};
    for (int kt = 0; kt < 8; ++kt) {
      __syncthreads();
#pragma unroll
      for (int i = 0; i < 4; ++i) { int c = tid + 256 * i; int row = c >> 4, ch = c & 15;
        *(u32x4*)(lds + koff(row, ch)) = *(const u32x4*)(Kb + (kt * 64 + row) * D + ch * 8);
        *(u32x4*)(lds + 16384 + voff(row, ch)) = *(const u32x4*)(Vb + (kt * 64 + row) * D + ch * 8); }
      __syncthreads();
#pragma unroll
      for (int kb = 0; kb < 2; ++kb) {
        f32x16 s = {};
#pragma unroll
        for (int d0 = 0; d0 < 8; ++d0) {
          bf16x8 kf = *(const bf16x8*)(lds + koff(kb * 32 + r32, 2 * d0 + hi));
          s = __builtin_amdgcn_mfma_f32_32x32x16_bf16(kf, qr[d0], s, 0, 0, 0);
        }
        float pm = s[0];
#pragma unroll
        for (int r = 1; r < 16; ++r) pm = fmaxf(pm, s[r]);
        pm = fmaxf(pm, __shfl_xor(pm, 32));
        float mn = fmaxf(m, pm), al = __builtin_amdgcn_exp2f((m - mn) * C); m = mn;
        float ps = 0.f;
#pragma unroll
        for (int r = 0; r < 16; ++r) { s[r] = __builtin_amdgcn_exp2f((s[r] - mn) * C); ps += s[r]; }
        l = l * al + ps;
#pragma unroll
        for (int d = 0; d < 4; ++d)
#pragma unroll
          for (int r = 0; r < 16; ++r) o[d][r] *= al;
        u32x4 p0 = {cvtpk(s[0], s[1]), cvtpk(s[2], s[3]), cvtpk(s[4], s[5]), cvtpk(s[6], s[7])};
        u32x4 p1 = {cvtpk(s[8], s[9]), cvtpk(s[10], s[11]), cvtpk(s[12], s[13]), cvtpk(s[14], s[15])};
        bf16x8 pa0 = __builtin_bit_cast(bf16x8, p0), pa1 = __builtin_bit_cast(bf16x8, p1);
        const int vbb = vb + kb * 8192;
        o[0] = __builtin_amdgcn_mfma_f32_32x32x16_bf16(vfrag<0, 0>(vbb), pa0, o[0], 0, 0, 0);
        o[1] = __builtin_amdgcn_mfma_f32_32x32x16_bf16(vfrag<0, 1>(vbb), pa0, o[1], 0, 0, 0);
        o[2] = __builtin_amdgcn_mfma_f32_32x32x16_bf16(vfrag<0, 2>(vbb), pa0, o[2], 0, 0, 0);
        o[3] = __builtin_amdgcn_mfma_f32_32x32x16_bf16(vfrag<0, 3>(vbb), pa0, o[3], 0, 0, 0);
        o[0] = __builtin_amdgcn_mfma_f32_32x32x16_bf16(vfrag<1, 0>(vbb), pa1, o[0], 0, 0, 0);
        o[1] = __builtin_amdgcn_mfma_f32_32x32x16_bf16(vfrag<1, 1>(vbb), pa1, o[1], 0, 0, 0);
        o[2] = __builtin_amdgcn_mfma_f32_32x32x16_bf16(vfrag<1, 2>(vbb), pa1, o[2], 0, 0, 0);
        o[3] = __builtin_amdgcn_mfma_f32_32x32x16_bf16(vfrag<1, 3>(vbb), pa1, o[3], 0, 0, 0);
      }
    }
    l += __shfl_xor(l, 32);
    const float il = 1.f / l;
    if (act) {
#pragma unroll
      for (int db = 0; db < 4; ++db)
#pragma unroll
        for (int g = 0; g < 4; ++g) {
          u32x2 w = {cvtpk(o[db][4 * g] * il, o[db][4 * g + 1] * il), cvtpk(o[db][4 * g + 2] * il, o[db][4 * g + 3] * il)};
          *(u32x2*)(Ob + (t * 32 + r32) * D + 32 * db + 8 * g + 4 * hi) = w;
        }
    }
  }
}

extern "C" void kernel_launch(void* const* d_in, const int* in_sizes, int n_in, void* d_out, int out_size, void* d_ws, size_t ws_size, hipStream_t stream) {
  attn_base<<<256, 256, 0, stream>>>((const unsigned short*)d_in[0], (const unsigned short*)d_in[1], (const unsigned short*)d_in[2], (unsigned short*)d_out);
}
```

```cpp
#include <hip/hip_runtime.h>
#include <hip/hip_bf16.h>

typedef float    f32x4  __attribute__((ext_vector_type(4)));
typedef short    bf16x8 __attribute__((ext_vector_type(8)));
typedef short    s16x4  __attribute__((ext_vector_type(4)));
typedef unsigned u32x4  __attribute__((ext_vector_type(4)));
typedef unsigned u32x2  __attribute__((ext_vector_type(2)));

constexpr int NQ = 576, NK = 512, D = 128;

__device__ __forceinline__ unsigned cvtpk(float lo, float hi) {
  typedef float f32x2_ __attribute__((ext_vector_type(2))); typedef __bf16 bf16x2_ __attribute__((ext_vector_type(2)));
  f32x2_ v = {lo, hi}; bf16x2_ b = __builtin_convertvector(v, bf16x2_); return __builtin_bit_cast(unsigned, b);
}
template <int OFF> __device__ __forceinline__ bf16x8 vfrag(int vb) {
  s16x4 l, h;
  asm volatile("ds_read_b64_tr_b16 %0, %2 offset:%3\n\tds_read_b64_tr_b16 %1, %2 offset:%4\n\ts_waitcnt lgkmcnt(0)"
               : "=&v"(l), "=&v"(h) : "v"(vb), "i"(OFF), "i"(OFF + 4096) : "memory");
  return (bf16x8){l[0], l[1], l[2], l[3], h[0], h[1], h[2], h[3]};
}
__device__ __forceinline__ float xmax(float x) {
  auto a = __builtin_amdgcn_permlane16_swap(__float_as_uint(x), __float_as_uint(x), false, false);
  x = fmaxf(__uint_as_float(a[0]), __uint_as_float(a[1]));
  auto b = __builtin_amdgcn_permlane32_swap(__float_as_uint(x), __float_as_uint(x), false, false);
  return fmaxf(__uint_as_float(b[0]), __uint_as_float(b[1]));
}
__device__ __forceinline__ float xsum(float x) {
  auto a = __builtin_amdgcn_permlane16_swap(__float_as_uint(x), __float_as_uint(x), false, false);
  x = __uint_as_float(a[0]) + __uint_as_float(a[1]);
  auto b = __builtin_amdgcn_permlane32_swap(__float_as_uint(x), __float_as_uint(x), false, false);
  return __uint_as_float(b[0]) + __uint_as_float(b[1]);
}

__device__ __forceinline__ void slow_tile(const unsigned short* __restrict__ Qt, const unsigned short* __restrict__ Kb,
                                          const unsigned short* __restrict__ Vb, unsigned short* __restrict__ Ot, char* stage, int lane) {
  const int r = lane & 15, g = lane >> 4;
  const float C = 0.12751743082f;
  bf16x8 qr[4];
#pragma unroll
  for (int s = 0; s < 4; ++s) qr[s] = *(const bf16x8*)(Qt + r * D + 32 * s + 8 * g);
  float m = -3.0e38f, l = 0.f; f32x4 o[8] = {};
  const int vb = (int)(size_t)stage + 256 * (4 * g + ((lane >> 2) & 3)) + 8 * (lane & 3);
  for (int kb = 0; kb < 16; ++kb) {
#pragma unroll
    for (int i = 0; i < 8; ++i) *(u32x4*)(stage + 1024 * i + 16 * lane) = *(const u32x4*)(Vb + kb * 32 * D + 512 * i + 8 * lane);
    f32x4 s0 = {}, s1 = {};
#pragma unroll
    for (int s = 0; s < 4; ++s) {
      s0 = __builtin_amdgcn_mfma_f32_16x16x32_bf16(*(const bf16x8*)(Kb + (kb * 32 + r) * D + 32 * s + 8 * g), qr[s], s0, 0, 0, 0);
      s1 = __builtin_amdgcn_mfma_f32_16x16x32_bf16(*(const bf16x8*)(Kb + (kb * 32 + 16 + r) * D + 32 * s + 8 * g), qr[s], s1, 0, 0, 0);
    }
    const float pm = xmax(fmaxf(fmaxf(fmaxf(s0[0], s0[1]), fmaxf(s0[2], s0[3])), fmaxf(fmaxf(s1[0], s1[1]), fmaxf(s1[2], s1[3]))));
    const float mn = fmaxf(m, pm), al = __builtin_amdgcn_exp2f((m - mn) * C); m = mn;
    float ps = 0.f;
#pragma unroll
    for (int i = 0; i < 4; ++i) { s0[i] = __builtin_amdgcn_exp2f((s0[i] - mn) * C); s1[i] = __builtin_amdgcn_exp2f((s1[i] - mn) * C); ps += s0[i] + s1[i]; }
    l = l * al + ps;
    u32x4 pw = {cvtpk(s0[0], s0[1]), cvtpk(s0[2], s0[3]), cvtpk(s1[0], s1[1]), cvtpk(s1[2], s1[3])};
    const bf16x8 pa = __builtin_bit_cast(bf16x8, pw);
    asm volatile("s_waitcnt lgkmcnt(0)" ::: "memory");
#define PVS(DB) o[DB] *= al; o[DB] = __builtin_amdgcn_mfma_f32_16x16x32_bf16(vfrag<32 * DB>(vb), pa, o[DB], 0, 0, 0);
    PVS(0) PVS(1) PVS(2) PVS(3) PVS(4) PVS(5) PVS(6) PVS(7)
#undef PVS
  }
  const float il = 1.f / xsum(l);
#pragma unroll
  for (int db = 0; db < 8; ++db) {
    u32x2 w = {cvtpk(o[db][0] * il, o[db][1] * il), cvtpk(o[db][2] * il, o[db][3] * il)};
    *(u32x2*)(Ot + r * D + 16 * db + 4 * g) = w;
  }
}

__global__ __attribute__((aligned(4096))) __launch_bounds__(256, 1) void gqa_attn_kernel(const unsigned short* __restrict__ Q, const unsigned short* __restrict__ K,
                                                          const unsigned short* __restrict__ V, unsigned short* __restrict__ O
                                                          ) {
  __shared__ __attribute__((aligned(16))) char lds[163840];
  const unsigned wid = __builtin_amdgcn_readfirstlane(threadIdx.x >> 6);
  const unsigned bh = (blockIdx.x & 7) * 32 + (blockIdx.x >> 3);
  const unsigned ldsb = __builtin_amdgcn_readfirstlane((unsigned)(size_t)lds);
  unsigned bad;
  asm volatile(
    "s_load_dwordx8 s[16:23], %1, 0x0\n\t"
    "s_getpc_b64 s[62:63]\n\t"
    "v_mbcnt_lo_u32_b32 v79, -1, 0\n\t"
    "v_mbcnt_hi_u32_b32 v79, -1, v79\n\t"
    "s_lshr_b32 s98, %5, 3\n\t"
    "s_and_b32 s98, s98, 7\n\t"
    "s_lshl_b32 s98, s98, 2\n\t"
    "s_add_u32 s98, s98, %3\n\t"
    "s_lshl_b32 s98, s98, 11\n\t"
    "v_and_b32 v66, 15, v79\n\t"
    "v_lshl_add_u32 v64, v66, 7, s98\n\t"
    "v_min_u32 v64, 0xba90, v64\n\t"
    "global_load_dword v56, v64, s[62:63]\n\t"
    "s_mov_b32 s61, m0\n\t"
    "s_mov_b32 s38, 0x20000\n\t"
    "s_mov_b32 s39, s38\n\t"
    "s_mov_b64 s[42:43], s[38:39]\n\t"
    "s_mov_b32 s30, 0x24000\n\t"
    "s_mov_b32 s31, s38\n\t"
    "s_mov_b64 vcc, 0\n\t"
    "s_lshl_b32 s98, %3, 9\n\t"
    "v_and_b32 v55, 3, v79\n\t"
    "v_lshl_add_u32 v55, v55, 7, s98\n\t"
    "s_mov_b64 s[46:47], s[30:31]\n\t"
    "s_lshl_b32 s98, %3, 11\n\t"
    "s_add_u32 s51, %4, s98\n\t"
    "v_bfe_u32 v64, v79, 2, 2\n\t"
    "v_lshrrev_b32 v65, 4, v79\n\t"
    "v_lshlrev_b32 v64, 8, v64\n\t"
    "v_lshl_add_u32 v64, v65, 6, v64\n\t"
    "v_and_b32 v67, 3, v79\n\t"
    "v_lshl_add_u32 v208, v67, 4, v64\n\t"
    "s_and_b32 s99, %3, 1\n\t"
    "s_lshl_b32 s99, s99, 5\n\t"
    "s_or_b32 s98, s98, s99\n\t"
    "v_xor_b32 v52, s98, v208\n\t"
    "v_xor_b32 v53, 48, v52\n\t"
    "s_mul_i32 s52, %3, 24576\n\t"
    "s_lshl_b32 s99, s99, 7\n\t"
    "s_lshr_b32 s98, %3, 1\n\t"
    "s_mul_i32 s98, s98, 81920\n\t"
    "s_add_u32 s98, s98, %4\n\t"
    "s_add_u32 s99, s99, s98\n\t"
    "s_add_u32 s53, s52, 4096\n\t"
    "s_mov_b32 s64, 0\n\t"
    "s_add_u32 s54, s52, 8192\n\t"
    "s_add_u32 s55, s52, 12288\n\t"
    "s_add_u32 s56, s52, 16384\n\t"
    "s_add_u32 s57, s52, 20480\n\t"
    "s_mov_b32 s65, 8192\n\t"
    "s_mov_b32 s66, 16384\n\t"
    "s_mov_b32 s67, 24576\n\t"
    "s_mov_b32 s68, 32768\n\t"
    "s_mov_b32 s69, 40960\n\t"
    "s_mov_b32 s70, 49152\n\t"
    "s_mov_b32 s71, 57344\n\t"
    "s_mov_b32 s72, 65536\n\t"
    "s_mov_b32 s73, 73728\n\t"
    "s_mov_b32 s74, 81920\n\t"
    "s_mov_b32 s75, 90112\n\t"
    "s_mov_b32 s76, 98304\n\t"
    "s_mov_b32 s77, 106496\n\t"
    "s_mov_b32 s78, 114688\n\t"
    "s_mov_b32 s79, 122880\n\t"
    "s_lshl_b32 s48, %2, 17\n\t"
    "s_mul_i32 s49, %2, 0x24000\n\t"
    "s_waitcnt lgkmcnt(0) vmcnt(0)\n\t"
    "s_add_u32 s36, s18, s48\n\t"
    "s_addc_u32 s37, s19, 0\n\t"
    "s_add_u32 m0, s51, 0\n\t"
    "s_nop 0\n\t"
    "buffer_load_dwordx4 v52, s[36:39], s64 offen offset:0 lds\n\t"
    "buffer_load_dwordx4 v53, s[36:39], s64 offen offset:1024 lds\n\t"
    "s_add_u32 s28, s16, s49\n\t"
    "s_addc_u32 s29, s17, 0\n\t"
    "s_add_u32 m0, s99, 32768\n\t"
    "s_nop 0\n\t"
    "buffer_load_dwordx4 v208, s[28:31], s52 offen offset:0 nt lds\n\t"
    "buffer_load_dwordx4 v208, s[28:31], s52 offen offset:1024 nt lds\n\t"
    "buffer_load_dwordx4 v208, s[28:31], s52 offen offset:2048 nt lds\n\t"
    "buffer_load_dwordx4 v208, s[28:31], s52 offen offset:3072 nt lds\n\t"
    "s_add_u32 s40, s20, s48\n\t"
    "s_addc_u32 s41, s21, 0\n\t"
    "s_add_u32 m0, s51, 81920\n\t"
    "s_nop 0\n\t"
    "buffer_load_dwordx4 v52, s[40:43], s64 offen offset:0 lds\n\t"
    "buffer_load_dwordx4 v53, s[40:43], s64 offen offset:1024 lds\n\t"
    "s_add_u32 s44, s22, s49\n\t"
    "s_addc_u32 s45, s23, 0\n\t"
    "s_mov_b64 s[16:17], s[62:63]\n\t"
    "v_lshlrev_b32 v67, 8, v66\n\t"
    "v_lshl_add_u32 v54, v65, 4, v67\n\t"
    "s_mov_b32 s48, 0x3e0293ee\n\t"
    "s_mov_b32 s49, 0xffff0000\n\t"
    "s_mov_b32 s50, 1400897536\n\t"
    "s_mul_i32 s98, %3, 12288\n\t"
    "s_add_u32 s58, s98, 98304\n\t"
    "s_add_u32 s59, s98, 102400\n\t"
    "s_add_u32 s60, s98, 106496\n\t"
    "v_lshrrev_b32 v64, 2, v66\n\t"
    "v_and_b32 v68, 3, v66\n\t"
    "v_lshlrev_b32 v68, 6, v68\n\t"
    "v_lshl_add_u32 v68, v64, 10, v68\n\t"
    "v_lshl_add_u32 v209, v65, 4, v68\n\t"
    "v_add_u32 v209, s99, v209\n\t"
    "v_add_u32 v209, 32768, v209\n\t"
    "v_sub_u32 v67, 0, v64\n\t"
    "v_and_b32 v67, 3, v67\n\t"
    "v_xor_b32 v67, v67, v65\n\t"
    "v_lshl_add_u32 v67, v67, 4, v68\n\t"
    "v_add_u32 v48, %4, v67\n\t"
    "v_add_u32 v49, 0x10000, v48\n\t"
    "v_sub_u32 v67, 0, v65\n\t"
    "v_xor_b32 v67, v67, v79\n\t"
    "v_and_b32 v67, 3, v67\n\t"
    "v_lshlrev_b32 v67, 4, v67\n\t"
    "v_lshl_add_u32 v67, v64, 6, v67\n\t"
    "v_lshl_add_u32 v67, v65, 10, v67\n\t"
    "s_add_u32 s98, %4, 81920\n\t"
    "v_add_u32 v50, s98, v67\n\t"
    "v_add_u32 v51, 0x10000, v50\n\t"
    "v_mov_b32 v64, 0x3f803f80\n\t"
    "v_accvgpr_write_b32 a248, v64\n\t"
    "v_accvgpr_write_b32 a249, v64\n\t"
    "v_accvgpr_write_b32 a250, v64\n\t"
    "v_accvgpr_write_b32 a251, v64\n\t"
    "s_add_u32 m0, s99, 40960\n\t"
    "s_nop 0\n\t"
    "buffer_load_dwordx4 v208, s[28:31], s53 offen offset:0 nt lds\n\t"
    "buffer_load_dwordx4 v208, s[28:31], s53 offen offset:1024 nt lds\n\t"
    "buffer_load_dwordx4 v208, s[28:31], s53 offen offset:2048 nt lds\n\t"
    "buffer_load_dwordx4 v208, s[28:31], s53 offen offset:3072 nt lds\n\t"
    "s_add_u32 m0, s99, 49152\n\t"
    "s_nop 0\n\t"
    "buffer_load_dwordx4 v208, s[28:31], s54 offen offset:0 nt lds\n\t"
    "buffer_load_dwordx4 v208, s[28:31], s54 offen offset:1024 nt lds\n\t"
    "buffer_load_dwordx4 v208, s[28:31], s54 offen offset:2048 nt lds\n\t"
    "buffer_load_dwordx4 v208, s[28:31], s54 offen offset:3072 nt lds\n\t"
    "s_waitcnt vmcnt(14)\n\t"
    "s_barrier\n\t"
    "ds_read_b128 a[192:195], v48 offset:0\n\t"
    "ds_read_b128 a[196:199], v48 offset:256\n\t"
    "ds_read_b128 a[200:203], v48 offset:512\n\t"
    "ds_read_b128 a[204:207], v48 offset:768\n\t"
    "ds_read_b128 a[208:211], v48 offset:4096\n\t"
    "ds_read_b128 a[212:215], v48 offset:4352\n\t"
    "ds_read_b128 a[216:219], v48 offset:4608\n\t"
    "ds_read_b128 a[220:223], v48 offset:4864\n\t"
    "s_waitcnt vmcnt(10)\n\t"
    "ds_read_b128 v[80:83], v209 offset:0\n\t"
    "ds_read_b128 v[84:87], v209 offset:256\n\t"
    "ds_read_b128 v[88:91], v209 offset:512\n\t"
    "ds_read_b128 v[92:95], v209 offset:768\n\t"
    "s_waitcnt lgkmcnt(3)\n\t"
    "v_lshlrev_b32 v64, 16, v80\n\t"
    "v_and_b32 v65, s49, v80\n\t"
    "v_pk_mul_f32 v[64:65], v[64:65], s[48:49] op_sel_hi:[1,0]\n\t"
    "v_cvt_pk_bf16_f32 v80, v64, v65\n\t"
    "v_lshlrev_b32 v66, 16, v81\n\t"
    "v_and_b32 v67, s49, v81\n\t"
    "v_pk_mul_f32 v[66:67], v[66:67], s[48:49] op_sel_hi:[1,0]\n\t"
    "v_cvt_pk_bf16_f32 v81, v66, v67\n\t"
    "v_lshlrev_b32 v68, 16, v82\n\t"
    "v_and_b32 v69, s49, v82\n\t"
    "v_pk_mul_f32 v[68:69], v[68:69], s[48:49] op_sel_hi:[1,0]\n\t"
    "v_cvt_pk_bf16_f32 v82, v68, v69\n\t"
    "v_lshlrev_b32 v70, 16, v83\n\t"
    "v_and_b32 v71, s49, v83\n\t"
    "v_pk_mul_f32 v[70:71], v[70:71], s[48:49] op_sel_hi:[1,0]\n\t"
    "v_cvt_pk_bf16_f32 v83, v70, v71\n\t"
    "s_waitcnt lgkmcnt(2)\n\t"
    "v_lshlrev_b32 v64, 16, v84\n\t"
    "v_and_b32 v65, s49, v84\n\t"
    "v_pk_mul_f32 v[64:65], v[64:65], s[48:49] op_sel_hi:[1,0]\n\t"
    "v_cvt_pk_bf16_f32 v84, v64, v65\n\t"
    "v_lshlrev_b32 v66, 16, v85\n\t"
    "v_and_b32 v67, s49, v85\n\t"
    "v_pk_mul_f32 v[66:67], v[66:67], s[48:49] op_sel_hi:[1,0]\n\t"
    "v_cvt_pk_bf16_f32 v85, v66, v67\n\t"
    "v_lshlrev_b32 v68, 16, v86\n\t"
    "v_and_b32 v69, s49, v86\n\t"
    "v_pk_mul_f32 v[68:69], v[68:69], s[48:49] op_sel_hi:[1,0]\n\t"
    "v_cvt_pk_bf16_f32 v86, v68, v69\n\t"
    "v_lshlrev_b32 v70, 16, v87\n\t"
    "v_and_b32 v71, s49, v87\n\t"
    "v_pk_mul_f32 v[70:71], v[70:71], s[48:49] op_sel_hi:[1,0]\n\t"
    "v_cvt_pk_bf16_f32 v87, v70, v71\n\t"
    "s_waitcnt lgkmcnt(1)\n\t"
    "v_lshlrev_b32 v64, 16, v88\n\t"
    "v_and_b32 v65, s49, v88\n\t"
    "v_pk_mul_f32 v[64:65], v[64:65], s[48:49] op_sel_hi:[1,0]\n\t"
    "v_cvt_pk_bf16_f32 v88, v64, v65\n\t"
    "v_lshlrev_b32 v66, 16, v89\n\t"
    "v_and_b32 v67, s49, v89\n\t"
    "v_pk_mul_f32 v[66:67], v[66:67], s[48:49] op_sel_hi:[1,0]\n\t"
    "v_cvt_pk_bf16_f32 v89, v66, v67\n\t"
    "v_lshlrev_b32 v68, 16, v90\n\t"
    "v_and_b32 v69, s49, v90\n\t"
    "v_pk_mul_f32 v[68:69], v[68:69], s[48:49] op_sel_hi:[1,0]\n\t"
    "v_cvt_pk_bf16_f32 v90, v68, v69\n\t"
    "v_lshlrev_b32 v70, 16, v91\n\t"
    "v_and_b32 v71, s49, v91\n\t"
    "v_pk_mul_f32 v[70:71], v[70:71], s[48:49] op_sel_hi:[1,0]\n\t"
    "v_cvt_pk_bf16_f32 v91, v70, v71\n\t"
    "s_waitcnt lgkmcnt(0)\n\t"
    "v_lshlrev_b32 v64, 16, v92\n\t"
    "v_and_b32 v65, s49, v92\n\t"
    "v_pk_mul_f32 v[64:65], v[64:65], s[48:49] op_sel_hi:[1,0]\n\t"
    "v_cvt_pk_bf16_f32 v92, v64, v65\n\t"
    "v_lshlrev_b32 v66, 16, v93\n\t"
    "v_and_b32 v67, s49, v93\n\t"
    "v_pk_mul_f32 v[66:67], v[66:67], s[48:49] op_sel_hi:[1,0]\n\t"
    "v_cvt_pk_bf16_f32 v93, v66, v67\n\t"
    "v_lshlrev_b32 v68, 16, v94\n\t"
    "v_and_b32 v69, s49, v94\n\t"
    "v_pk_mul_f32 v[68:69], v[68:69], s[48:49] op_sel_hi:[1,0]\n\t"
    "v_cvt_pk_bf16_f32 v94, v68, v69\n\t"
    "v_lshlrev_b32 v70, 16, v95\n\t"
    "v_and_b32 v71, s49, v95\n\t"
    "v_pk_mul_f32 v[70:71], v[70:71], s[48:49] op_sel_hi:[1,0]\n\t"
    "v_cvt_pk_bf16_f32 v95, v70, v71\n\t"
    "v_mfma_f32_16x16x32_bf16 v[56:59], a[192:195], v[80:83], 0\n\t"
    "v_mfma_f32_16x16x32_bf16 v[56:59], a[196:199], v[84:87], v[56:59]\n\t"
    "v_mfma_f32_16x16x32_bf16 v[56:59], a[200:203], v[88:91], v[56:59]\n\t"
    "v_mfma_f32_16x16x32_bf16 v[56:59], a[204:207], v[92:95], v[56:59]\n\t"
    "s_nop 7\n\t"
    "v_max3_f32 v60, v56, v57, v58\n\t"
    "v_max_f32 v60, v60, v59\n\t"
    "v_mov_b32 v61, v60\n\t"
    "s_nop 1\n\t"
    "v_permlane16_swap_b32 v60, v61\n\t"
    "v_max_f32 v60, v60, v61\n\t"
    "v_mov_b32 v61, v60\n\t"
    "s_nop 1\n\t"
    "v_permlane32_swap_b32 v60, v61\n\t"
    "v_max_f32 v60, v60, v61\n\t"
    "v_sub_f32 v24, 0, v60\n\t"
    "v_sub_f32 v25, 0, v60\n\t"
    "v_sub_f32 v26, 0, v60\n\t"
    "v_sub_f32 v27, 0, v60\n\t"
    "s_nop 1\n\t"
    "v_mfma_f32_16x16x32_bf16 v[0:3], a[192:195], v[80:83], v[24:27]\n\t"
    "v_mfma_f32_16x16x32_bf16 v[0:3], a[196:199], v[84:87], v[0:3]\n\t"
    "v_mfma_f32_16x16x32_bf16 v[0:3], a[200:203], v[88:91], v[0:3]\n\t"
    "v_mfma_f32_16x16x32_bf16 v[0:3], a[204:207], v[92:95], v[0:3]\n\t"
    "v_mfma_f32_16x16x32_bf16 v[4:7], a[208:211], v[80:83], v[24:27]\n\t"
    "v_mfma_f32_16x16x32_bf16 v[4:7], a[212:215], v[84:87], v[4:7]\n\t"
    "v_mfma_f32_16x16x32_bf16 v[4:7], a[216:219], v[88:91], v[4:7]\n\t"
    "v_mfma_f32_16x16x32_bf16 v[4:7], a[220:223], v[92:95], v[4:7]\n\t"
    "v_exp_f32 v0, v0\n\t"
    "v_exp_f32 v1, v1\n\t"
    "v_exp_f32 v2, v2\n\t"
    "v_exp_f32 v3, v3\n\t"
    "v_cvt_pk_bf16_f32 v16, v0, v1\n\t"
    "v_cvt_pk_bf16_f32 v17, v2, v3\n\t"
    "s_nop 1\n\t"
    "v_exp_f32 v4, v4\n\t"
    "v_exp_f32 v5, v5\n\t"
    "v_exp_f32 v6, v6\n\t"
    "v_exp_f32 v7, v7\n\t"
    "v_cvt_pk_bf16_f32 v18, v4, v5\n\t"
    "v_cvt_pk_bf16_f32 v19, v6, v7\n\t"
    "s_add_u32 m0, s51, 8192\n\t"
    "s_nop 0\n\t"
    "buffer_load_dwordx4 v52, s[36:39], s65 offen offset:0 lds\n\t"
    "buffer_load_dwordx4 v53, s[36:39], s65 offen offset:1024 lds\n\t"
    "s_add_u32 m0, s51, 90112\n\t"
    "s_nop 0\n\t"
    "buffer_load_dwordx4 v52, s[40:43], s65 offen offset:0 lds\n\t"
    "buffer_load_dwordx4 v53, s[40:43], s65 offen offset:1024 lds\n\t"
    "s_waitcnt vmcnt(12)\n\t"
    "s_barrier\n\t"
    "ds_read_b64_tr_b16 v[224:225], v50 offset:0\n\t"
    "ds_read_b64_tr_b16 v[226:227], v50 offset:4096\n\t"
    "ds_read_b64_tr_b16 v[228:229], v50 offset:8\n\t"
    "ds_read_b64_tr_b16 v[230:231], v50 offset:4104\n\t"
    "ds_read_b64_tr_b16 v[232:233], v50 offset:256\n\t"
    "ds_read_b64_tr_b16 v[234:235], v50 offset:4352\n\t"
    "ds_read_b64_tr_b16 v[236:237], v50 offset:264\n\t"
    "ds_read_b64_tr_b16 v[238:239], v50 offset:4360\n\t"
    "ds_read_b64_tr_b16 v[240:241], v50 offset:512\n\t"
    "ds_read_b64_tr_b16 v[242:243], v50 offset:4608\n\t"
    "ds_read_b64_tr_b16 v[244:245], v50 offset:520\n\t"
    "ds_read_b64_tr_b16 v[246:247], v50 offset:4616\n\t"
    "ds_read_b64_tr_b16 v[248:249], v50 offset:768\n\t"
    "ds_read_b64_tr_b16 v[250:251], v50 offset:4864\n\t"
    "ds_read_b64_tr_b16 v[252:253], v50 offset:776\n\t"
    "ds_read_b64_tr_b16 v[254:255], v50 offset:4872\n\t"
    "s_waitcnt vmcnt(8)\n\t"
    "ds_read_b128 v[96:99], v209 offset:8192\n\t"
    "ds_read_b128 v[100:103], v209 offset:8448\n\t"
    "ds_read_b128 v[104:107], v209 offset:8704\n\t"
    "ds_read_b128 v[108:111], v209 offset:8960\n\t"
    "s_waitcnt lgkmcnt(3)\n\t"
    "v_lshlrev_b32 v64, 16, v96\n\t"
    "v_and_b32 v65, s49, v96\n\t"
    "v_pk_mul_f32 v[64:65], v[64:65], s[48:49] op_sel_hi:[1,0]\n\t"
    "v_cvt_pk_bf16_f32 v96, v64, v65\n\t"
    "v_lshlrev_b32 v66, 16, v97\n\t"
    "v_and_b32 v67, s49, v97\n\t"
    "v_pk_mul_f32 v[66:67], v[66:67], s[48:49] op_sel_hi:[1,0]\n\t"
    "v_cvt_pk_bf16_f32 v97, v66, v67\n\t"
    "v_lshlrev_b32 v68, 16, v98\n\t"
    "v_and_b32 v69, s49, v98\n\t"
    "v_pk_mul_f32 v[68:69], v[68:69], s[48:49] op_sel_hi:[1,0]\n\t"
    "v_cvt_pk_bf16_f32 v98, v68, v69\n\t"
    "v_lshlrev_b32 v70, 16, v99\n\t"
    "v_and_b32 v71, s49, v99\n\t"
    "v_pk_mul_f32 v[70:71], v[70:71], s[48:49] op_sel_hi:[1,0]\n\t"
    "v_cvt_pk_bf16_f32 v99, v70, v71\n\t"
    "s_waitcnt lgkmcnt(2)\n\t"
    "v_lshlrev_b32 v64, 16, v100\n\t"
    "v_and_b32 v65, s49, v100\n\t"
    "v_pk_mul_f32 v[64:65], v[64:65], s[48:49] op_sel_hi:[1,0]\n\t"
    "v_cvt_pk_bf16_f32 v100, v64, v65\n\t"
    "v_lshlrev_b32 v66, 16, v101\n\t"
    "v_and_b32 v67, s49, v101\n\t"
    "v_pk_mul_f32 v[66:67], v[66:67], s[48:49] op_sel_hi:[1,0]\n\t"
    "v_cvt_pk_bf16_f32 v101, v66, v67\n\t"
    "v_lshlrev_b32 v68, 16, v102\n\t"
    "v_and_b32 v69, s49, v102\n\t"
    "v_pk_mul_f32 v[68:69], v[68:69], s[48:49] op_sel_hi:[1,0]\n\t"
    "v_cvt_pk_bf16_f32 v102, v68, v69\n\t"
    "v_lshlrev_b32 v70, 16, v103\n\t"
    "v_and_b32 v71, s49, v103\n\t"
    "v_pk_mul_f32 v[70:71], v[70:71], s[48:49] op_sel_hi:[1,0]\n\t"
    "v_cvt_pk_bf16_f32 v103, v70, v71\n\t"
    "s_waitcnt lgkmcnt(1)\n\t"
    "v_lshlrev_b32 v64, 16, v104\n\t"
    "v_and_b32 v65, s49, v104\n\t"
    "v_pk_mul_f32 v[64:65], v[64:65], s[48:49] op_sel_hi:[1,0]\n\t"
    "v_cvt_pk_bf16_f32 v104, v64, v65\n\t"
    "v_lshlrev_b32 v66, 16, v105\n\t"
    "v_and_b32 v67, s49, v105\n\t"
    "v_pk_mul_f32 v[66:67], v[66:67], s[48:49] op_sel_hi:[1,0]\n\t"
    "v_cvt_pk_bf16_f32 v105, v66, v67\n\t"
    "v_lshlrev_b32 v68, 16, v106\n\t"
    "v_and_b32 v69, s49, v106\n\t"
    "v_pk_mul_f32 v[68:69], v[68:69], s[48:49] op_sel_hi:[1,0]\n\t"
    "v_cvt_pk_bf16_f32 v106, v68, v69\n\t"
    "v_lshlrev_b32 v70, 16, v107\n\t"
    "v_and_b32 v71, s49, v107\n\t"
    "v_pk_mul_f32 v[70:71], v[70:71], s[48:49] op_sel_hi:[1,0]\n\t"
    "v_cvt_pk_bf16_f32 v107, v70, v71\n\t"
    "s_waitcnt lgkmcnt(0)\n\t"
    "v_lshlrev_b32 v64, 16, v108\n\t"
    "v_and_b32 v65, s49, v108\n\t"
    "v_pk_mul_f32 v[64:65], v[64:65], s[48:49] op_sel_hi:[1,0]\n\t"
    "v_cvt_pk_bf16_f32 v108, v64, v65\n\t"
    "v_lshlrev_b32 v66, 16, v109\n\t"
    "v_and_b32 v67, s49, v109\n\t"
    "v_pk_mul_f32 v[66:67], v[66:67], s[48:49] op_sel_hi:[1,0]\n\t"
    "v_cvt_pk_bf16_f32 v109, v66, v67\n\t"
    "v_lshlrev_b32 v68, 16, v110\n\t"
    "v_and_b32 v69, s49, v110\n\t"
    "v_pk_mul_f32 v[68:69], v[68:69], s[48:49] op_sel_hi:[1,0]\n\t"
    "v_cvt_pk_bf16_f32 v110, v68, v69\n\t"
    "v_lshlrev_b32 v70, 16, v111\n\t"
    "v_and_b32 v71, s49, v111\n\t"
    "v_pk_mul_f32 v[70:71], v[70:71], s[48:49] op_sel_hi:[1,0]\n\t"
    "v_cvt_pk_bf16_f32 v111, v70, v71\n\t"
    "v_mfma_f32_16x16x32_bf16 v[56:59], a[192:195], v[96:99], 0\n\t"
    "v_mfma_f32_16x16x32_bf16 v[56:59], a[196:199], v[100:103], v[56:59]\n\t"
    "v_mfma_f32_16x16x32_bf16 v[56:59], a[200:203], v[104:107], v[56:59]\n\t"
    "v_mfma_f32_16x16x32_bf16 v[56:59], a[204:207], v[108:111], v[56:59]\n\t"
    "v_mfma_f32_16x16x32_bf16 a[224:227], a[248:251], v[16:19], 0\n\t"
    "v_mfma_f32_16x16x32_bf16 a[0:3], v[224:227], v[16:19], 0\n\t"
    "v_mfma_f32_16x16x32_bf16 a[4:7], v[228:231], v[16:19], 0\n\t"
    "v_mfma_f32_16x16x32_bf16 a[8:11], v[232:235], v[16:19], 0\n\t"
    "v_max3_f32 v60, v56, v57, v58\n\t"
    "v_max_f32 v60, v60, v59\n\t"
    "v_mfma_f32_16x16x32_bf16 a[12:15], v[236:239], v[16:19], 0\n\t"
    "v_mov_b32 v61, v60\n\t"
    "v_mfma_f32_16x16x32_bf16 a[16:19], v[240:243], v[16:19], 0\n\t"
    "s_nop 0\n\t"
    "v_permlane16_swap_b32 v60, v61\n\t"
    "v_mfma_f32_16x16x32_bf16 a[20:23], v[244:247], v[16:19], 0\n\t"
    "v_max_f32 v60, v60, v61\n\t"
    "v_mov_b32 v61, v60\n\t"
    "v_mfma_f32_16x16x32_bf16 a[24:27], v[248:251], v[16:19], 0\n\t"
    "s_nop 0\n\t"
    "v_permlane32_swap_b32 v60, v61\n\t"
    "v_max_f32 v60, v60, v61\n\t"
    "v_sub_f32 v28, 0, v60\n\t"
    "v_sub_f32 v29, 0, v60\n\t"
    "v_sub_f32 v30, 0, v60\n\t"
    "v_sub_f32 v31, 0, v60\n\t"
    "v_mfma_f32_16x16x32_bf16 a[28:31], v[252:255], v[16:19], 0\n\t"
    "s_nop 0\n\t"
    "v_mfma_f32_16x16x32_bf16 v[8:11], a[192:195], v[96:99], v[28:31]\n\t"
    "v_mfma_f32_16x16x32_bf16 v[8:11], a[196:199], v[100:103], v[8:11]\n\t"
    "v_mfma_f32_16x16x32_bf16 v[8:11], a[200:203], v[104:107], v[8:11]\n\t"
    "v_mfma_f32_16x16x32_bf16 v[8:11], a[204:207], v[108:111], v[8:11]\n\t"
    "v_mfma_f32_16x16x32_bf16 v[12:15], a[208:211], v[96:99], v[28:31]\n\t"
    "v_mfma_f32_16x16x32_bf16 v[12:15], a[212:215], v[100:103], v[12:15]\n\t"
    "v_mfma_f32_16x16x32_bf16 v[12:15], a[216:219], v[104:107], v[12:15]\n\t"
    "v_mfma_f32_16x16x32_bf16 v[12:15], a[220:223], v[108:111], v[12:15]\n\t"
    "v_exp_f32 v8, v8\n\t"
    "v_exp_f32 v9, v9\n\t"
    "v_exp_f32 v10, v10\n\t"
    "v_exp_f32 v11, v11\n\t"
    "v_cvt_pk_bf16_f32 v20, v8, v9\n\t"
    "v_cvt_pk_bf16_f32 v21, v10, v11\n\t"
    "s_nop 1\n\t"
    "v_exp_f32 v12, v12\n\t"
    "v_exp_f32 v13, v13\n\t"
    "v_exp_f32 v14, v14\n\t"
    "v_exp_f32 v15, v15\n\t"
    "v_cvt_pk_bf16_f32 v22, v12, v13\n\t"
    "v_cvt_pk_bf16_f32 v23, v14, v15\n\t"
    "s_add_u32 m0, s99, 57344\n\t"
    "s_nop 0\n\t"
    "buffer_load_dwordx4 v208, s[28:31], s55 offen offset:0 nt lds\n\t"
    "buffer_load_dwordx4 v208, s[28:31], s55 offen offset:1024 nt lds\n\t"
    "buffer_load_dwordx4 v208, s[28:31], s55 offen offset:2048 nt lds\n\t"
    "buffer_load_dwordx4 v208, s[28:31], s55 offen offset:3072 nt lds\n\t"
    "s_waitcnt vmcnt(8)\n\t"
    "ds_read_b128 v[112:115], v209 offset:16384\n\t"
    "ds_read_b128 v[116:119], v209 offset:16640\n\t"
    "ds_read_b128 v[120:123], v209 offset:16896\n\t"
    "ds_read_b128 v[124:127], v209 offset:17152\n\t"
    "s_waitcnt lgkmcnt(3)\n\t"
    "v_lshlrev_b32 v64, 16, v112\n\t"
    "v_and_b32 v65, s49, v112\n\t"
    "v_pk_mul_f32 v[64:65], v[64:65], s[48:49] op_sel_hi:[1,0]\n\t"
    "v_cvt_pk_bf16_f32 v112, v64, v65\n\t"
    "v_lshlrev_b32 v66, 16, v113\n\t"
    "v_and_b32 v67, s49, v113\n\t"
    "v_pk_mul_f32 v[66:67], v[66:67], s[48:49] op_sel_hi:[1,0]\n\t"
    "v_cvt_pk_bf16_f32 v113, v66, v67\n\t"
    "v_lshlrev_b32 v68, 16, v114\n\t"
    "v_and_b32 v69, s49, v114\n\t"
    "v_pk_mul_f32 v[68:69], v[68:69], s[48:49] op_sel_hi:[1,0]\n\t"
    "v_cvt_pk_bf16_f32 v114, v68, v69\n\t"
    "v_lshlrev_b32 v70, 16, v115\n\t"
    "v_and_b32 v71, s49, v115\n\t"
    "v_pk_mul_f32 v[70:71], v[70:71], s[48:49] op_sel_hi:[1,0]\n\t"
    "v_cvt_pk_bf16_f32 v115, v70, v71\n\t"
    "s_waitcnt lgkmcnt(2)\n\t"
    "v_lshlrev_b32 v64, 16, v116\n\t"
    "v_and_b32 v65, s49, v116\n\t"
    "v_pk_mul_f32 v[64:65], v[64:65], s[48:49] op_sel_hi:[1,0]\n\t"
    "v_cvt_pk_bf16_f32 v116, v64, v65\n\t"
    "v_lshlrev_b32 v66, 16, v117\n\t"
    "v_and_b32 v67, s49, v117\n\t"
    "v_pk_mul_f32 v[66:67], v[66:67], s[48:49] op_sel_hi:[1,0]\n\t"
    "v_cvt_pk_bf16_f32 v117, v66, v67\n\t"
    "v_lshlrev_b32 v68, 16, v118\n\t"
    "v_and_b32 v69, s49, v118\n\t"
    "v_pk_mul_f32 v[68:69], v[68:69], s[48:49] op_sel_hi:[1,0]\n\t"
    "v_cvt_pk_bf16_f32 v118, v68, v69\n\t"
    "v_lshlrev_b32 v70, 16, v119\n\t"
    "v_and_b32 v71, s49, v119\n\t"
    "v_pk_mul_f32 v[70:71], v[70:71], s[48:49] op_sel_hi:[1,0]\n\t"
    "v_cvt_pk_bf16_f32 v119, v70, v71\n\t"
    "s_waitcnt lgkmcnt(1)\n\t"
    "v_lshlrev_b32 v64, 16, v120\n\t"
    "v_and_b32 v65, s49, v120\n\t"
    "v_pk_mul_f32 v[64:65], v[64:65], s[48:49] op_sel_hi:[1,0]\n\t"
    "v_cvt_pk_bf16_f32 v120, v64, v65\n\t"
    "v_lshlrev_b32 v66, 16, v121\n\t"
    "v_and_b32 v67, s49, v121\n\t"
    "v_pk_mul_f32 v[66:67], v[66:67], s[48:49] op_sel_hi:[1,0]\n\t"
    "v_cvt_pk_bf16_f32 v121, v66, v67\n\t"
    "v_lshlrev_b32 v68, 16, v122\n\t"
    "v_and_b32 v69, s49, v122\n\t"
    "v_pk_mul_f32 v[68:69], v[68:69], s[48:49] op_sel_hi:[1,0]\n\t"
    "v_cvt_pk_bf16_f32 v122, v68, v69\n\t"
    "v_lshlrev_b32 v70, 16, v123\n\t"
    "v_and_b32 v71, s49, v123\n\t"
    "v_pk_mul_f32 v[70:71], v[70:71], s[48:49] op_sel_hi:[1,0]\n\t"
    "v_cvt_pk_bf16_f32 v123, v70, v71\n\t"
    "s_waitcnt lgkmcnt(0)\n\t"
    "v_lshlrev_b32 v64, 16, v124\n\t"
    "v_and_b32 v65, s49, v124\n\t"
    "v_pk_mul_f32 v[64:65], v[64:65], s[48:49] op_sel_hi:[1,0]\n\t"
    "v_cvt_pk_bf16_f32 v124, v64, v65\n\t"
    "v_lshlrev_b32 v66, 16, v125\n\t"
    "v_and_b32 v67, s49, v125\n\t"
    "v_pk_mul_f32 v[66:67], v[66:67], s[48:49] op_sel_hi:[1,0]\n\t"
    "v_cvt_pk_bf16_f32 v125, v66, v67\n\t"
    "v_lshlrev_b32 v68, 16, v126\n\t"
    "v_and_b32 v69, s49, v126\n\t"
    "v_pk_mul_f32 v[68:69], v[68:69], s[48:49] op_sel_hi:[1,0]\n\t"
    "v_cvt_pk_bf16_f32 v126, v68, v69\n\t"
    "v_lshlrev_b32 v70, 16, v127\n\t"
    "v_and_b32 v71, s49, v127\n\t"
    "v_pk_mul_f32 v[70:71], v[70:71], s[48:49] op_sel_hi:[1,0]\n\t"
    "v_cvt_pk_bf16_f32 v127, v70, v71\n\t"
    "v_mfma_f32_16x16x32_bf16 v[56:59], a[192:195], v[112:115], 0\n\t"
    "v_mfma_f32_16x16x32_bf16 v[56:59], a[196:199], v[116:119], v[56:59]\n\t"
    "v_mfma_f32_16x16x32_bf16 v[56:59], a[200:203], v[120:123], v[56:59]\n\t"
    "v_mfma_f32_16x16x32_bf16 v[56:59], a[204:207], v[124:127], v[56:59]\n\t"
    "v_mfma_f32_16x16x32_bf16 a[228:231], a[248:251], v[20:23], 0\n\t"
    "v_mfma_f32_16x16x32_bf16 a[32:35], v[224:227], v[20:23], 0\n\t"
    "v_mfma_f32_16x16x32_bf16 a[36:39], v[228:231], v[20:23], 0\n\t"
    "v_mfma_f32_16x16x32_bf16 a[40:43], v[232:235], v[20:23], 0\n\t"
    "v_max3_f32 v60, v56, v57, v58\n\t"
    "v_max_f32 v60, v60, v59\n\t"
    "v_mfma_f32_16x16x32_bf16 a[44:47], v[236:239], v[20:23], 0\n\t"
    "v_mov_b32 v61, v60\n\t"
    "v_mfma_f32_16x16x32_bf16 a[48:51], v[240:243], v[20:23], 0\n\t"
    "s_nop 0\n\t"
    "v_permlane16_swap_b32 v60, v61\n\t"
    "v_mfma_f32_16x16x32_bf16 a[52:55], v[244:247], v[20:23], 0\n\t"
    "v_max_f32 v60, v60, v61\n\t"
    "v_mov_b32 v61, v60\n\t"
    "v_mfma_f32_16x16x32_bf16 a[56:59], v[248:251], v[20:23], 0\n\t"
    "s_nop 0\n\t"
    "v_permlane32_swap_b32 v60, v61\n\t"
    "v_max_f32 v60, v60, v61\n\t"
    "v_sub_f32 v32, 0, v60\n\t"
    "v_sub_f32 v33, 0, v60\n\t"
    "v_sub_f32 v34, 0, v60\n\t"
    "v_sub_f32 v35, 0, v60\n\t"
    "v_mfma_f32_16x16x32_bf16 a[60:63], v[252:255], v[20:23], 0\n\t"
    "s_nop 0\n\t"
    "v_mfma_f32_16x16x32_bf16 v[0:3], a[192:195], v[112:115], v[32:35]\n\t"
    "v_mfma_f32_16x16x32_bf16 v[0:3], a[196:199], v[116:119], v[0:3]\n\t"
    "v_mfma_f32_16x16x32_bf16 v[0:3], a[200:203], v[120:123], v[0:3]\n\t"
    "v_mfma_f32_16x16x32_bf16 v[0:3], a[204:207], v[124:127], v[0:3]\n\t"
    "v_mfma_f32_16x16x32_bf16 v[4:7], a[208:211], v[112:115], v[32:35]\n\t"
    "v_mfma_f32_16x16x32_bf16 v[4:7], a[212:215], v[116:119], v[4:7]\n\t"
    "v_mfma_f32_16x16x32_bf16 v[4:7], a[216:219], v[120:123], v[4:7]\n\t"
    "v_mfma_f32_16x16x32_bf16 v[4:7], a[220:223], v[124:127], v[4:7]\n\t"
    "v_exp_f32 v0, v0\n\t"
    "v_exp_f32 v1, v1\n\t"
    "v_exp_f32 v2, v2\n\t"
    "v_exp_f32 v3, v3\n\t"
    "v_cvt_pk_bf16_f32 v16, v0, v1\n\t"
    "v_cvt_pk_bf16_f32 v17, v2, v3\n\t"
    "s_nop 1\n\t"
    "v_exp_f32 v4, v4\n\t"
    "v_exp_f32 v5, v5\n\t"
    "v_exp_f32 v6, v6\n\t"
    "v_exp_f32 v7, v7\n\t"
    "v_cvt_pk_bf16_f32 v18, v4, v5\n\t"
    "v_cvt_pk_bf16_f32 v19, v6, v7\n\t"
    "s_add_u32 m0, s99, 65536\n\t"
    "s_nop 0\n\t"
    "buffer_load_dwordx4 v208, s[28:31], s56 offen offset:0 nt lds\n\t"
    "buffer_load_dwordx4 v208, s[28:31], s56 offen offset:1024 nt lds\n\t"
    "buffer_load_dwordx4 v208, s[28:31], s56 offen offset:2048 nt lds\n\t"
    "buffer_load_dwordx4 v208, s[28:31], s56 offen offset:3072 nt lds\n\t"
    "s_waitcnt vmcnt(10)\n\t"
    "s_barrier\n\t"
    "ds_read_b128 a[160:163], v48 offset:8192\n\t"
    "ds_read_b128 a[164:167], v48 offset:8448\n\t"
    "ds_read_b128 a[168:171], v48 offset:8704\n\t"
    "ds_read_b128 a[172:175], v48 offset:8960\n\t"
    "ds_read_b128 a[176:179], v48 offset:12288\n\t"
    "ds_read_b128 a[180:183], v48 offset:12544\n\t"
    "ds_read_b128 a[184:187], v48 offset:12800\n\t"
    "ds_read_b128 a[188:191], v48 offset:13056\n\t"
    "v_mfma_f32_16x16x32_bf16 a[232:235], a[248:251], v[16:19], 0\n\t"
    "v_mfma_f32_16x16x32_bf16 a[64:67], v[224:227], v[16:19], 0\n\t"
    "v_mfma_f32_16x16x32_bf16 a[68:71], v[228:231], v[16:19], 0\n\t"
    "v_mfma_f32_16x16x32_bf16 a[72:75], v[232:235], v[16:19], 0\n\t"
    "v_mfma_f32_16x16x32_bf16 a[76:79], v[236:239], v[16:19], 0\n\t"
    "v_mfma_f32_16x16x32_bf16 a[80:83], v[240:243], v[16:19], 0\n\t"
    "v_mfma_f32_16x16x32_bf16 a[84:87], v[244:247], v[16:19], 0\n\t"
    "v_mfma_f32_16x16x32_bf16 a[88:91], v[248:251], v[16:19], 0\n\t"
    "v_mfma_f32_16x16x32_bf16 a[92:95], v[252:255], v[16:19], 0\n\t"
    "s_waitcnt lgkmcnt(7)\n\t"
    "v_mfma_f32_16x16x32_bf16 v[8:11], a[160:163], v[80:83], v[24:27]\n\t"
    "s_waitcnt vmcnt(8) lgkmcnt(6)\n\t"
    "v_mfma_f32_16x16x32_bf16 v[8:11], a[164:167], v[84:87], v[8:11]\n\t"
    "s_waitcnt lgkmcnt(5)\n\t"
    "s_barrier\n\t"
    "ds_read_b64_tr_b16 v[176:177], v50 offset:8192\n\t"
    "ds_read_b64_tr_b16 v[178:179], v50 offset:12288\n\t"
    "v_mfma_f32_16x16x32_bf16 v[8:11], a[168:171], v[88:91], v[8:11]\n\t"
    "s_waitcnt lgkmcnt(6)\n\t"
    "ds_read_b64_tr_b16 v[180:181], v50 offset:8200\n\t"
    "ds_read_b64_tr_b16 v[182:183], v50 offset:12296\n\t"
    "v_mfma_f32_16x16x32_bf16 v[8:11], a[172:175], v[92:95], v[8:11]\n\t"
    "s_waitcnt lgkmcnt(7)\n\t"
    "ds_read_b64_tr_b16 v[184:185], v50 offset:8448\n\t"
    "ds_read_b64_tr_b16 v[186:187], v50 offset:12544\n\t"
    "v_mfma_f32_16x16x32_bf16 v[12:15], a[176:179], v[80:83], v[24:27]\n\t"
    "s_waitcnt lgkmcnt(8)\n\t"
    "ds_read_b64_tr_b16 v[188:189], v50 offset:8456\n\t"
    "ds_read_b64_tr_b16 v[190:191], v50 offset:12552\n\t"
    "v_mfma_f32_16x16x32_bf16 v[12:15], a[180:183], v[84:87], v[12:15]\n\t"
    "s_waitcnt lgkmcnt(9)\n\t"
    "ds_read_b64_tr_b16 v[192:193], v50 offset:8704\n\t"
    "ds_read_b64_tr_b16 v[194:195], v50 offset:12800\n\t"
    "v_mfma_f32_16x16x32_bf16 v[12:15], a[184:187], v[88:91], v[12:15]\n\t"
    "s_waitcnt lgkmcnt(10)\n\t"
    "ds_read_b64_tr_b16 v[196:197], v50 offset:8712\n\t"
    "ds_read_b64_tr_b16 v[198:199], v50 offset:12808\n\t"
    "v_mfma_f32_16x16x32_bf16 v[12:15], a[188:191], v[92:95], v[12:15]\n\t"
    "v_exp_f32 v8, v8\n\t"
    "v_mfma_f32_16x16x32_bf16 v[0:3], a[160:163], v[96:99], v[28:31]\n\t"
    "v_exp_f32 v9, v9\n\t"
    "v_mfma_f32_16x16x32_bf16 v[0:3], a[164:167], v[100:103], v[0:3]\n\t"
    "v_exp_f32 v10, v10\n\t"
    "v_mfma_f32_16x16x32_bf16 v[0:3], a[168:171], v[104:107], v[0:3]\n\t"
    "v_exp_f32 v11, v11\n\t"
    "v_mfma_f32_16x16x32_bf16 v[0:3], a[172:175], v[108:111], v[0:3]\n\t"
    "v_exp_f32 v12, v12\n\t"
    "v_mfma_f32_16x16x32_bf16 v[4:7], a[176:179], v[96:99], v[28:31]\n\t"
    "v_exp_f32 v13, v13\n\t"
    "v_mfma_f32_16x16x32_bf16 v[4:7], a[180:183], v[100:103], v[4:7]\n\t"
    "v_exp_f32 v14, v14\n\t"
    "v_exp_f32 v15, v15\n\t"
    "v_mfma_f32_16x16x32_bf16 v[4:7], a[184:187], v[104:107], v[4:7]\n\t"
    "v_cvt_pk_bf16_f32 v20, v8, v9\n\t"
    "v_cvt_pk_bf16_f32 v21, v10, v11\n\t"
    "v_cvt_pk_bf16_f32 v22, v12, v13\n\t"
    "v_cvt_pk_bf16_f32 v23, v14, v15\n\t"
    "v_mfma_f32_16x16x32_bf16 v[4:7], a[188:191], v[108:111], v[4:7]\n\t"
    "ds_read_b64_tr_b16 v[200:201], v50 offset:8960\n\t"
    "ds_read_b64_tr_b16 v[202:203], v50 offset:13056\n\t"
    "v_mfma_f32_16x16x32_bf16 a[224:227], a[248:251], v[20:23], a[224:227]\n\t"
    "s_waitcnt lgkmcnt(2)\n\t"
    "ds_read_b64_tr_b16 v[204:205], v50 offset:8968\n\t"
    "ds_read_b64_tr_b16 v[206:207], v50 offset:13064\n\t"
    "v_mfma_f32_16x16x32_bf16 a[0:3], v[176:179], v[20:23], a[0:3]\n\t"
    "v_exp_f32 v0, v0\n\t"
    "v_mfma_f32_16x16x32_bf16 a[4:7], v[180:183], v[20:23], a[4:7]\n\t"
    "v_exp_f32 v1, v1\n\t"
    "v_mfma_f32_16x16x32_bf16 a[8:11], v[184:187], v[20:23], a[8:11]\n\t"
    "v_exp_f32 v2, v2\n\t"
    "v_mfma_f32_16x16x32_bf16 a[12:15], v[188:191], v[20:23], a[12:15]\n\t"
    "v_exp_f32 v3, v3\n\t"
    "v_mfma_f32_16x16x32_bf16 a[16:19], v[192:195], v[20:23], a[16:19]\n\t"
    "v_exp_f32 v4, v4\n\t"
    "v_mfma_f32_16x16x32_bf16 a[20:23], v[196:199], v[20:23], a[20:23]\n\t"
    "s_waitcnt lgkmcnt(2)\n\t"
    "v_exp_f32 v5, v5\n\t"
    "v_mfma_f32_16x16x32_bf16 a[24:27], v[200:203], v[20:23], a[24:27]\n\t"
    "s_waitcnt lgkmcnt(0)\n\t"
    "v_exp_f32 v6, v6\n\t"
    "v_mfma_f32_16x16x32_bf16 a[28:31], v[204:207], v[20:23], a[28:31]\n\t"
    "v_exp_f32 v7, v7\n\t"
    "v_mfma_f32_16x16x32_bf16 v[8:11], a[160:163], v[112:115], v[32:35]\n\t"
    "v_cvt_pk_bf16_f32 v16, v0, v1\n\t"
    "v_cvt_pk_bf16_f32 v17, v2, v3\n\t"
    "v_mfma_f32_16x16x32_bf16 v[8:11], a[164:167], v[116:119], v[8:11]\n\t"
    "v_cvt_pk_bf16_f32 v18, v4, v5\n\t"
    "v_cvt_pk_bf16_f32 v19, v6, v7\n\t"
    "v_mfma_f32_16x16x32_bf16 v[8:11], a[168:171], v[120:123], v[8:11]\n\t"
    "v_mfma_f32_16x16x32_bf16 v[8:11], a[172:175], v[124:127], v[8:11]\n\t"
    "v_mfma_f32_16x16x32_bf16 v[12:15], a[176:179], v[112:115], v[32:35]\n\t"
    "v_mfma_f32_16x16x32_bf16 v[12:15], a[180:183], v[116:119], v[12:15]\n\t"
    "v_mfma_f32_16x16x32_bf16 v[12:15], a[184:187], v[120:123], v[12:15]\n\t"
    "v_mfma_f32_16x16x32_bf16 v[12:15], a[188:191], v[124:127], v[12:15]\n\t"
    "v_exp_f32 v8, v8\n\t"
    "v_mfma_f32_16x16x32_bf16 a[228:231], a[248:251], v[16:19], a[228:231]\n\t"
    "v_exp_f32 v9, v9\n\t"
    "v_mfma_f32_16x16x32_bf16 a[32:35], v[176:179], v[16:19], a[32:35]\n\t"
    "v_exp_f32 v10, v10\n\t"
    "v_mfma_f32_16x16x32_bf16 a[36:39], v[180:183], v[16:19], a[36:39]\n\t"
    "v_exp_f32 v11, v11\n\t"
    "v_mfma_f32_16x16x32_bf16 a[40:43], v[184:187], v[16:19], a[40:43]\n\t"
    "v_exp_f32 v12, v12\n\t"
    "v_mfma_f32_16x16x32_bf16 a[44:47], v[188:191], v[16:19], a[44:47]\n\t"
    "v_exp_f32 v13, v13\n\t"
    "v_mfma_f32_16x16x32_bf16 a[48:51], v[192:195], v[16:19], a[48:51]\n\t"
    "v_exp_f32 v14, v14\n\t"
    "v_mfma_f32_16x16x32_bf16 a[52:55], v[196:199], v[16:19], a[52:55]\n\t"
    "v_exp_f32 v15, v15\n\t"
    "v_mfma_f32_16x16x32_bf16 a[56:59], v[200:203], v[16:19], a[56:59]\n\t"
    "v_cvt_pk_bf16_f32 v20, v8, v9\n\t"
    "v_cvt_pk_bf16_f32 v21, v10, v11\n\t"
    "v_mfma_f32_16x16x32_bf16 a[60:63], v[204:207], v[16:19], a[60:63]\n\t"
    "v_cvt_pk_bf16_f32 v22, v12, v13\n\t"
    "v_cvt_pk_bf16_f32 v23, v14, v15\n\t"
    "s_add_u32 m0, s51, 16384\n\t"
    "s_nop 0\n\t"
    "buffer_load_dwordx4 v52, s[36:39], s66 offen offset:0 lds\n\t"
    "buffer_load_dwordx4 v53, s[36:39], s66 offen offset:1024 lds\n\t"
    "s_add_u32 m0, s51, 98304\n\t"
    "s_nop 0\n\t"
    "buffer_load_dwordx4 v52, s[40:43], s66 offen offset:0 lds\n\t"
    "buffer_load_dwordx4 v53, s[40:43], s66 offen offset:1024 lds\n\t"
    "s_mov_b64 vcc, -1\n\ts_cmp_eq_u32 %3, 0\n\ts_cbranch_scc1 PAD1_%=\n\ts_cmp_eq_u32 %3, 1\n\ts_cbranch_scc1 PAD2_%=\n\ts_cmp_eq_u32 %3, 2\n\ts_cbranch_scc1 PAD3_%=\n\ts_branch PAD4_%=\nPADRET_%=:\n\ts_mov_b64 vcc, 0\n\t"
    "s_waitcnt vmcnt(8)\n\t"
    "ds_read_b128 v[128:131], v209 offset:24576\n\t"
    "ds_read_b128 v[132:135], v209 offset:24832\n\t"
    "ds_read_b128 v[136:139], v209 offset:25088\n\t"
    "ds_read_b128 v[140:143], v209 offset:25344\n\t"
    "s_waitcnt lgkmcnt(3)\n\t"
    "v_lshlrev_b32 v64, 16, v128\n\t"
    "v_and_b32 v65, s49, v128\n\t"
    "v_pk_mul_f32 v[64:65], v[64:65], s[48:49] op_sel_hi:[1,0]\n\t"
    "v_cvt_pk_bf16_f32 v128, v64, v65\n\t"
    "v_lshlrev_b32 v66, 16, v129\n\t"
    "v_and_b32 v67, s49, v129\n\t"
    "v_pk_mul_f32 v[66:67], v[66:67], s[48:49] op_sel_hi:[1,0]\n\t"
    "v_cvt_pk_bf16_f32 v129, v66, v67\n\t"
    "v_lshlrev_b32 v68, 16, v130\n\t"
    "v_and_b32 v69, s49, v130\n\t"
    "v_pk_mul_f32 v[68:69], v[68:69], s[48:49] op_sel_hi:[1,0]\n\t"
    "v_cvt_pk_bf16_f32 v130, v68, v69\n\t"
    "v_lshlrev_b32 v70, 16, v131\n\t"
    "v_and_b32 v71, s49, v131\n\t"
    "v_pk_mul_f32 v[70:71], v[70:71], s[48:49] op_sel_hi:[1,0]\n\t"
    "v_cvt_pk_bf16_f32 v131, v70, v71\n\t"
    "s_waitcnt lgkmcnt(2)\n\t"
    "v_lshlrev_b32 v64, 16, v132\n\t"
    "v_and_b32 v65, s49, v132\n\t"
    "v_pk_mul_f32 v[64:65], v[64:65], s[48:49] op_sel_hi:[1,0]\n\t"
    "v_cvt_pk_bf16_f32 v132, v64, v65\n\t"
    "v_lshlrev_b32 v66, 16, v133\n\t"
    "v_and_b32 v67, s49, v133\n\t"
    "v_pk_mul_f32 v[66:67], v[66:67], s[48:49] op_sel_hi:[1,0]\n\t"
    "v_cvt_pk_bf16_f32 v133, v66, v67\n\t"
    "v_lshlrev_b32 v68, 16, v134\n\t"
    "v_and_b32 v69, s49, v134\n\t"
    "v_pk_mul_f32 v[68:69], v[68:69], s[48:49] op_sel_hi:[1,0]\n\t"
    "v_cvt_pk_bf16_f32 v134, v68, v69\n\t"
    "v_lshlrev_b32 v70, 16, v135\n\t"
    "v_and_b32 v71, s49, v135\n\t"
    "v_pk_mul_f32 v[70:71], v[70:71], s[48:49] op_sel_hi:[1,0]\n\t"
    "v_cvt_pk_bf16_f32 v135, v70, v71\n\t"
    "s_waitcnt lgkmcnt(1)\n\t"
    "v_lshlrev_b32 v64, 16, v136\n\t"
    "v_and_b32 v65, s49, v136\n\t"
    "v_pk_mul_f32 v[64:65], v[64:65], s[48:49] op_sel_hi:[1,0]\n\t"
    "v_cvt_pk_bf16_f32 v136, v64, v65\n\t"
    "v_lshlrev_b32 v66, 16, v137\n\t"
    "v_and_b32 v67, s49, v137\n\t"
    "v_pk_mul_f32 v[66:67], v[66:67], s[48:49] op_sel_hi:[1,0]\n\t"
    "v_cvt_pk_bf16_f32 v137, v66, v67\n\t"
    "v_lshlrev_b32 v68, 16, v138\n\t"
    "v_and_b32 v69, s49, v138\n\t"
    "v_pk_mul_f32 v[68:69], v[68:69], s[48:49] op_sel_hi:[1,0]\n\t"
    "v_cvt_pk_bf16_f32 v138, v68, v69\n\t"
    "v_lshlrev_b32 v70, 16, v139\n\t"
    "v_and_b32 v71, s49, v139\n\t"
    "v_pk_mul_f32 v[70:71], v[70:71], s[48:49] op_sel_hi:[1,0]\n\t"
    "v_cvt_pk_bf16_f32 v139, v70, v71\n\t"
    "s_waitcnt lgkmcnt(0)\n\t"
    "v_lshlrev_b32 v64, 16, v140\n\t"
    "v_and_b32 v65, s49, v140\n\t"
    "v_pk_mul_f32 v[64:65], v[64:65], s[48:49] op_sel_hi:[1,0]\n\t"
    "v_cvt_pk_bf16_f32 v140, v64, v65\n\t"
    "v_lshlrev_b32 v66, 16, v141\n\t"
    "v_and_b32 v67, s49, v141\n\t"
    "v_pk_mul_f32 v[66:67], v[66:67], s[48:49] op_sel_hi:[1,0]\n\t"
    "v_cvt_pk_bf16_f32 v141, v66, v67\n\t"
    "v_lshlrev_b32 v68, 16, v142\n\t"
    "v_and_b32 v69, s49, v142\n\t"
    "v_pk_mul_f32 v[68:69], v[68:69], s[48:49] op_sel_hi:[1,0]\n\t"
    "v_cvt_pk_bf16_f32 v142, v68, v69\n\t"
    "v_lshlrev_b32 v70, 16, v143\n\t"
    "v_and_b32 v71, s49, v143\n\t"
    "v_pk_mul_f32 v[70:71], v[70:71], s[48:49] op_sel_hi:[1,0]\n\t"
    "v_cvt_pk_bf16_f32 v143, v70, v71\n\t"
    "v_mfma_f32_16x16x32_bf16 v[56:59], a[192:195], v[128:131], 0\n\t"
    "v_mfma_f32_16x16x32_bf16 v[56:59], a[196:199], v[132:135], v[56:59]\n\t"
    "v_mfma_f32_16x16x32_bf16 v[56:59], a[200:203], v[136:139], v[56:59]\n\t"
    "v_mfma_f32_16x16x32_bf16 v[56:59], a[204:207], v[140:143], v[56:59]\n\t"
    "v_mfma_f32_16x16x32_bf16 a[232:235], a[248:251], v[20:23], a[232:235]\n\t"
    "v_mfma_f32_16x16x32_bf16 a[64:67], v[176:179], v[20:23], a[64:67]\n\t"
    "v_mfma_f32_16x16x32_bf16 a[68:71], v[180:183], v[20:23], a[68:71]\n\t"
    "v_mfma_f32_16x16x32_bf16 a[72:75], v[184:187], v[20:23], a[72:75]\n\t"
    "v_max3_f32 v60, v56, v57, v58\n\t"
    "v_max_f32 v60, v60, v59\n\t"
    "v_mfma_f32_16x16x32_bf16 a[76:79], v[188:191], v[20:23], a[76:79]\n\t"
    "v_mov_b32 v61, v60\n\t"
    "v_mfma_f32_16x16x32_bf16 a[80:83], v[192:195], v[20:23], a[80:83]\n\t"
    "s_nop 0\n\t"
    "v_permlane16_swap_b32 v60, v61\n\t"
    "v_mfma_f32_16x16x32_bf16 a[84:87], v[196:199], v[20:23], a[84:87]\n\t"
    "v_max_f32 v60, v60, v61\n\t"
    "v_mov_b32 v61, v60\n\t"
    "v_mfma_f32_16x16x32_bf16 a[88:91], v[200:203], v[20:23], a[88:91]\n\t"
    "s_nop 0\n\t"
    "v_permlane32_swap_b32 v60, v61\n\t"
    "v_max_f32 v60, v60, v61\n\t"
    "v_sub_f32 v36, 0, v60\n\t"
    "v_sub_f32 v37, 0, v60\n\t"
    "v_sub_f32 v38, 0, v60\n\t"
    "v_sub_f32 v39, 0, v60\n\t"
    "v_mfma_f32_16x16x32_bf16 a[92:95], v[204:207], v[20:23], a[92:95]\n\t"
    "s_nop 0\n\t"
    "v_mfma_f32_16x16x32_bf16 v[0:3], a[160:163], v[128:131], v[36:39]\n\t"
    "v_mfma_f32_16x16x32_bf16 v[0:3], a[164:167], v[132:135], v[0:3]\n\t"
    "v_mfma_f32_16x16x32_bf16 v[0:3], a[168:171], v[136:139], v[0:3]\n\t"
    "v_mfma_f32_16x16x32_bf16 v[0:3], a[172:175], v[140:143], v[0:3]\n\t"
    "v_mfma_f32_16x16x32_bf16 v[4:7], a[176:179], v[128:131], v[36:39]\n\t"
    "v_mfma_f32_16x16x32_bf16 v[4:7], a[180:183], v[132:135], v[4:7]\n\t"
    "v_mfma_f32_16x16x32_bf16 v[4:7], a[184:187], v[136:139], v[4:7]\n\t"
    "v_mfma_f32_16x16x32_bf16 v[4:7], a[188:191], v[140:143], v[4:7]\n\t"
    "v_exp_f32 v0, v0\n\t"
    "v_mfma_f32_16x16x32_bf16 v[8:11], a[192:195], v[128:131], v[36:39]\n\t"
    "v_exp_f32 v1, v1\n\t"
    "v_mfma_f32_16x16x32_bf16 v[8:11], a[196:199], v[132:135], v[8:11]\n\t"
    "v_exp_f32 v2, v2\n\t"
    "v_mfma_f32_16x16x32_bf16 v[8:11], a[200:203], v[136:139], v[8:11]\n\t"
    "v_exp_f32 v3, v3\n\t"
    "v_mfma_f32_16x16x32_bf16 v[8:11], a[204:207], v[140:143], v[8:11]\n\t"
    "v_exp_f32 v4, v4\n\t"
    "v_mfma_f32_16x16x32_bf16 v[12:15], a[208:211], v[128:131], v[36:39]\n\t"
    "v_exp_f32 v5, v5\n\t"
    "v_mfma_f32_16x16x32_bf16 v[12:15], a[212:215], v[132:135], v[12:15]\n\t"
    "v_exp_f32 v6, v6\n\t"
    "v_exp_f32 v7, v7\n\t"
    "v_mfma_f32_16x16x32_bf16 v[12:15], a[216:219], v[136:139], v[12:15]\n\t"
    "v_cvt_pk_bf16_f32 v16, v0, v1\n\t"
    "v_cvt_pk_bf16_f32 v17, v2, v3\n\t"
    "v_cvt_pk_bf16_f32 v18, v4, v5\n\t"
    "v_cvt_pk_bf16_f32 v19, v6, v7\n\t"
    "v_mfma_f32_16x16x32_bf16 v[12:15], a[220:223], v[140:143], v[12:15]\n\t"
    "v_exp_f32 v8, v8\n\t"
    "v_mfma_f32_16x16x32_bf16 a[236:239], a[248:251], v[16:19], 0\n\t"
    "v_exp_f32 v9, v9\n\t"
    "v_mfma_f32_16x16x32_bf16 a[96:99], v[176:179], v[16:19], 0\n\t"
    "v_exp_f32 v10, v10\n\t"
    "v_mfma_f32_16x16x32_bf16 a[100:103], v[180:183], v[16:19], 0\n\t"
    "v_exp_f32 v11, v11\n\t"
    "v_mfma_f32_16x16x32_bf16 a[104:107], v[184:187], v[16:19], 0\n\t"
    "v_exp_f32 v12, v12\n\t"
    "v_mfma_f32_16x16x32_bf16 a[108:111], v[188:191], v[16:19], 0\n\t"
    "v_exp_f32 v13, v13\n\t"
    "v_mfma_f32_16x16x32_bf16 a[112:115], v[192:195], v[16:19], 0\n\t"
    "v_exp_f32 v14, v14\n\t"
    "v_mfma_f32_16x16x32_bf16 a[116:119], v[196:199], v[16:19], 0\n\t"
    "v_exp_f32 v15, v15\n\t"
    "v_mfma_f32_16x16x32_bf16 a[120:123], v[200:203], v[16:19], 0\n\t"
    "v_cvt_pk_bf16_f32 v20, v8, v9\n\t"
    "v_cvt_pk_bf16_f32 v21, v10, v11\n\t"
    "v_mfma_f32_16x16x32_bf16 a[124:127], v[204:207], v[16:19], 0\n\t"
    "v_cvt_pk_bf16_f32 v22, v12, v13\n\t"
    "v_cvt_pk_bf16_f32 v23, v14, v15\n\t"
    "s_add_u32 m0, s99, 73728\n\t"
    "s_nop 0\n\t"
    "buffer_load_dwordx4 v208, s[28:31], s57 offen offset:0 nt lds\n\t"
    "buffer_load_dwordx4 v208, s[28:31], s57 offen offset:1024 nt lds\n\t"
    "buffer_load_dwordx4 v208, s[28:31], s57 offen offset:2048 nt lds\n\t"
    "buffer_load_dwordx4 v208, s[28:31], s57 offen offset:3072 nt lds\n\t"
    "s_add_u32 m0, s51, 24576\n\t"
    "s_nop 0\n\t"
    "buffer_load_dwordx4 v52, s[36:39], s67 offen offset:0 lds\n\t"
    "buffer_load_dwordx4 v53, s[36:39], s67 offen offset:1024 lds\n\t"
    "s_waitcnt vmcnt(10)\n\t"
    "ds_read_b128 v[144:147], v209 offset:32768\n\t"
    "ds_read_b128 v[148:151], v209 offset:33024\n\t"
    "ds_read_b128 v[152:155], v209 offset:33280\n\t"
    "ds_read_b128 v[156:159], v209 offset:33536\n\t"
    "s_waitcnt lgkmcnt(3)\n\t"
    "v_lshlrev_b32 v64, 16, v144\n\t"
    "v_and_b32 v65, s49, v144\n\t"
    "v_pk_mul_f32 v[64:65], v[64:65], s[48:49] op_sel_hi:[1,0]\n\t"
    "v_cvt_pk_bf16_f32 v144, v64, v65\n\t"
    "v_lshlrev_b32 v66, 16, v145\n\t"
    "v_and_b32 v67, s49, v145\n\t"
    "v_pk_mul_f32 v[66:67], v[66:67], s[48:49] op_sel_hi:[1,0]\n\t"
    "v_cvt_pk_bf16_f32 v145, v66, v67\n\t"
    "v_lshlrev_b32 v68, 16, v146\n\t"
    "v_and_b32 v69, s49, v146\n\t"
    "v_pk_mul_f32 v[68:69], v[68:69], s[48:49] op_sel_hi:[1,0]\n\t"
    "v_cvt_pk_bf16_f32 v146, v68, v69\n\t"
    "v_lshlrev_b32 v70, 16, v147\n\t"
    "v_and_b32 v71, s49, v147\n\t"
    "v_pk_mul_f32 v[70:71], v[70:71], s[48:49] op_sel_hi:[1,0]\n\t"
    "v_cvt_pk_bf16_f32 v147, v70, v71\n\t"
    "s_waitcnt lgkmcnt(2)\n\t"
    "v_lshlrev_b32 v64, 16, v148\n\t"
    "v_and_b32 v65, s49, v148\n\t"
    "v_pk_mul_f32 v[64:65], v[64:65], s[48:49] op_sel_hi:[1,0]\n\t"
    "v_cvt_pk_bf16_f32 v148, v64, v65\n\t"
    "v_lshlrev_b32 v66, 16, v149\n\t"
    "v_and_b32 v67, s49, v149\n\t"
    "v_pk_mul_f32 v[66:67], v[66:67], s[48:49] op_sel_hi:[1,0]\n\t"
    "v_cvt_pk_bf16_f32 v149, v66, v67\n\t"
    "v_lshlrev_b32 v68, 16, v150\n\t"
    "v_and_b32 v69, s49, v150\n\t"
    "v_pk_mul_f32 v[68:69], v[68:69], s[48:49] op_sel_hi:[1,0]\n\t"
    "v_cvt_pk_bf16_f32 v150, v68, v69\n\t"
    "v_lshlrev_b32 v70, 16, v151\n\t"
    "v_and_b32 v71, s49, v151\n\t"
    "v_pk_mul_f32 v[70:71], v[70:71], s[48:49] op_sel_hi:[1,0]\n\t"
    "v_cvt_pk_bf16_f32 v151, v70, v71\n\t"
    "s_waitcnt lgkmcnt(1)\n\t"
    "v_lshlrev_b32 v64, 16, v152\n\t"
    "v_and_b32 v65, s49, v152\n\t"
    "v_pk_mul_f32 v[64:65], v[64:65], s[48:49] op_sel_hi:[1,0]\n\t"
    "v_cvt_pk_bf16_f32 v152, v64, v65\n\t"
    "v_lshlrev_b32 v66, 16, v153\n\t"
    "v_and_b32 v67, s49, v153\n\t"
    "v_pk_mul_f32 v[66:67], v[66:67], s[48:49] op_sel_hi:[1,0]\n\t"
    "v_cvt_pk_bf16_f32 v153, v66, v67\n\t"
    "v_lshlrev_b32 v68, 16, v154\n\t"
    "v_and_b32 v69, s49, v154\n\t"
    "v_pk_mul_f32 v[68:69], v[68:69], s[48:49] op_sel_hi:[1,0]\n\t"
    "v_cvt_pk_bf16_f32 v154, v68, v69\n\t"
    "v_lshlrev_b32 v70, 16, v155\n\t"
    "v_and_b32 v71, s49, v155\n\t"
    "v_pk_mul_f32 v[70:71], v[70:71], s[48:49] op_sel_hi:[1,0]\n\t"
    "v_cvt_pk_bf16_f32 v155, v70, v71\n\t"
    "s_waitcnt lgkmcnt(0)\n\t"
    "v_lshlrev_b32 v64, 16, v156\n\t"
    "v_and_b32 v65, s49, v156\n\t"
    "v_pk_mul_f32 v[64:65], v[64:65], s[48:49] op_sel_hi:[1,0]\n\t"
    "v_cvt_pk_bf16_f32 v156, v64, v65\n\t"
    "v_lshlrev_b32 v66, 16, v157\n\t"
    "v_and_b32 v67, s49, v157\n\t"
    "v_pk_mul_f32 v[66:67], v[66:67], s[48:49] op_sel_hi:[1,0]\n\t"
    "v_cvt_pk_bf16_f32 v157, v66, v67\n\t"
    "v_lshlrev_b32 v68, 16, v158\n\t"
    "v_and_b32 v69, s49, v158\n\t"
    "v_pk_mul_f32 v[68:69], v[68:69], s[48:49] op_sel_hi:[1,0]\n\t"
    "v_cvt_pk_bf16_f32 v158, v68, v69\n\t"
    "v_lshlrev_b32 v70, 16, v159\n\t"
    "v_and_b32 v71, s49, v159\n\t"
    "v_pk_mul_f32 v[70:71], v[70:71], s[48:49] op_sel_hi:[1,0]\n\t"
    "v_cvt_pk_bf16_f32 v159, v70, v71\n\t"
    "v_mfma_f32_16x16x32_bf16 v[56:59], a[192:195], v[144:147], 0\n\t"
    "v_mfma_f32_16x16x32_bf16 v[56:59], a[196:199], v[148:151], v[56:59]\n\t"
    "v_mfma_f32_16x16x32_bf16 v[56:59], a[200:203], v[152:155], v[56:59]\n\t"
    "v_mfma_f32_16x16x32_bf16 v[56:59], a[204:207], v[156:159], v[56:59]\n\t"
    "v_mfma_f32_16x16x32_bf16 a[236:239], a[248:251], v[20:23], a[236:239]\n\t"
    "v_mfma_f32_16x16x32_bf16 a[96:99], v[224:227], v[20:23], a[96:99]\n\t"
    "v_mfma_f32_16x16x32_bf16 a[100:103], v[228:231], v[20:23], a[100:103]\n\t"
    "v_mfma_f32_16x16x32_bf16 a[104:107], v[232:235], v[20:23], a[104:107]\n\t"
    "v_max3_f32 v60, v56, v57, v58\n\t"
    "v_max_f32 v60, v60, v59\n\t"
    "v_mfma_f32_16x16x32_bf16 a[108:111], v[236:239], v[20:23], a[108:111]\n\t"
    "v_mov_b32 v61, v60\n\t"
    "v_mfma_f32_16x16x32_bf16 a[112:115], v[240:243], v[20:23], a[112:115]\n\t"
    "s_nop 0\n\t"
    "v_permlane16_swap_b32 v60, v61\n\t"
    "v_mfma_f32_16x16x32_bf16 a[116:119], v[244:247], v[20:23], a[116:119]\n\t"
    "v_max_f32 v60, v60, v61\n\t"
    "v_mov_b32 v61, v60\n\t"
    "v_mfma_f32_16x16x32_bf16 a[120:123], v[248:251], v[20:23], a[120:123]\n\t"
    "s_nop 0\n\t"
    "v_permlane32_swap_b32 v60, v61\n\t"
    "v_max_f32 v60, v60, v61\n\t"
    "v_sub_f32 v40, 0, v60\n\t"
    "v_sub_f32 v41, 0, v60\n\t"
    "v_sub_f32 v42, 0, v60\n\t"
    "v_sub_f32 v43, 0, v60\n\t"
    "v_mfma_f32_16x16x32_bf16 a[124:127], v[252:255], v[20:23], a[124:127]\n\t"
    "s_nop 0\n\t"
    "v_mfma_f32_16x16x32_bf16 v[0:3], a[192:195], v[144:147], v[40:43]\n\t"
    "v_mfma_f32_16x16x32_bf16 v[0:3], a[196:199], v[148:151], v[0:3]\n\t"
    "v_mfma_f32_16x16x32_bf16 v[0:3], a[200:203], v[152:155], v[0:3]\n\t"
    "v_mfma_f32_16x16x32_bf16 v[0:3], a[204:207], v[156:159], v[0:3]\n\t"
    "v_mfma_f32_16x16x32_bf16 v[4:7], a[208:211], v[144:147], v[40:43]\n\t"
    "v_mfma_f32_16x16x32_bf16 v[4:7], a[212:215], v[148:151], v[4:7]\n\t"
    "v_mfma_f32_16x16x32_bf16 v[4:7], a[216:219], v[152:155], v[4:7]\n\t"
    "v_mfma_f32_16x16x32_bf16 v[4:7], a[220:223], v[156:159], v[4:7]\n\t"
    "v_exp_f32 v0, v0\n\t"
    "v_mfma_f32_16x16x32_bf16 v[8:11], a[160:163], v[144:147], v[40:43]\n\t"
    "v_exp_f32 v1, v1\n\t"
    "v_mfma_f32_16x16x32_bf16 v[8:11], a[164:167], v[148:151], v[8:11]\n\t"
    "v_exp_f32 v2, v2\n\t"
    "v_mfma_f32_16x16x32_bf16 v[8:11], a[168:171], v[152:155], v[8:11]\n\t"
    "v_exp_f32 v3, v3\n\t"
    "v_mfma_f32_16x16x32_bf16 v[8:11], a[172:175], v[156:159], v[8:11]\n\t"
    "v_exp_f32 v4, v4\n\t"
    "v_mfma_f32_16x16x32_bf16 v[12:15], a[176:179], v[144:147], v[40:43]\n\t"
    "v_exp_f32 v5, v5\n\t"
    "v_mfma_f32_16x16x32_bf16 v[12:15], a[180:183], v[148:151], v[12:15]\n\t"
    "v_exp_f32 v6, v6\n\t"
    "v_exp_f32 v7, v7\n\t"
    "v_mfma_f32_16x16x32_bf16 v[12:15], a[184:187], v[152:155], v[12:15]\n\t"
    "v_cvt_pk_bf16_f32 v16, v0, v1\n\t"
    "v_cvt_pk_bf16_f32 v17, v2, v3\n\t"
    "v_cvt_pk_bf16_f32 v18, v4, v5\n\t"
    "v_cvt_pk_bf16_f32 v19, v6, v7\n\t"
    "v_mfma_f32_16x16x32_bf16 v[12:15], a[188:191], v[156:159], v[12:15]\n\t"
    "v_exp_f32 v8, v8\n\t"
    "v_mfma_f32_16x16x32_bf16 a[240:243], a[248:251], v[16:19], 0\n\t"
    "v_exp_f32 v9, v9\n\t"
    "v_mfma_f32_16x16x32_bf16 a[128:131], v[224:227], v[16:19], 0\n\t"
    "v_exp_f32 v10, v10\n\t"
    "v_mfma_f32_16x16x32_bf16 a[132:135], v[228:231], v[16:19], 0\n\t"
    "v_exp_f32 v11, v11\n\t"
    "v_mfma_f32_16x16x32_bf16 a[136:139], v[232:235], v[16:19], 0\n\t"
    "v_exp_f32 v12, v12\n\t"
    "v_mfma_f32_16x16x32_bf16 a[140:143], v[236:239], v[16:19], 0\n\t"
    "v_exp_f32 v13, v13\n\t"
    "v_mfma_f32_16x16x32_bf16 a[144:147], v[240:243], v[16:19], 0\n\t"
    "v_exp_f32 v14, v14\n\t"
    "v_mfma_f32_16x16x32_bf16 a[148:151], v[244:247], v[16:19], 0\n\t"
    "v_exp_f32 v15, v15\n\t"
    "v_mfma_f32_16x16x32_bf16 a[152:155], v[248:251], v[16:19], 0\n\t"
    "v_cvt_pk_bf16_f32 v20, v8, v9\n\t"
    "v_cvt_pk_bf16_f32 v21, v10, v11\n\t"
    "v_mfma_f32_16x16x32_bf16 a[156:159], v[252:255], v[16:19], 0\n\t"
    "v_cvt_pk_bf16_f32 v22, v12, v13\n\t"
    "v_cvt_pk_bf16_f32 v23, v14, v15\n\t"
    "s_add_u32 m0, s51, 106496\n\t"
    "s_nop 0\n\t"
    "buffer_load_dwordx4 v52, s[40:43], s67 offen offset:0 lds\n\t"
    "buffer_load_dwordx4 v53, s[40:43], s67 offen offset:1024 lds\n\t"
    "s_waitcnt vmcnt(10)\n\t"
    "s_barrier\n\t"
    "ds_read_b128 a[192:195], v48 offset:16384\n\t"
    "ds_read_b128 a[196:199], v48 offset:16640\n\t"
    "ds_read_b128 a[200:203], v48 offset:16896\n\t"
    "ds_read_b128 a[204:207], v48 offset:17152\n\t"
    "ds_read_b128 a[208:211], v48 offset:20480\n\t"
    "ds_read_b128 a[212:215], v48 offset:20736\n\t"
    "ds_read_b128 a[216:219], v48 offset:20992\n\t"
    "ds_read_b128 a[220:223], v48 offset:21248\n\t"
    "v_mfma_f32_16x16x32_bf16 a[240:243], a[248:251], v[20:23], a[240:243]\n\t"
    "v_mfma_f32_16x16x32_bf16 a[128:131], v[176:179], v[20:23], a[128:131]\n\t"
    "v_mfma_f32_16x16x32_bf16 a[132:135], v[180:183], v[20:23], a[132:135]\n\t"
    "v_mfma_f32_16x16x32_bf16 a[136:139], v[184:187], v[20:23], a[136:139]\n\t"
    "v_mfma_f32_16x16x32_bf16 a[140:143], v[188:191], v[20:23], a[140:143]\n\t"
    "v_mfma_f32_16x16x32_bf16 a[144:147], v[192:195], v[20:23], a[144:147]\n\t"
    "v_mfma_f32_16x16x32_bf16 a[148:151], v[196:199], v[20:23], a[148:151]\n\t"
    "v_mfma_f32_16x16x32_bf16 a[152:155], v[200:203], v[20:23], a[152:155]\n\t"
    "v_mfma_f32_16x16x32_bf16 a[156:159], v[204:207], v[20:23], a[156:159]\n\t"
    "s_waitcnt lgkmcnt(7)\n\t"
    "v_mfma_f32_16x16x32_bf16 v[0:3], a[192:195], v[80:83], v[24:27]\n\t"
    "s_waitcnt vmcnt(8) lgkmcnt(6)\n\t"
    "v_mfma_f32_16x16x32_bf16 v[0:3], a[196:199], v[84:87], v[0:3]\n\t"
    "s_waitcnt lgkmcnt(5)\n\t"
    "s_barrier\n\t"
    "ds_read_b64_tr_b16 v[224:225], v50 offset:16384\n\t"
    "ds_read_b64_tr_b16 v[226:227], v50 offset:20480\n\t"
    "v_mfma_f32_16x16x32_bf16 v[0:3], a[200:203], v[88:91], v[0:3]\n\t"
    "s_waitcnt lgkmcnt(6)\n\t"
    "ds_read_b64_tr_b16 v[228:229], v50 offset:16392\n\t"
    "ds_read_b64_tr_b16 v[230:231], v50 offset:20488\n\t"
    "v_mfma_f32_16x16x32_bf16 v[0:3], a[204:207], v[92:95], v[0:3]\n\t"
    "s_waitcnt lgkmcnt(7)\n\t"
    "ds_read_b64_tr_b16 v[232:233], v50 offset:16640\n\t"
    "ds_read_b64_tr_b16 v[234:235], v50 offset:20736\n\t"
    "v_mfma_f32_16x16x32_bf16 v[4:7], a[208:211], v[80:83], v[24:27]\n\t"
    "s_waitcnt lgkmcnt(8)\n\t"
    "ds_read_b64_tr_b16 v[236:237], v50 offset:16648\n\t"
    "ds_read_b64_tr_b16 v[238:239], v50 offset:20744\n\t"
    "v_mfma_f32_16x16x32_bf16 v[4:7], a[212:215], v[84:87], v[4:7]\n\t"
    "s_waitcnt lgkmcnt(9)\n\t"
    "ds_read_b64_tr_b16 v[240:241], v50 offset:16896\n\t"
    "ds_read_b64_tr_b16 v[242:243], v50 offset:20992\n\t"
    "v_mfma_f32_16x16x32_bf16 v[4:7], a[216:219], v[88:91], v[4:7]\n\t"
    "s_waitcnt lgkmcnt(10)\n\t"
    "ds_read_b64_tr_b16 v[244:245], v50 offset:16904\n\t"
    "ds_read_b64_tr_b16 v[246:247], v50 offset:21000\n\t"
    "v_mfma_f32_16x16x32_bf16 v[4:7], a[220:223], v[92:95], v[4:7]\n\t"
    "v_exp_f32 v0, v0\n\t"
    "v_mfma_f32_16x16x32_bf16 v[8:11], a[192:195], v[96:99], v[28:31]\n\t"
    "v_exp_f32 v1, v1\n\t"
    "v_mfma_f32_16x16x32_bf16 v[8:11], a[196:199], v[100:103], v[8:11]\n\t"
    "v_exp_f32 v2, v2\n\t"
    "v_mfma_f32_16x16x32_bf16 v[8:11], a[200:203], v[104:107], v[8:11]\n\t"
    "v_exp_f32 v3, v3\n\t"
    "v_mfma_f32_16x16x32_bf16 v[8:11], a[204:207], v[108:111], v[8:11]\n\t"
    "v_exp_f32 v4, v4\n\t"
    "v_mfma_f32_16x16x32_bf16 v[12:15], a[208:211], v[96:99], v[28:31]\n\t"
    "v_exp_f32 v5, v5\n\t"
    "v_mfma_f32_16x16x32_bf16 v[12:15], a[212:215], v[100:103], v[12:15]\n\t"
    "v_exp_f32 v6, v6\n\t"
    "v_exp_f32 v7, v7\n\t"
    "v_mfma_f32_16x16x32_bf16 v[12:15], a[216:219], v[104:107], v[12:15]\n\t"
    "v_cvt_pk_bf16_f32 v16, v0, v1\n\t"
    "v_cvt_pk_bf16_f32 v17, v2, v3\n\t"
    "v_cvt_pk_bf16_f32 v18, v4, v5\n\t"
    "v_cvt_pk_bf16_f32 v19, v6, v7\n\t"
    "v_mfma_f32_16x16x32_bf16 v[12:15], a[220:223], v[108:111], v[12:15]\n\t"
    "ds_read_b64_tr_b16 v[248:249], v50 offset:17152\n\t"
    "ds_read_b64_tr_b16 v[250:251], v50 offset:21248\n\t"
    "v_mfma_f32_16x16x32_bf16 a[224:227], a[248:251], v[16:19], a[224:227]\n\t"
    "s_waitcnt lgkmcnt(2)\n\t"
    "ds_read_b64_tr_b16 v[252:253], v50 offset:17160\n\t"
    "ds_read_b64_tr_b16 v[254:255], v50 offset:21256\n\t"
    "v_mfma_f32_16x16x32_bf16 a[0:3], v[224:227], v[16:19], a[0:3]\n\t"
    "v_exp_f32 v8, v8\n\t"
    "v_mfma_f32_16x16x32_bf16 a[4:7], v[228:231], v[16:19], a[4:7]\n\t"
    "v_exp_f32 v9, v9\n\t"
    "v_mfma_f32_16x16x32_bf16 a[8:11], v[232:235], v[16:19], a[8:11]\n\t"
    "v_exp_f32 v10, v10\n\t"
    "v_mfma_f32_16x16x32_bf16 a[12:15], v[236:239], v[16:19], a[12:15]\n\t"
    "v_exp_f32 v11, v11\n\t"
    "v_mfma_f32_16x16x32_bf16 a[16:19], v[240:243], v[16:19], a[16:19]\n\t"
    "v_exp_f32 v12, v12\n\t"
    "v_mfma_f32_16x16x32_bf16 a[20:23], v[244:247], v[16:19], a[20:23]\n\t"
    "s_waitcnt lgkmcnt(2)\n\t"
    "v_exp_f32 v13, v13\n\t"
    "v_mfma_f32_16x16x32_bf16 a[24:27], v[248:251], v[16:19], a[24:27]\n\t"
    "s_waitcnt lgkmcnt(0)\n\t"
    "v_exp_f32 v14, v14\n\t"
    "v_mfma_f32_16x16x32_bf16 a[28:31], v[252:255], v[16:19], a[28:31]\n\t"
    "v_exp_f32 v15, v15\n\t"
    "v_mfma_f32_16x16x32_bf16 v[0:3], a[192:195], v[112:115], v[32:35]\n\t"
    "v_cvt_pk_bf16_f32 v20, v8, v9\n\t"
    "v_cvt_pk_bf16_f32 v21, v10, v11\n\t"
    "v_mfma_f32_16x16x32_bf16 v[0:3], a[196:199], v[116:119], v[0:3]\n\t"
    "v_cvt_pk_bf16_f32 v22, v12, v13\n\t"
    "v_cvt_pk_bf16_f32 v23, v14, v15\n\t"
    "v_mfma_f32_16x16x32_bf16 v[0:3], a[200:203], v[120:123], v[0:3]\n\t"
    "v_mfma_f32_16x16x32_bf16 v[0:3], a[204:207], v[124:127], v[0:3]\n\t"
    "v_mfma_f32_16x16x32_bf16 v[4:7], a[208:211], v[112:115], v[32:35]\n\t"
    "v_mfma_f32_16x16x32_bf16 v[4:7], a[212:215], v[116:119], v[4:7]\n\t"
    "v_mfma_f32_16x16x32_bf16 v[4:7], a[216:219], v[120:123], v[4:7]\n\t"
    "v_mfma_f32_16x16x32_bf16 v[4:7], a[220:223], v[124:127], v[4:7]\n\t"
    "v_exp_f32 v0, v0\n\t"
    "v_mfma_f32_16x16x32_bf16 a[228:231], a[248:251], v[20:23], a[228:231]\n\t"
    "v_exp_f32 v1, v1\n\t"
    "v_mfma_f32_16x16x32_bf16 a[32:35], v[224:227], v[20:23], a[32:35]\n\t"
    "v_exp_f32 v2, v2\n\t"
    "v_mfma_f32_16x16x32_bf16 a[36:39], v[228:231], v[20:23], a[36:39]\n\t"
    "v_exp_f32 v3, v3\n\t"
    "v_mfma_f32_16x16x32_bf16 a[40:43], v[232:235], v[20:23], a[40:43]\n\t"
    "v_exp_f32 v4, v4\n\t"
    "v_mfma_f32_16x16x32_bf16 a[44:47], v[236:239], v[20:23], a[44:47]\n\t"
    "v_exp_f32 v5, v5\n\t"
    "v_mfma_f32_16x16x32_bf16 a[48:51], v[240:243], v[20:23], a[48:51]\n\t"
    "v_exp_f32 v6, v6\n\t"
    "v_mfma_f32_16x16x32_bf16 a[52:55], v[244:247], v[20:23], a[52:55]\n\t"
    "v_exp_f32 v7, v7\n\t"
    "v_mfma_f32_16x16x32_bf16 a[56:59], v[248:251], v[20:23], a[56:59]\n\t"
    "v_cvt_pk_bf16_f32 v16, v0, v1\n\t"
    "v_cvt_pk_bf16_f32 v17, v2, v3\n\t"
    "v_mfma_f32_16x16x32_bf16 a[60:63], v[252:255], v[20:23], a[60:63]\n\t"
    "v_cvt_pk_bf16_f32 v18, v4, v5\n\t"
    "v_cvt_pk_bf16_f32 v19, v6, v7\n\t"
    "v_mfma_f32_16x16x32_bf16 v[8:11], a[192:195], v[128:131], v[36:39]\n\t"
    "v_mfma_f32_16x16x32_bf16 v[8:11], a[196:199], v[132:135], v[8:11]\n\t"
    "v_mfma_f32_16x16x32_bf16 v[8:11], a[200:203], v[136:139], v[8:11]\n\t"
    "v_mfma_f32_16x16x32_bf16 v[8:11], a[204:207], v[140:143], v[8:11]\n\t"
    "v_mfma_f32_16x16x32_bf16 v[12:15], a[208:211], v[128:131], v[36:39]\n\t"
    "v_mfma_f32_16x16x32_bf16 v[12:15], a[212:215], v[132:135], v[12:15]\n\t"
    "v_mfma_f32_16x16x32_bf16 v[12:15], a[216:219], v[136:139], v[12:15]\n\t"
    "v_mfma_f32_16x16x32_bf16 v[12:15], a[220:223], v[140:143], v[12:15]\n\t"
    "v_exp_f32 v8, v8\n\t"
    "v_mfma_f32_16x16x32_bf16 a[232:235], a[248:251], v[16:19], a[232:235]\n\t"
    "v_exp_f32 v9, v9\n\t"
    "v_mfma_f32_16x16x32_bf16 a[64:67], v[224:227], v[16:19], a[64:67]\n\t"
    "v_exp_f32 v10, v10\n\t"
    "v_mfma_f32_16x16x32_bf16 a[68:71], v[228:231], v[16:19], a[68:71]\n\t"
    "v_exp_f32 v11, v11\n\t"
    "v_mfma_f32_16x16x32_bf16 a[72:75], v[232:235], v[16:19], a[72:75]\n\t"
    "v_exp_f32 v12, v12\n\t"
    "v_mfma_f32_16x16x32_bf16 a[76:79], v[236:239], v[16:19], a[76:79]\n\t"
    "v_exp_f32 v13, v13\n\t"
    "v_mfma_f32_16x16x32_bf16 a[80:83], v[240:243], v[16:19], a[80:83]\n\t"
    "v_exp_f32 v14, v14\n\t"
    "v_mfma_f32_16x16x32_bf16 a[84:87], v[244:247], v[16:19], a[84:87]\n\t"
    "v_exp_f32 v15, v15\n\t"
    "v_mfma_f32_16x16x32_bf16 a[88:91], v[248:251], v[16:19], a[88:91]\n\t"
    "v_cvt_pk_bf16_f32 v20, v8, v9\n\t"
    "v_cvt_pk_bf16_f32 v21, v10, v11\n\t"
    "v_mfma_f32_16x16x32_bf16 a[92:95], v[252:255], v[16:19], a[92:95]\n\t"
    "v_cvt_pk_bf16_f32 v22, v12, v13\n\t"
    "v_cvt_pk_bf16_f32 v23, v14, v15\n\t"
    "v_mfma_f32_16x16x32_bf16 v[0:3], a[192:195], v[144:147], v[40:43]\n\t"
    "v_mfma_f32_16x16x32_bf16 v[0:3], a[196:199], v[148:151], v[0:3]\n\t"
    "v_mfma_f32_16x16x32_bf16 v[0:3], a[200:203], v[152:155], v[0:3]\n\t"
    "v_mfma_f32_16x16x32_bf16 v[0:3], a[204:207], v[156:159], v[0:3]\n\t"
    "v_mfma_f32_16x16x32_bf16 v[4:7], a[208:211], v[144:147], v[40:43]\n\t"
    "v_mfma_f32_16x16x32_bf16 v[4:7], a[212:215], v[148:151], v[4:7]\n\t"
    "v_mfma_f32_16x16x32_bf16 v[4:7], a[216:219], v[152:155], v[4:7]\n\t"
    "v_mfma_f32_16x16x32_bf16 v[4:7], a[220:223], v[156:159], v[4:7]\n\t"
    "v_exp_f32 v0, v0\n\t"
    "v_mfma_f32_16x16x32_bf16 a[236:239], a[248:251], v[20:23], a[236:239]\n\t"
    "v_exp_f32 v1, v1\n\t"
    "v_mfma_f32_16x16x32_bf16 a[96:99], v[224:227], v[20:23], a[96:99]\n\t"
    "v_exp_f32 v2, v2\n\t"
    "v_mfma_f32_16x16x32_bf16 a[100:103], v[228:231], v[20:23], a[100:103]\n\t"
    "v_exp_f32 v3, v3\n\t"
    "v_mfma_f32_16x16x32_bf16 a[104:107], v[232:235], v[20:23], a[104:107]\n\t"
    "v_exp_f32 v4, v4\n\t"
    "v_mfma_f32_16x16x32_bf16 a[108:111], v[236:239], v[20:23], a[108:111]\n\t"
    "v_exp_f32 v5, v5\n\t"
    "v_mfma_f32_16x16x32_bf16 a[112:115], v[240:243], v[20:23], a[112:115]\n\t"
    "v_exp_f32 v6, v6\n\t"
    "v_mfma_f32_16x16x32_bf16 a[116:119], v[244:247], v[20:23], a[116:119]\n\t"
    "v_exp_f32 v7, v7\n\t"
    "v_mfma_f32_16x16x32_bf16 a[120:123], v[248:251], v[20:23], a[120:123]\n\t"
    "v_cvt_pk_bf16_f32 v16, v0, v1\n\t"
    "v_cvt_pk_bf16_f32 v17, v2, v3\n\t"
    "v_mfma_f32_16x16x32_bf16 a[124:127], v[252:255], v[20:23], a[124:127]\n\t"
    "v_cvt_pk_bf16_f32 v18, v4, v5\n\t"
    "v_cvt_pk_bf16_f32 v19, v6, v7\n\t"
    "s_add_u32 m0, s51, 32768\n\t"
    "s_nop 0\n\t"
    "buffer_load_dwordx4 v52, s[36:39], s68 offen offset:0 lds\n\t"
    "buffer_load_dwordx4 v53, s[36:39], s68 offen offset:1024 lds\n\t"
    "s_add_u32 m0, s51, 114688\n\t"
    "s_nop 0\n\t"
    "buffer_load_dwordx4 v52, s[40:43], s68 offen offset:0 lds\n\t"
    "buffer_load_dwordx4 v53, s[40:43], s68 offen offset:1024 lds\n\t"
    "s_waitcnt vmcnt(8)\n\t"
    "ds_read_b128 v[160:163], v209 offset:40960\n\t"
    "ds_read_b128 v[164:167], v209 offset:41216\n\t"
    "ds_read_b128 v[168:171], v209 offset:41472\n\t"
    "ds_read_b128 v[172:175], v209 offset:41728\n\t"
    "s_waitcnt lgkmcnt(3)\n\t"
    "v_lshlrev_b32 v64, 16, v160\n\t"
    "v_and_b32 v65, s49, v160\n\t"
    "v_pk_mul_f32 v[64:65], v[64:65], s[48:49] op_sel_hi:[1,0]\n\t"
    "v_cvt_pk_bf16_f32 v160, v64, v65\n\t"
    "v_lshlrev_b32 v66, 16, v161\n\t"
    "v_and_b32 v67, s49, v161\n\t"
    "v_pk_mul_f32 v[66:67], v[66:67], s[48:49] op_sel_hi:[1,0]\n\t"
    "v_cvt_pk_bf16_f32 v161, v66, v67\n\t"
    "v_lshlrev_b32 v68, 16, v162\n\t"
    "v_and_b32 v69, s49, v162\n\t"
    "v_pk_mul_f32 v[68:69], v[68:69], s[48:49] op_sel_hi:[1,0]\n\t"
    "v_cvt_pk_bf16_f32 v162, v68, v69\n\t"
    "v_lshlrev_b32 v70, 16, v163\n\t"
    "v_and_b32 v71, s49, v163\n\t"
    "v_pk_mul_f32 v[70:71], v[70:71], s[48:49] op_sel_hi:[1,0]\n\t"
    "v_cvt_pk_bf16_f32 v163, v70, v71\n\t"
    "s_waitcnt lgkmcnt(2)\n\t"
    "v_lshlrev_b32 v64, 16, v164\n\t"
    "v_and_b32 v65, s49, v164\n\t"
    "v_pk_mul_f32 v[64:65], v[64:65], s[48:49] op_sel_hi:[1,0]\n\t"
    "v_cvt_pk_bf16_f32 v164, v64, v65\n\t"
    "v_lshlrev_b32 v66, 16, v165\n\t"
    "v_and_b32 v67, s49, v165\n\t"
    "v_pk_mul_f32 v[66:67], v[66:67], s[48:49] op_sel_hi:[1,0]\n\t"
    "v_cvt_pk_bf16_f32 v165, v66, v67\n\t"
    "v_lshlrev_b32 v68, 16, v166\n\t"
    "v_and_b32 v69, s49, v166\n\t"
    "v_pk_mul_f32 v[68:69], v[68:69], s[48:49] op_sel_hi:[1,0]\n\t"
    "v_cvt_pk_bf16_f32 v166, v68, v69\n\t"
    "v_lshlrev_b32 v70, 16, v167\n\t"
    "v_and_b32 v71, s49, v167\n\t"
    "v_pk_mul_f32 v[70:71], v[70:71], s[48:49] op_sel_hi:[1,0]\n\t"
    "v_cvt_pk_bf16_f32 v167, v70, v71\n\t"
    "s_waitcnt lgkmcnt(1)\n\t"
    "v_lshlrev_b32 v64, 16, v168\n\t"
    "v_and_b32 v65, s49, v168\n\t"
    "v_pk_mul_f32 v[64:65], v[64:65], s[48:49] op_sel_hi:[1,0]\n\t"
    "v_cvt_pk_bf16_f32 v168, v64, v65\n\t"
    "v_lshlrev_b32 v66, 16, v169\n\t"
    "v_and_b32 v67, s49, v169\n\t"
    "v_pk_mul_f32 v[66:67], v[66:67], s[48:49] op_sel_hi:[1,0]\n\t"
    "v_cvt_pk_bf16_f32 v169, v66, v67\n\t"
    "v_lshlrev_b32 v68, 16, v170\n\t"
    "v_and_b32 v69, s49, v170\n\t"
    "v_pk_mul_f32 v[68:69], v[68:69], s[48:49] op_sel_hi:[1,0]\n\t"
    "v_cvt_pk_bf16_f32 v170, v68, v69\n\t"
    "v_lshlrev_b32 v70, 16, v171\n\t"
    "v_and_b32 v71, s49, v171\n\t"
    "v_pk_mul_f32 v[70:71], v[70:71], s[48:49] op_sel_hi:[1,0]\n\t"
    "v_cvt_pk_bf16_f32 v171, v70, v71\n\t"
    "s_waitcnt lgkmcnt(0)\n\t"
    "v_lshlrev_b32 v64, 16, v172\n\t"
    "v_and_b32 v65, s49, v172\n\t"
    "v_pk_mul_f32 v[64:65], v[64:65], s[48:49] op_sel_hi:[1,0]\n\t"
    "v_cvt_pk_bf16_f32 v172, v64, v65\n\t"
    "v_lshlrev_b32 v66, 16, v173\n\t"
    "v_and_b32 v67, s49, v173\n\t"
    "v_pk_mul_f32 v[66:67], v[66:67], s[48:49] op_sel_hi:[1,0]\n\t"
    "v_cvt_pk_bf16_f32 v173, v66, v67\n\t"
    "v_lshlrev_b32 v68, 16, v174\n\t"
    "v_and_b32 v69, s49, v174\n\t"
    "v_pk_mul_f32 v[68:69], v[68:69], s[48:49] op_sel_hi:[1,0]\n\t"
    "v_cvt_pk_bf16_f32 v174, v68, v69\n\t"
    "v_lshlrev_b32 v70, 16, v175\n\t"
    "v_and_b32 v71, s49, v175\n\t"
    "v_pk_mul_f32 v[70:71], v[70:71], s[48:49] op_sel_hi:[1,0]\n\t"
    "v_cvt_pk_bf16_f32 v175, v70, v71\n\t"
    "v_mfma_f32_16x16x32_bf16 v[56:59], a[192:195], v[160:163], 0\n\t"
    "v_mfma_f32_16x16x32_bf16 v[56:59], a[196:199], v[164:167], v[56:59]\n\t"
    "v_mfma_f32_16x16x32_bf16 v[56:59], a[200:203], v[168:171], v[56:59]\n\t"
    "v_mfma_f32_16x16x32_bf16 v[56:59], a[204:207], v[172:175], v[56:59]\n\t"
    "v_mfma_f32_16x16x32_bf16 a[240:243], a[248:251], v[16:19], a[240:243]\n\t"
    "v_mfma_f32_16x16x32_bf16 a[128:131], v[224:227], v[16:19], a[128:131]\n\t"
    "v_mfma_f32_16x16x32_bf16 a[132:135], v[228:231], v[16:19], a[132:135]\n\t"
    "v_mfma_f32_16x16x32_bf16 a[136:139], v[232:235], v[16:19], a[136:139]\n\t"
    "v_max3_f32 v60, v56, v57, v58\n\t"
    "v_max_f32 v60, v60, v59\n\t"
    "v_mfma_f32_16x16x32_bf16 a[140:143], v[236:239], v[16:19], a[140:143]\n\t"
    "v_mov_b32 v61, v60\n\t"
    "v_mfma_f32_16x16x32_bf16 a[144:147], v[240:243], v[16:19], a[144:147]\n\t"
    "s_nop 0\n\t"
    "v_permlane16_swap_b32 v60, v61\n\t"
    "v_mfma_f32_16x16x32_bf16 a[148:151], v[244:247], v[16:19], a[148:151]\n\t"
    "v_max_f32 v60, v60, v61\n\t"
    "v_mov_b32 v61, v60\n\t"
    "v_mfma_f32_16x16x32_bf16 a[152:155], v[248:251], v[16:19], a[152:155]\n\t"
    "s_nop 0\n\t"
    "v_permlane32_swap_b32 v60, v61\n\t"
    "v_max_f32 v60, v60, v61\n\t"
    "v_sub_f32 v44, 0, v60\n\t"
    "v_sub_f32 v45, 0, v60\n\t"
    "v_sub_f32 v46, 0, v60\n\t"
    "v_sub_f32 v47, 0, v60\n\t"
    "v_mfma_f32_16x16x32_bf16 a[156:159], v[252:255], v[16:19], a[156:159]\n\t"
    "s_nop 0\n\t"
    "v_mfma_f32_16x16x32_bf16 v[8:11], a[160:163], v[160:163], v[44:47]\n\t"
    "PAD1_%=:\n\ts_cbranch_vccnz PAD5_%=\n\t"
    "v_mfma_f32_16x16x32_bf16 v[8:11], a[164:167], v[164:167], v[8:11]\n\t"
    "v_mfma_f32_16x16x32_bf16 v[8:11], a[168:171], v[168:171], v[8:11]\n\t"
    "v_mfma_f32_16x16x32_bf16 v[8:11], a[172:175], v[172:175], v[8:11]\n\t"
    "v_mfma_f32_16x16x32_bf16 v[12:15], a[176:179], v[160:163], v[44:47]\n\t"
    "v_mfma_f32_16x16x32_bf16 v[12:15], a[180:183], v[164:167], v[12:15]\n\t"
    "v_mfma_f32_16x16x32_bf16 v[12:15], a[184:187], v[168:171], v[12:15]\n\t"
    "s_add_u32 m0, s51, 40960\n\t"
    "v_mfma_f32_16x16x32_bf16 v[12:15], a[188:191], v[172:175], v[12:15]\n\t"
    "v_exp_f32 v8, v8\n\t"
    "v_mfma_f32_16x16x32_bf16 v[0:3], a[192:195], v[160:163], v[44:47]\n\t"
    "v_exp_f32 v9, v9\n\t"
    "v_mfma_f32_16x16x32_bf16 v[0:3], a[196:199], v[164:167], v[0:3]\n\t"
    "v_exp_f32 v10, v10\n\t"
    "v_mfma_f32_16x16x32_bf16 v[0:3], a[200:203], v[168:171], v[0:3]\n\t"
    "v_exp_f32 v11, v11\n\t"
    "v_mfma_f32_16x16x32_bf16 v[0:3], a[204:207], v[172:175], v[0:3]\n\t"
    "v_exp_f32 v12, v12\n\t"
    "v_mfma_f32_16x16x32_bf16 v[4:7], a[208:211], v[160:163], v[44:47]\n\t"
    "v_exp_f32 v13, v13\n\t"
    "v_mfma_f32_16x16x32_bf16 v[4:7], a[212:215], v[164:167], v[4:7]\n\t"
    "buffer_load_dwordx4 v52, s[36:39], s69 offen offset:0 lds\n\t"
    "v_exp_f32 v14, v14\n\t"
    "v_exp_f32 v15, v15\n\t"
    "v_mfma_f32_16x16x32_bf16 v[4:7], a[216:219], v[168:171], v[4:7]\n\t"
    "v_cvt_pk_bf16_f32 v20, v8, v9\n\t"
    "v_cvt_pk_bf16_f32 v21, v10, v11\n\t"
    "v_cvt_pk_bf16_f32 v22, v12, v13\n\t"
    "v_cvt_pk_bf16_f32 v23, v14, v15\n\t"
    "v_mfma_f32_16x16x32_bf16 v[4:7], a[220:223], v[172:175], v[4:7]\n\t"
    "v_exp_f32 v0, v0\n\t"
    "v_mfma_f32_16x16x32_bf16 a[244:247], a[248:251], v[20:23], 0\n\t"
    "v_exp_f32 v1, v1\n\t"
    "v_mfma_f32_16x16x32_bf16 a[160:163], v[176:179], v[20:23], 0\n\t"
    "ds_read_b128 a[192:195], v48 offset:0\n\t"
    "v_cvt_pk_bf16_f32 v16, v0, v1\n\t"
    "v_mfma_f32_16x16x32_bf16 a[164:167], v[180:183], v[20:23], 0\n\t"
    "v_exp_f32 v2, v2\n\t"
    "v_mfma_f32_16x16x32_bf16 a[168:171], v[184:187], v[20:23], 0\n\t"
    "ds_read_b128 a[196:199], v48 offset:256\n\t"
    "v_mfma_f32_16x16x32_bf16 a[172:175], v[188:191], v[20:23], 0\n\t"
    "ds_read_b128 a[200:203], v48 offset:512\n\t"
    "v_mfma_f32_16x16x32_bf16 a[176:179], v[192:195], v[20:23], 0\n\t"
    "ds_read_b128 a[204:207], v48 offset:768\n\t"
    "v_mfma_f32_16x16x32_bf16 a[180:183], v[196:199], v[20:23], 0\n\t"
    "ds_read_b128 a[208:211], v48 offset:4096\n\t"
    "v_mfma_f32_16x16x32_bf16 a[184:187], v[200:203], v[20:23], 0\n\t"
    "ds_read_b128 a[212:215], v48 offset:4352\n\t"
    "v_mfma_f32_16x16x32_bf16 a[188:191], v[204:207], v[20:23], 0\n\t"
    "s_waitcnt vmcnt(7) lgkmcnt(5)\n\t"
    "ds_read_b128 a[216:219], v48 offset:4608\n\t"
    "v_mfma_f32_16x16x32_bf16 v[8:11], a[192:195], v[160:163], v[44:47]\n\t"
    "s_waitcnt lgkmcnt(5)\n\t"
    "s_barrier\n\t"
    "ds_read_b128 a[220:223], v48 offset:4864\n\t"
    "v_mfma_f32_16x16x32_bf16 v[8:11], a[196:199], v[164:167], v[8:11]\n\t"
    "s_waitcnt lgkmcnt(5)\n\t"
    "v_exp_f32 v3, v3\n\t"
    "v_mfma_f32_16x16x32_bf16 v[8:11], a[200:203], v[168:171], v[8:11]\n\t"
    "s_waitcnt lgkmcnt(4)\n\t"
    "v_exp_f32 v4, v4\n\t"
    "v_mfma_f32_16x16x32_bf16 v[8:11], a[204:207], v[172:175], v[8:11]\n\t"
    "s_waitcnt lgkmcnt(3)\n\t"
    "v_exp_f32 v5, v5\n\t"
    "v_mfma_f32_16x16x32_bf16 v[12:15], a[208:211], v[160:163], v[44:47]\n\t"
    "s_waitcnt lgkmcnt(2)\n\t"
    "v_exp_f32 v6, v6\n\t"
    "v_mfma_f32_16x16x32_bf16 v[12:15], a[212:215], v[164:167], v[12:15]\n\t"
    "s_waitcnt lgkmcnt(1)\n\t"
    "v_exp_f32 v7, v7\n\t"
    "v_mfma_f32_16x16x32_bf16 v[12:15], a[216:219], v[168:171], v[12:15]\n\t"
    "s_waitcnt lgkmcnt(0)\n\t"
    "v_cvt_pk_bf16_f32 v17, v2, v3\n\t"
    "v_cvt_pk_bf16_f32 v18, v4, v5\n\t"
    "v_cvt_pk_bf16_f32 v19, v6, v7\n\t"
    "v_mfma_f32_16x16x32_bf16 v[12:15], a[220:223], v[172:175], v[12:15]\n\t"
    "buffer_load_dwordx4 v53, s[36:39], s69 offen offset:1024 lds\n\t"
    "v_mfma_f32_16x16x32_bf16 a[244:247], a[248:251], v[16:19], a[244:247]\n\t"
    "ds_read_b64_tr_b16 v[176:177], v50 offset:0\n\t"
    "ds_read_b64_tr_b16 v[178:179], v50 offset:4096\n\t"
    "v_mfma_f32_16x16x32_bf16 a[160:163], v[224:227], v[16:19], a[160:163]\n\t"
    "ds_read_b64_tr_b16 v[180:181], v50 offset:8\n\t"
    "ds_read_b64_tr_b16 v[182:183], v50 offset:4104\n\t"
    "v_mfma_f32_16x16x32_bf16 a[164:167], v[228:231], v[16:19], a[164:167]\n\t"
    "ds_read_b128 a[192:195], v48 offset:24576\n\t"
    "v_mfma_f32_16x16x32_bf16 a[168:171], v[232:235], v[16:19], a[168:171]\n\t"
    "ds_read_b128 a[196:199], v48 offset:24832\n\t"
    "v_mfma_f32_16x16x32_bf16 a[172:175], v[236:239], v[16:19], a[172:175]\n\t"
    "ds_read_b128 a[200:203], v48 offset:25088\n\t"
    "v_mfma_f32_16x16x32_bf16 a[176:179], v[240:243], v[16:19], a[176:179]\n\t"
    "ds_read_b128 a[204:207], v48 offset:25344\n\t"
    "v_mfma_f32_16x16x32_bf16 a[180:183], v[244:247], v[16:19], a[180:183]\n\t"
    "s_add_u32 s16, s16, 15000\n\ts_addc_u32 s17, s17, 0\n\t"
    "ds_read_b128 a[208:211], v48 offset:28672\n\t"
    "v_mfma_f32_16x16x32_bf16 a[184:187], v[248:251], v[16:19], a[184:187]\n\t"
    "ds_read_b128 a[212:215], v48 offset:28928\n\t"
    "v_mfma_f32_16x16x32_bf16 a[188:191], v[252:255], v[16:19], a[188:191]\n\t"
    "s_waitcnt lgkmcnt(5)\n\t"
    "ds_read_b128 a[216:219], v48 offset:29184\n\t"
    "global_load_dword a255, v55, s[16:17]\n\t"
    "v_mfma_f32_16x16x32_bf16 v[0:3], a[192:195], v[80:83], v[24:27]\n\t"
    "s_waitcnt lgkmcnt(5)\n\t"
    "ds_read_b128 a[220:223], v48 offset:29440\n\t"
    "v_mfma_f32_16x16x32_bf16 v[0:3], a[196:199], v[84:87], v[0:3]\n\t"
    "s_waitcnt vmcnt(7) lgkmcnt(5)\n\t"
    "ds_read_b64_tr_b16 v[184:185], v50 offset:256\n\t"
    "ds_read_b64_tr_b16 v[186:187], v50 offset:4352\n\t"
    "v_mfma_f32_16x16x32_bf16 v[0:3], a[200:203], v[88:91], v[0:3]\n\t"
    "s_waitcnt lgkmcnt(6)\n\t"
    "s_barrier\n\t"
    "ds_read_b64_tr_b16 v[188:189], v50 offset:264\n\t"
    "ds_read_b64_tr_b16 v[190:191], v50 offset:4360\n\t"
    "v_mfma_f32_16x16x32_bf16 v[0:3], a[204:207], v[92:95], v[0:3]\n\t"
    "s_waitcnt lgkmcnt(7)\n\t"
    "v_exp_f32 v8, v8\n\t"
    "v_mfma_f32_16x16x32_bf16 v[4:7], a[208:211], v[80:83], v[24:27]\n\t"
    "s_waitcnt lgkmcnt(6)\n\t"
    "s_add_u32 m0, s51, 122880\n\t"
    "v_exp_f32 v9, v9\n\t"
    "v_mfma_f32_16x16x32_bf16 v[4:7], a[212:215], v[84:87], v[4:7]\n\t"
    "s_waitcnt lgkmcnt(5)\n\t"
    "v_exp_f32 v10, v10\n\t"
    "v_exp_f32 v11, v11\n\t"
    "v_exp_f32 v12, v12\n\t"
    "v_exp_f32 v13, v13\n\t"
    "v_exp_f32 v14, v14\n\t"
    "v_exp_f32 v15, v15\n\t"
    "v_mfma_f32_16x16x32_bf16 v[4:7], a[216:219], v[88:91], v[4:7]\n\t"
    "s_waitcnt lgkmcnt(4)\n\t"
    "ds_read_b64_tr_b16 v[192:193], v50 offset:512\n\t"
    "ds_read_b64_tr_b16 v[194:195], v50 offset:4608\n\t"
    "v_cvt_pk_bf16_f32 v20, v8, v9\n\t"
    "v_cvt_pk_bf16_f32 v21, v10, v11\n\t"
    "v_cvt_pk_bf16_f32 v22, v12, v13\n\t"
    "v_cvt_pk_bf16_f32 v23, v14, v15\n\t"
    "v_mfma_f32_16x16x32_bf16 v[4:7], a[220:223], v[92:95], v[4:7]\n\t"
    "ds_read_b64_tr_b16 v[196:197], v50 offset:520\n\t"
    "ds_read_b64_tr_b16 v[198:199], v50 offset:4616\n\t"
    "v_mfma_f32_16x16x32_bf16 a[244:247], a[248:251], v[20:23], a[244:247]\n\t"
    "ds_read_b64_tr_b16 v[200:201], v50 offset:768\n\t"
    "ds_read_b64_tr_b16 v[202:203], v50 offset:4864\n\t"
    "v_mfma_f32_16x16x32_bf16 a[160:163], v[176:179], v[20:23], a[160:163]\n\t"
    "ds_read_b64_tr_b16 v[204:205], v50 offset:776\n\t"
    "ds_read_b64_tr_b16 v[206:207], v50 offset:4872\n\t"
    "v_mfma_f32_16x16x32_bf16 a[164:167], v[180:183], v[20:23], a[164:167]\n\t"
    "s_waitcnt lgkmcnt(8)\n\t"
    "buffer_load_dwordx4 v52, s[40:43], s69 offen offset:0 lds\n\t"
    "v_mfma_f32_16x16x32_bf16 a[168:171], v[184:187], v[20:23], a[168:171]\n\t"
    "ds_read_b64_tr_b16 v[224:225], v50 offset:24576\n\t"
    "ds_read_b64_tr_b16 v[226:227], v50 offset:28672\n\t"
    "v_mfma_f32_16x16x32_bf16 a[172:175], v[188:191], v[20:23], a[172:175]\n\t"
    "s_waitcnt lgkmcnt(8)\n\t"
    "ds_read_b64_tr_b16 v[228:229], v50 offset:24584\n\t"
    "ds_read_b64_tr_b16 v[230:231], v50 offset:28680\n\t"
    "v_mfma_f32_16x16x32_bf16 a[176:179], v[192:195], v[20:23], a[176:179]\n\t"
    "s_waitcnt lgkmcnt(8)\n\t"
    "ds_read_b64_tr_b16 v[232:233], v50 offset:24832\n\t"
    "ds_read_b64_tr_b16 v[234:235], v50 offset:28928\n\t"
    "v_mfma_f32_16x16x32_bf16 a[180:183], v[196:199], v[20:23], a[180:183]\n\t"
    "s_waitcnt lgkmcnt(8)\n\t"
    "ds_read_b64_tr_b16 v[236:237], v50 offset:24840\n\t"
    "ds_read_b64_tr_b16 v[238:239], v50 offset:28936\n\t"
    "v_mfma_f32_16x16x32_bf16 a[184:187], v[200:203], v[20:23], a[184:187]\n\t"
    "s_waitcnt lgkmcnt(8)\n\t"
    "v_exp_f32 v0, v0\n\t"
    "v_mfma_f32_16x16x32_bf16 a[188:191], v[204:207], v[20:23], a[188:191]\n\t"
    "v_exp_f32 v1, v1\n\t"
    "v_mfma_f32_16x16x32_bf16 v[8:11], a[192:195], v[96:99], v[28:31]\n\t"
    "v_exp_f32 v2, v2\n\t"
    "v_mfma_f32_16x16x32_bf16 v[8:11], a[196:199], v[100:103], v[8:11]\n\t"
    "v_exp_f32 v3, v3\n\t"
    "v_mfma_f32_16x16x32_bf16 v[8:11], a[200:203], v[104:107], v[8:11]\n\t"
    "v_exp_f32 v4, v4\n\t"
    "v_mfma_f32_16x16x32_bf16 v[8:11], a[204:207], v[108:111], v[8:11]\n\t"
    "v_exp_f32 v5, v5\n\t"
    "v_mfma_f32_16x16x32_bf16 v[12:15], a[208:211], v[96:99], v[28:31]\n\t"
    "v_exp_f32 v6, v6\n\t"
    "v_mfma_f32_16x16x32_bf16 v[12:15], a[212:215], v[100:103], v[12:15]\n\t"
    "v_exp_f32 v7, v7\n\t"
    "v_mfma_f32_16x16x32_bf16 v[12:15], a[216:219], v[104:107], v[12:15]\n\t"
    "ds_read_b64_tr_b16 v[240:241], v50 offset:25088\n\t"
    "ds_read_b64_tr_b16 v[242:243], v50 offset:29184\n\t"
    "v_cvt_pk_bf16_f32 v16, v0, v1\n\t"
    "v_cvt_pk_bf16_f32 v17, v2, v3\n\t"
    "v_cvt_pk_bf16_f32 v18, v4, v5\n\t"
    "v_cvt_pk_bf16_f32 v19, v6, v7\n\t"
    "v_mfma_f32_16x16x32_bf16 v[12:15], a[220:223], v[108:111], v[12:15]\n\t"
    "ds_read_b64_tr_b16 v[244:245], v50 offset:25096\n\t"
    "ds_read_b64_tr_b16 v[246:247], v50 offset:29192\n\t"
    "v_mfma_f32_16x16x32_bf16 a[224:227], a[248:251], v[16:19], a[224:227]\n\t"
    "s_waitcnt lgkmcnt(4)\n\t"
    "ds_read_b64_tr_b16 v[248:249], v50 offset:25344\n\t"
    "ds_read_b64_tr_b16 v[250:251], v50 offset:29440\n\t"
    "v_mfma_f32_16x16x32_bf16 a[0:3], v[224:227], v[16:19], a[0:3]\n\t"
    "ds_read_b64_tr_b16 v[252:253], v50 offset:25352\n\t"
    "ds_read_b64_tr_b16 v[254:255], v50 offset:29448\n\t"
    "v_mfma_f32_16x16x32_bf16 a[4:7], v[228:231], v[16:19], a[4:7]\n\t"
    "buffer_load_dwordx4 v53, s[40:43], s69 offen offset:1024 lds\n\t"
    "v_mfma_f32_16x16x32_bf16 a[8:11], v[232:235], v[16:19], a[8:11]\n\t"
    "v_exp_f32 v8, v8\n\t"
    "v_mfma_f32_16x16x32_bf16 a[12:15], v[236:239], v[16:19], a[12:15]\n\t"
    "s_waitcnt lgkmcnt(6)\n\t"
    "v_exp_f32 v9, v9\n\t"
    "v_mfma_f32_16x16x32_bf16 a[16:19], v[240:243], v[16:19], a[16:19]\n\t"
    "s_waitcnt lgkmcnt(4)\n\t"
    "v_exp_f32 v10, v10\n\t"
    "v_mfma_f32_16x16x32_bf16 a[20:23], v[244:247], v[16:19], a[20:23]\n\t"
    "s_waitcnt lgkmcnt(2)\n\t"
    "v_exp_f32 v11, v11\n\t"
    "v_mfma_f32_16x16x32_bf16 a[24:27], v[248:251], v[16:19], a[24:27]\n\t"
    "s_waitcnt lgkmcnt(0)\n\t"
    "v_exp_f32 v12, v12\n\t"
    "v_mfma_f32_16x16x32_bf16 a[28:31], v[252:255], v[16:19], a[28:31]\n\t"
    "v_exp_f32 v13, v13\n\t"
    "v_mfma_f32_16x16x32_bf16 v[0:3], a[192:195], v[112:115], v[32:35]\n\t"
    "v_exp_f32 v14, v14\n\t"
    "v_mfma_f32_16x16x32_bf16 v[0:3], a[196:199], v[116:119], v[0:3]\n\t"
    "v_exp_f32 v15, v15\n\t"
    "v_mfma_f32_16x16x32_bf16 v[0:3], a[200:203], v[120:123], v[0:3]\n\t"
    "v_cvt_pk_bf16_f32 v20, v8, v9\n\t"
    "v_cvt_pk_bf16_f32 v21, v10, v11\n\t"
    "v_mfma_f32_16x16x32_bf16 v[0:3], a[204:207], v[124:127], v[0:3]\n\t"
    "v_cvt_pk_bf16_f32 v22, v12, v13\n\t"
    "v_cvt_pk_bf16_f32 v23, v14, v15\n\t"
    "v_mfma_f32_16x16x32_bf16 v[4:7], a[208:211], v[112:115], v[32:35]\n\t"
    "v_mfma_f32_16x16x32_bf16 v[4:7], a[212:215], v[116:119], v[4:7]\n\t"
    "v_mfma_f32_16x16x32_bf16 v[4:7], a[216:219], v[120:123], v[4:7]\n\t"
    "v_mfma_f32_16x16x32_bf16 v[4:7], a[220:223], v[124:127], v[4:7]\n\t"
    "v_exp_f32 v0, v0\n\t"
    "v_mfma_f32_16x16x32_bf16 a[228:231], a[248:251], v[20:23], a[228:231]\n\t"
    "v_exp_f32 v1, v1\n\t"
    "v_mfma_f32_16x16x32_bf16 a[32:35], v[224:227], v[20:23], a[32:35]\n\t"
    "v_exp_f32 v2, v2\n\t"
    "v_mfma_f32_16x16x32_bf16 a[36:39], v[228:231], v[20:23], a[36:39]\n\t"
    "s_add_u32 m0, s51, 49152\n\t"
    "v_exp_f32 v3, v3\n\t"
    "v_mfma_f32_16x16x32_bf16 a[40:43], v[232:235], v[20:23], a[40:43]\n\t"
    "buffer_load_dwordx4 v52, s[36:39], s70 offen offset:0 lds\n\t"
    "v_mfma_f32_16x16x32_bf16 a[44:47], v[236:239], v[20:23], a[44:47]\n\t"
    "v_exp_f32 v4, v4\n\t"
    "v_mfma_f32_16x16x32_bf16 a[48:51], v[240:243], v[20:23], a[48:51]\n\t"
    "v_exp_f32 v5, v5\n\t"
    "v_mfma_f32_16x16x32_bf16 a[52:55], v[244:247], v[20:23], a[52:55]\n\t"
    "v_exp_f32 v6, v6\n\t"
    "v_mfma_f32_16x16x32_bf16 a[56:59], v[248:251], v[20:23], a[56:59]\n\t"
    "v_exp_f32 v7, v7\n\t"
    "v_mfma_f32_16x16x32_bf16 a[60:63], v[252:255], v[20:23], a[60:63]\n\t"
    "v_cvt_pk_bf16_f32 v16, v0, v1\n\t"
    "v_cvt_pk_bf16_f32 v17, v2, v3\n\t"
    "v_mfma_f32_16x16x32_bf16 v[8:11], a[192:195], v[128:131], v[36:39]\n\t"
    "v_cvt_pk_bf16_f32 v18, v4, v5\n\t"
    "v_cvt_pk_bf16_f32 v19, v6, v7\n\t"
    "v_mfma_f32_16x16x32_bf16 v[8:11], a[196:199], v[132:135], v[8:11]\n\t"
    "v_mfma_f32_16x16x32_bf16 v[8:11], a[200:203], v[136:139], v[8:11]\n\t"
    "v_mfma_f32_16x16x32_bf16 v[8:11], a[204:207], v[140:143], v[8:11]\n\t"
    "v_mfma_f32_16x16x32_bf16 v[12:15], a[208:211], v[128:131], v[36:39]\n\t"
    "v_mfma_f32_16x16x32_bf16 v[12:15], a[212:215], v[132:135], v[12:15]\n\t"
    "v_mfma_f32_16x16x32_bf16 v[12:15], a[216:219], v[136:139], v[12:15]\n\t"
    "v_mfma_f32_16x16x32_bf16 v[12:15], a[220:223], v[140:143], v[12:15]\n\t"
    "v_exp_f32 v8, v8\n\t"
    "v_mfma_f32_16x16x32_bf16 a[232:235], a[248:251], v[16:19], a[232:235]\n\t"
    "v_exp_f32 v9, v9\n\t"
    "v_mfma_f32_16x16x32_bf16 a[64:67], v[224:227], v[16:19], a[64:67]\n\t"
    "v_exp_f32 v10, v10\n\t"
    "v_mfma_f32_16x16x32_bf16 a[68:71], v[228:231], v[16:19], a[68:71]\n\t"
    "v_exp_f32 v11, v11\n\t"
    "v_mfma_f32_16x16x32_bf16 a[72:75], v[232:235], v[16:19], a[72:75]\n\t"
    "v_exp_f32 v12, v12\n\t"
    "v_mfma_f32_16x16x32_bf16 a[76:79], v[236:239], v[16:19], a[76:79]\n\t"
    "v_exp_f32 v13, v13\n\t"
    "v_mfma_f32_16x16x32_bf16 a[80:83], v[240:243], v[16:19], a[80:83]\n\t"
    "v_exp_f32 v14, v14\n\t"
    "v_mfma_f32_16x16x32_bf16 a[84:87], v[244:247], v[16:19], a[84:87]\n\t"
    "buffer_load_dwordx4 v53, s[36:39], s70 offen offset:1024 lds\n\t"
    "v_mfma_f32_16x16x32_bf16 a[88:91], v[248:251], v[16:19], a[88:91]\n\t"
    "v_exp_f32 v15, v15\n\t"
    "v_mfma_f32_16x16x32_bf16 a[92:95], v[252:255], v[16:19], a[92:95]\n\t"
    "v_cvt_pk_bf16_f32 v20, v8, v9\n\t"
    "v_cvt_pk_bf16_f32 v21, v10, v11\n\t"
    "v_mfma_f32_16x16x32_bf16 v[0:3], a[192:195], v[144:147], v[40:43]\n\t"
    "v_cvt_pk_bf16_f32 v22, v12, v13\n\t"
    "v_cvt_pk_bf16_f32 v23, v14, v15\n\t"
    "v_mfma_f32_16x16x32_bf16 v[0:3], a[196:199], v[148:151], v[0:3]\n\t"
    "v_mfma_f32_16x16x32_bf16 v[0:3], a[200:203], v[152:155], v[0:3]\n\t"
    "v_mfma_f32_16x16x32_bf16 v[0:3], a[204:207], v[156:159], v[0:3]\n\t"
    "v_mfma_f32_16x16x32_bf16 v[4:7], a[208:211], v[144:147], v[40:43]\n\t"
    "v_mfma_f32_16x16x32_bf16 v[4:7], a[212:215], v[148:151], v[4:7]\n\t"
    "v_mfma_f32_16x16x32_bf16 v[4:7], a[216:219], v[152:155], v[4:7]\n\t"
    "v_mfma_f32_16x16x32_bf16 v[4:7], a[220:223], v[156:159], v[4:7]\n\t"
    "v_exp_f32 v0, v0\n\t"
    "v_mfma_f32_16x16x32_bf16 a[236:239], a[248:251], v[20:23], a[236:239]\n\t"
    "v_exp_f32 v1, v1\n\t"
    "v_mfma_f32_16x16x32_bf16 a[96:99], v[224:227], v[20:23], a[96:99]\n\t"
    "v_exp_f32 v2, v2\n\t"
    "v_mfma_f32_16x16x32_bf16 a[100:103], v[228:231], v[20:23], a[100:103]\n\t"
    "v_exp_f32 v3, v3\n\t"
    "v_mfma_f32_16x16x32_bf16 a[104:107], v[232:235], v[20:23], a[104:107]\n\t"
    "v_exp_f32 v4, v4\n\t"
    "v_mfma_f32_16x16x32_bf16 a[108:111], v[236:239], v[20:23], a[108:111]\n\t"
    "v_exp_f32 v5, v5\n\t"
    "v_mfma_f32_16x16x32_bf16 a[112:115], v[240:243], v[20:23], a[112:115]\n\t"
    "v_exp_f32 v6, v6\n\t"
    "v_mfma_f32_16x16x32_bf16 a[116:119], v[244:247], v[20:23], a[116:119]\n\t"
    "v_exp_f32 v7, v7\n\t"
    "v_mfma_f32_16x16x32_bf16 a[120:123], v[248:251], v[20:23], a[120:123]\n\t"
    "v_cvt_pk_bf16_f32 v16, v0, v1\n\t"
    "v_cvt_pk_bf16_f32 v17, v2, v3\n\t"
    "v_mfma_f32_16x16x32_bf16 a[124:127], v[252:255], v[20:23], a[124:127]\n\t"
    "s_waitcnt vmcnt(9)\n\t"
    "v_cvt_pk_bf16_f32 v18, v4, v5\n\t"
    "v_cvt_pk_bf16_f32 v19, v6, v7\n\t"
    "v_mfma_f32_16x16x32_bf16 v[8:11], a[192:195], v[160:163], v[44:47]\n\t"
    "s_barrier\n\t"
    "ds_read_b128 a[192:195], v48 offset:32768\n\t"
    "v_mfma_f32_16x16x32_bf16 v[8:11], a[196:199], v[164:167], v[8:11]\n\t"
    "s_add_u32 m0, s51, 131072\n\t"
    "ds_read_b128 a[196:199], v48 offset:33024\n\t"
    "v_mfma_f32_16x16x32_bf16 v[8:11], a[200:203], v[168:171], v[8:11]\n\t"
    "buffer_load_dwordx4 v52, s[40:43], s70 offen offset:0 lds\n\t"
    "v_mfma_f32_16x16x32_bf16 v[8:11], a[204:207], v[172:175], v[8:11]\n\t"
    "ds_read_b128 a[200:203], v48 offset:33280\n\t"
    "v_mfma_f32_16x16x32_bf16 v[12:15], a[208:211], v[160:163], v[44:47]\n\t"
    "ds_read_b128 a[204:207], v48 offset:33536\n\t"
    "v_mfma_f32_16x16x32_bf16 v[12:15], a[212:215], v[164:167], v[12:15]\n\t"
    "ds_read_b128 a[208:211], v48 offset:36864\n\t"
    "v_mfma_f32_16x16x32_bf16 v[12:15], a[216:219], v[168:171], v[12:15]\n\t"
    "ds_read_b128 a[212:215], v48 offset:37120\n\t"
    "v_mfma_f32_16x16x32_bf16 v[12:15], a[220:223], v[172:175], v[12:15]\n\t"
    "v_exp_f32 v8, v8\n\t"
    "v_mfma_f32_16x16x32_bf16 a[240:243], a[248:251], v[16:19], a[240:243]\n\t"
    "v_exp_f32 v9, v9\n\t"
    "v_mfma_f32_16x16x32_bf16 a[128:131], v[224:227], v[16:19], a[128:131]\n\t"
    "ds_read_b128 a[216:219], v48 offset:37376\n\t"
    "v_cvt_pk_bf16_f32 v20, v8, v9\n\t"
    "v_mfma_f32_16x16x32_bf16 a[132:135], v[228:231], v[16:19], a[132:135]\n\t"
    "v_exp_f32 v10, v10\n\t"
    "v_mfma_f32_16x16x32_bf16 a[136:139], v[232:235], v[16:19], a[136:139]\n\t"
    "v_exp_f32 v11, v11\n\t"
    "v_mfma_f32_16x16x32_bf16 a[140:143], v[236:239], v[16:19], a[140:143]\n\t"
    "ds_read_b128 a[220:223], v48 offset:37632\n\t"
    "v_cvt_pk_bf16_f32 v21, v10, v11\n\t"
    "v_mfma_f32_16x16x32_bf16 a[144:147], v[240:243], v[16:19], a[144:147]\n\t"
    "v_exp_f32 v12, v12\n\t"
    "v_mfma_f32_16x16x32_bf16 a[148:151], v[244:247], v[16:19], a[148:151]\n\t"
    "v_exp_f32 v13, v13\n\t"
    "v_mfma_f32_16x16x32_bf16 a[152:155], v[248:251], v[16:19], a[152:155]\n\t"
    "v_exp_f32 v14, v14\n\t"
    "v_mfma_f32_16x16x32_bf16 a[156:159], v[252:255], v[16:19], a[156:159]\n\t"
    "s_waitcnt lgkmcnt(2)\n\t"
    "v_exp_f32 v15, v15\n\t"
    "v_mfma_f32_16x16x32_bf16 v[0:3], a[192:195], v[80:83], v[24:27]\n\t"
    "v_cvt_pk_bf16_f32 v22, v12, v13\n\t"
    "v_cvt_pk_bf16_f32 v23, v14, v15\n\t"
    "v_mfma_f32_16x16x32_bf16 v[0:3], a[196:199], v[84:87], v[0:3]\n\t"
    "s_waitcnt vmcnt(8)\n\t"
    "v_mfma_f32_16x16x32_bf16 v[0:3], a[200:203], v[88:91], v[0:3]\n\t"
    "s_barrier\n\t"
    "ds_read_b64_tr_b16 v[176:177], v50 offset:32768\n\t"
    "ds_read_b64_tr_b16 v[178:179], v50 offset:36864\n\t"
    "v_mfma_f32_16x16x32_bf16 v[0:3], a[204:207], v[92:95], v[0:3]\n\t"
    "buffer_load_dwordx4 v53, s[40:43], s70 offen offset:1024 lds\n\t"
    "v_mfma_f32_16x16x32_bf16 v[4:7], a[208:211], v[80:83], v[24:27]\n\t"
    "ds_read_b64_tr_b16 v[180:181], v50 offset:32776\n\t"
    "ds_read_b64_tr_b16 v[182:183], v50 offset:36872\n\t"
    "v_mfma_f32_16x16x32_bf16 v[4:7], a[212:215], v[84:87], v[4:7]\n\t"
    "s_waitcnt lgkmcnt(4)\n\t"
    "ds_read_b64_tr_b16 v[184:185], v50 offset:33024\n\t"
    "ds_read_b64_tr_b16 v[186:187], v50 offset:37120\n\t"
    "v_mfma_f32_16x16x32_bf16 v[4:7], a[216:219], v[88:91], v[4:7]\n\t"
    "s_add_u32 s16, s16, 2048\n\ts_addc_u32 s17, s17, 0\n\t"
    "ds_read_b64_tr_b16 v[188:189], v50 offset:33032\n\t"
    "ds_read_b64_tr_b16 v[190:191], v50 offset:37128\n\t"
    "v_mfma_f32_16x16x32_bf16 v[4:7], a[220:223], v[92:95], v[4:7]\n\t"
    "v_exp_f32 v0, v0\n\t"
    "v_mfma_f32_16x16x32_bf16 a[244:247], a[248:251], v[20:23], a[244:247]\n\t"
    "ds_read_b64_tr_b16 v[192:193], v50 offset:33280\n\t"
    "global_load_dword a255, v55, s[16:17]\n\t"
    "v_mfma_f32_16x16x32_bf16 a[160:163], v[224:227], v[20:23], a[160:163]\n\t"
    "v_exp_f32 v1, v1\n\t"
    "v_mfma_f32_16x16x32_bf16 a[164:167], v[228:231], v[20:23], a[164:167]\n\t"
    "v_exp_f32 v2, v2\n\t"
    "v_mfma_f32_16x16x32_bf16 a[168:171], v[232:235], v[20:23], a[168:171]\n\t"
    "v_exp_f32 v3, v3\n\t"
    "v_mfma_f32_16x16x32_bf16 a[172:175], v[236:239], v[20:23], a[172:175]\n\t"
    "v_exp_f32 v4, v4\n\t"
    "v_mfma_f32_16x16x32_bf16 a[176:179], v[240:243], v[20:23], a[176:179]\n\t"
    "v_exp_f32 v5, v5\n\t"
    "v_mfma_f32_16x16x32_bf16 a[180:183], v[244:247], v[20:23], a[180:183]\n\t"
    "v_exp_f32 v6, v6\n\t"
    "v_mfma_f32_16x16x32_bf16 a[184:187], v[248:251], v[20:23], a[184:187]\n\t"
    "v_exp_f32 v7, v7\n\t"
    "v_mfma_f32_16x16x32_bf16 a[188:191], v[252:255], v[20:23], a[188:191]\n\t"
    "ds_read_b64_tr_b16 v[194:195], v50 offset:37376\n\t"
    "v_cvt_pk_bf16_f32 v16, v0, v1\n\t"
    "v_mfma_f32_16x16x32_bf16 v[8:11], a[192:195], v[96:99], v[28:31]\n\t"
    "v_cvt_pk_bf16_f32 v17, v2, v3\n\t"
    "v_cvt_pk_bf16_f32 v18, v4, v5\n\t"
    "v_mfma_f32_16x16x32_bf16 v[8:11], a[196:199], v[100:103], v[8:11]\n\t"
    "ds_read_b64_tr_b16 v[196:197], v50 offset:33288\n\t"
    "v_cvt_pk_bf16_f32 v19, v6, v7\n\t"
    "v_mfma_f32_16x16x32_bf16 v[8:11], a[200:203], v[104:107], v[8:11]\n\t"
    "ds_read_b64_tr_b16 v[198:199], v50 offset:37384\n\t"
    "ds_read_b64_tr_b16 v[200:201], v50 offset:33536\n\t"
    "v_mfma_f32_16x16x32_bf16 v[8:11], a[204:207], v[108:111], v[8:11]\n\t"
    "ds_read_b64_tr_b16 v[202:203], v50 offset:37632\n\t"
    "ds_read_b64_tr_b16 v[204:205], v50 offset:33544\n\t"
    "v_mfma_f32_16x16x32_bf16 v[12:15], a[208:211], v[96:99], v[28:31]\n\t"
    "ds_read_b64_tr_b16 v[206:207], v50 offset:37640\n\t"
    "v_mfma_f32_16x16x32_bf16 v[12:15], a[212:215], v[100:103], v[12:15]\n\t"
    "s_add_u32 m0, s51, 57344\n\t"
    "v_mfma_f32_16x16x32_bf16 v[12:15], a[216:219], v[104:107], v[12:15]\n\t"
    "buffer_load_dwordx4 v52, s[36:39], s71 offen offset:0 lds\n\t"
    "v_mfma_f32_16x16x32_bf16 v[12:15], a[220:223], v[108:111], v[12:15]\n\t"
    "v_exp_f32 v8, v8\n\t"
    "v_mfma_f32_16x16x32_bf16 a[224:227], a[248:251], v[16:19], a[224:227]\n\t"
    "s_waitcnt lgkmcnt(6)\n\t"
    "v_exp_f32 v9, v9\n\t"
    "v_mfma_f32_16x16x32_bf16 a[0:3], v[176:179], v[16:19], a[0:3]\n\t"
    "v_exp_f32 v10, v10\n\t"
    "v_mfma_f32_16x16x32_bf16 a[4:7], v[180:183], v[16:19], a[4:7]\n\t"
    "v_exp_f32 v11, v11\n\t"
    "v_mfma_f32_16x16x32_bf16 a[8:11], v[184:187], v[16:19], a[8:11]\n\t"
    "v_exp_f32 v12, v12\n\t"
    "v_mfma_f32_16x16x32_bf16 a[12:15], v[188:191], v[16:19], a[12:15]\n\t"
    "v_exp_f32 v13, v13\n\t"
    "v_mfma_f32_16x16x32_bf16 a[16:19], v[192:195], v[16:19], a[16:19]\n\t"
    "s_waitcnt lgkmcnt(0)\n\t"
    "v_exp_f32 v14, v14\n\t"
    "v_mfma_f32_16x16x32_bf16 a[20:23], v[196:199], v[16:19], a[20:23]\n\t"
    "v_exp_f32 v15, v15\n\t"
    "v_mfma_f32_16x16x32_bf16 a[24:27], v[200:203], v[16:19], a[24:27]\n\t"
    "v_cvt_pk_bf16_f32 v20, v8, v9\n\t"
    "v_cvt_pk_bf16_f32 v21, v10, v11\n\t"
    "v_mfma_f32_16x16x32_bf16 a[28:31], v[204:207], v[16:19], a[28:31]\n\t"
    "s_waitcnt vmcnt(6)\n\t"
    "v_cvt_pk_bf16_f32 v22, v12, v13\n\t"
    "v_cvt_pk_bf16_f32 v23, v14, v15\n\t"
    "v_mfma_f32_16x16x32_bf16 v[0:3], a[192:195], v[112:115], v[32:35]\n\t"
    "s_barrier\n\t"
    "ds_read_b64_tr_b16 v[224:225], v50 offset:40960\n\t"
    "v_mfma_f32_16x16x32_bf16 v[0:3], a[196:199], v[116:119], v[0:3]\n\t"
    "v_mfma_f32_16x16x32_bf16 v[0:3], a[200:203], v[120:123], v[0:3]\n\t"
    "ds_read_b64_tr_b16 v[226:227], v50 offset:45056\n\t"
    "v_mfma_f32_16x16x32_bf16 v[0:3], a[204:207], v[124:127], v[0:3]\n\t"
    "v_mfma_f32_16x16x32_bf16 v[4:7], a[208:211], v[112:115], v[32:35]\n\t"
    "ds_read_b64_tr_b16 v[228:229], v50 offset:40968\n\t"
    "v_mfma_f32_16x16x32_bf16 v[4:7], a[212:215], v[116:119], v[4:7]\n\t"
    "v_mfma_f32_16x16x32_bf16 v[4:7], a[216:219], v[120:123], v[4:7]\n\t"
    "ds_read_b64_tr_b16 v[230:231], v50 offset:45064\n\t"
    "v_mfma_f32_16x16x32_bf16 v[4:7], a[220:223], v[124:127], v[4:7]\n\t"
    "v_exp_f32 v0, v0\n\t"
    "v_mfma_f32_16x16x32_bf16 a[228:231], a[248:251], v[20:23], a[228:231]\n\t"
    "v_exp_f32 v1, v1\n\t"
    "v_mfma_f32_16x16x32_bf16 a[32:35], v[176:179], v[20:23], a[32:35]\n\t"
    "buffer_load_dwordx4 v53, s[36:39], s71 offen offset:1024 lds\n\t"
    "v_mfma_f32_16x16x32_bf16 a[36:39], v[180:183], v[20:23], a[36:39]\n\t"
    "v_exp_f32 v2, v2\n\t"
    "v_mfma_f32_16x16x32_bf16 a[40:43], v[184:187], v[20:23], a[40:43]\n\t"
    "v_exp_f32 v3, v3\n\t"
    "v_mfma_f32_16x16x32_bf16 a[44:47], v[188:191], v[20:23], a[44:47]\n\t"
    "v_exp_f32 v4, v4\n\t"
    "v_mfma_f32_16x16x32_bf16 a[48:51], v[192:195], v[20:23], a[48:51]\n\t"
    "v_exp_f32 v5, v5\n\t"
    "v_mfma_f32_16x16x32_bf16 a[52:55], v[196:199], v[20:23], a[52:55]\n\t"
    "v_exp_f32 v6, v6\n\t"
    "v_mfma_f32_16x16x32_bf16 a[56:59], v[200:203], v[20:23], a[56:59]\n\t"
    "v_exp_f32 v7, v7\n\t"
    "v_mfma_f32_16x16x32_bf16 a[60:63], v[204:207], v[20:23], a[60:63]\n\t"
    "v_cvt_pk_bf16_f32 v16, v0, v1\n\t"
    "v_cvt_pk_bf16_f32 v17, v2, v3\n\t"
    "v_mfma_f32_16x16x32_bf16 v[8:11], a[192:195], v[128:131], v[36:39]\n\t"
    "v_cvt_pk_bf16_f32 v18, v4, v5\n\t"
    "v_cvt_pk_bf16_f32 v19, v6, v7\n\t"
    "v_mfma_f32_16x16x32_bf16 v[8:11], a[196:199], v[132:135], v[8:11]\n\t"
    "ds_read_b64_tr_b16 v[232:233], v50 offset:41216\n\t"
    "v_mfma_f32_16x16x32_bf16 v[8:11], a[200:203], v[136:139], v[8:11]\n\t"
    "v_mfma_f32_16x16x32_bf16 v[8:11], a[204:207], v[140:143], v[8:11]\n\t"
    "ds_read_b64_tr_b16 v[234:235], v50 offset:45312\n\t"
    "v_mfma_f32_16x16x32_bf16 v[12:15], a[208:211], v[128:131], v[36:39]\n\t"
    "v_mfma_f32_16x16x32_bf16 v[12:15], a[212:215], v[132:135], v[12:15]\n\t"
    "ds_read_b64_tr_b16 v[236:237], v50 offset:41224\n\t"
    "v_mfma_f32_16x16x32_bf16 v[12:15], a[216:219], v[136:139], v[12:15]\n\t"
    "v_mfma_f32_16x16x32_bf16 v[12:15], a[220:223], v[140:143], v[12:15]\n\t"
    "v_exp_f32 v8, v8\n\t"
    "v_mfma_f32_16x16x32_bf16 a[232:235], a[248:251], v[16:19], a[232:235]\n\t"
    "v_exp_f32 v9, v9\n\t"
    "v_mfma_f32_16x16x32_bf16 a[64:67], v[176:179], v[16:19], a[64:67]\n\t"
    "v_exp_f32 v10, v10\n\t"
    "v_mfma_f32_16x16x32_bf16 a[68:71], v[180:183], v[16:19], a[68:71]\n\t"
    "v_exp_f32 v11, v11\n\t"
    "v_mfma_f32_16x16x32_bf16 a[72:75], v[184:187], v[16:19], a[72:75]\n\t"
    "s_add_u32 m0, s51, 139264\n\t"
    "v_exp_f32 v12, v12\n\t"
    "v_mfma_f32_16x16x32_bf16 a[76:79], v[188:191], v[16:19], a[76:79]\n\t"
    "buffer_load_dwordx4 v52, s[40:43], s71 offen offset:0 lds\n\t"
    "v_mfma_f32_16x16x32_bf16 a[80:83], v[192:195], v[16:19], a[80:83]\n\t"
    "v_exp_f32 v13, v13\n\t"
    "v_mfma_f32_16x16x32_bf16 a[84:87], v[196:199], v[16:19], a[84:87]\n\t"
    "v_exp_f32 v14, v14\n\t"
    "v_mfma_f32_16x16x32_bf16 a[88:91], v[200:203], v[16:19], a[88:91]\n\t"
    "v_exp_f32 v15, v15\n\t"
    "v_mfma_f32_16x16x32_bf16 a[92:95], v[204:207], v[16:19], a[92:95]\n\t"
    "v_cvt_pk_bf16_f32 v20, v8, v9\n\t"
    "v_cvt_pk_bf16_f32 v21, v10, v11\n\t"
    "v_mfma_f32_16x16x32_bf16 v[0:3], a[192:195], v[144:147], v[40:43]\n\t"
    "v_cvt_pk_bf16_f32 v22, v12, v13\n\t"
    "v_cvt_pk_bf16_f32 v23, v14, v15\n\t"
    "v_mfma_f32_16x16x32_bf16 v[0:3], a[196:199], v[148:151], v[0:3]\n\t"
    "ds_read_b64_tr_b16 v[238:239], v50 offset:45320\n\t"
    "v_mfma_f32_16x16x32_bf16 v[0:3], a[200:203], v[152:155], v[0:3]\n\t"
    "v_mfma_f32_16x16x32_bf16 v[0:3], a[204:207], v[156:159], v[0:3]\n\t"
    "ds_read_b64_tr_b16 v[240:241], v50 offset:41472\n\t"
    "v_mfma_f32_16x16x32_bf16 v[4:7], a[208:211], v[144:147], v[40:43]\n\t"
    "v_mfma_f32_16x16x32_bf16 v[4:7], a[212:215], v[148:151], v[4:7]\n\t"
    "ds_read_b64_tr_b16 v[242:243], v50 offset:45568\n\t"
    "v_mfma_f32_16x16x32_bf16 v[4:7], a[216:219], v[152:155], v[4:7]\n\t"
    "v_mfma_f32_16x16x32_bf16 v[4:7], a[220:223], v[156:159], v[4:7]\n\t"
    "v_exp_f32 v0, v0\n\t"
    "v_mfma_f32_16x16x32_bf16 a[236:239], a[248:251], v[20:23], a[236:239]\n\t"
    "v_exp_f32 v1, v1\n\t"
    "v_mfma_f32_16x16x32_bf16 a[96:99], v[176:179], v[20:23], a[96:99]\n\t"
    "v_exp_f32 v2, v2\n\t"
    "v_mfma_f32_16x16x32_bf16 a[100:103], v[180:183], v[20:23], a[100:103]\n\t"
    "v_exp_f32 v3, v3\n\t"
    "v_mfma_f32_16x16x32_bf16 a[104:107], v[184:187], v[20:23], a[104:107]\n\t"
    "v_exp_f32 v4, v4\n\t"
    "v_mfma_f32_16x16x32_bf16 a[108:111], v[188:191], v[20:23], a[108:111]\n\t"
    "v_exp_f32 v5, v5\n\t"
    "v_mfma_f32_16x16x32_bf16 a[112:115], v[192:195], v[20:23], a[112:115]\n\t"
    "v_exp_f32 v6, v6\n\t"
    "v_mfma_f32_16x16x32_bf16 a[116:119], v[196:199], v[20:23], a[116:119]\n\t"
    "v_exp_f32 v7, v7\n\t"
    "v_mfma_f32_16x16x32_bf16 a[120:123], v[200:203], v[20:23], a[120:123]\n\t"
    "buffer_load_dwordx4 v53, s[40:43], s71 offen offset:1024 lds\n\t"
    "v_mfma_f32_16x16x32_bf16 a[124:127], v[204:207], v[20:23], a[124:127]\n\t"
    "v_cvt_pk_bf16_f32 v16, v0, v1\n\t"
    "v_cvt_pk_bf16_f32 v17, v2, v3\n\t"
    "v_mfma_f32_16x16x32_bf16 v[8:11], a[192:195], v[160:163], v[44:47]\n\t"
    "v_cvt_pk_bf16_f32 v18, v4, v5\n\t"
    "v_cvt_pk_bf16_f32 v19, v6, v7\n\t"
    "v_mfma_f32_16x16x32_bf16 v[8:11], a[196:199], v[164:167], v[8:11]\n\t"
    "ds_read_b128 a[192:195], v48 offset:40960\n\t"
    "v_mfma_f32_16x16x32_bf16 v[8:11], a[200:203], v[168:171], v[8:11]\n\t"
    "ds_read_b128 a[196:199], v48 offset:41216\n\t"
    "v_mfma_f32_16x16x32_bf16 v[8:11], a[204:207], v[172:175], v[8:11]\n\t"
    "ds_read_b128 a[200:203], v48 offset:41472\n\t"
    "v_mfma_f32_16x16x32_bf16 v[12:15], a[208:211], v[160:163], v[44:47]\n\t"
    "ds_read_b128 a[204:207], v48 offset:41728\n\t"
    "v_mfma_f32_16x16x32_bf16 v[12:15], a[212:215], v[164:167], v[12:15]\n\t"
    "ds_read_b128 a[208:211], v48 offset:45056\n\t"
    "v_mfma_f32_16x16x32_bf16 v[12:15], a[216:219], v[168:171], v[12:15]\n\t"
    "ds_read_b128 a[212:215], v48 offset:45312\n\t"
    "v_mfma_f32_16x16x32_bf16 v[12:15], a[220:223], v[172:175], v[12:15]\n\t"
    "v_exp_f32 v8, v8\n\t"
    "v_mfma_f32_16x16x32_bf16 a[240:243], a[248:251], v[16:19], a[240:243]\n\t"
    "v_exp_f32 v9, v9\n\t"
    "v_mfma_f32_16x16x32_bf16 a[128:131], v[176:179], v[16:19], a[128:131]\n\t"
    "ds_read_b128 a[216:219], v48 offset:45568\n\t"
    "v_cvt_pk_bf16_f32 v20, v8, v9\n\t"
    "v_mfma_f32_16x16x32_bf16 a[132:135], v[180:183], v[16:19], a[132:135]\n\t"
    "v_exp_f32 v10, v10\n\t"
    "v_mfma_f32_16x16x32_bf16 a[136:139], v[184:187], v[16:19], a[136:139]\n\t"
    "v_exp_f32 v11, v11\n\t"
    "v_mfma_f32_16x16x32_bf16 a[140:143], v[188:191], v[16:19], a[140:143]\n\t"
    "ds_read_b128 a[220:223], v48 offset:45824\n\t"
    "v_cvt_pk_bf16_f32 v21, v10, v11\n\t"
    "v_mfma_f32_16x16x32_bf16 a[144:147], v[192:195], v[16:19], a[144:147]\n\t"
    "v_exp_f32 v12, v12\n\t"
    "v_mfma_f32_16x16x32_bf16 a[148:151], v[196:199], v[16:19], a[148:151]\n\t"
    "v_exp_f32 v13, v13\n\t"
    "v_mfma_f32_16x16x32_bf16 a[152:155], v[200:203], v[16:19], a[152:155]\n\t"
    "v_exp_f32 v14, v14\n\t"
    "v_mfma_f32_16x16x32_bf16 a[156:159], v[204:207], v[16:19], a[156:159]\n\t"
    "s_waitcnt lgkmcnt(2)\n\t"
    "v_exp_f32 v15, v15\n\t"
    "v_mfma_f32_16x16x32_bf16 v[0:3], a[192:195], v[80:83], v[24:27]\n\t"
    "s_add_u32 m0, s51, 65536\n\t"
    "v_cvt_pk_bf16_f32 v22, v12, v13\n\t"
    "v_cvt_pk_bf16_f32 v23, v14, v15\n\t"
    "v_mfma_f32_16x16x32_bf16 v[0:3], a[196:199], v[84:87], v[0:3]\n\t"
    "buffer_load_dwordx4 v52, s[36:39], s72 offen offset:0 lds\n\t"
    "v_mfma_f32_16x16x32_bf16 v[0:3], a[200:203], v[88:91], v[0:3]\n\t"
    "ds_read_b64_tr_b16 v[244:245], v50 offset:41480\n\t"
    "v_mfma_f32_16x16x32_bf16 v[0:3], a[204:207], v[92:95], v[0:3]\n\t"
    "v_mfma_f32_16x16x32_bf16 v[4:7], a[208:211], v[80:83], v[24:27]\n\t"
    "ds_read_b64_tr_b16 v[246:247], v50 offset:45576\n\t"
    "v_mfma_f32_16x16x32_bf16 v[4:7], a[212:215], v[84:87], v[4:7]\n\t"
    "s_waitcnt lgkmcnt(2)\n\t"
    "v_mfma_f32_16x16x32_bf16 v[4:7], a[216:219], v[88:91], v[4:7]\n\t"
    "ds_read_b64_tr_b16 v[248:249], v50 offset:41728\n\t"
    "v_mfma_f32_16x16x32_bf16 v[4:7], a[220:223], v[92:95], v[4:7]\n\t"
    "PAD2_%=:\n\ts_cbranch_vccnz PAD6_%=\n\t"
    "v_exp_f32 v0, v0\n\t"
    "v_mfma_f32_16x16x32_bf16 a[244:247], a[248:251], v[20:23], a[244:247]\n\t"
    "v_exp_f32 v1, v1\n\t"
    "v_mfma_f32_16x16x32_bf16 a[160:163], v[176:179], v[20:23], a[160:163]\n\t"
    "ds_read_b64_tr_b16 v[250:251], v50 offset:45824\n\t"
    "v_cvt_pk_bf16_f32 v16, v0, v1\n\t"
    "v_mfma_f32_16x16x32_bf16 a[164:167], v[180:183], v[20:23], a[164:167]\n\t"
    "v_exp_f32 v2, v2\n\t"
    "v_mfma_f32_16x16x32_bf16 a[168:171], v[184:187], v[20:23], a[168:171]\n\t"
    "v_exp_f32 v3, v3\n\t"
    "v_mfma_f32_16x16x32_bf16 a[172:175], v[188:191], v[20:23], a[172:175]\n\t"
    "v_exp_f32 v4, v4\n\t"
    "v_mfma_f32_16x16x32_bf16 a[176:179], v[192:195], v[20:23], a[176:179]\n\t"
    "v_exp_f32 v5, v5\n\t"
    "v_mfma_f32_16x16x32_bf16 a[180:183], v[196:199], v[20:23], a[180:183]\n\t"
    "v_exp_f32 v6, v6\n\t"
    "v_mfma_f32_16x16x32_bf16 a[184:187], v[200:203], v[20:23], a[184:187]\n\t"
    "v_exp_f32 v7, v7\n\t"
    "v_mfma_f32_16x16x32_bf16 a[188:191], v[204:207], v[20:23], a[188:191]\n\t"
    "s_add_u32 s16, s16, 2048\n\ts_addc_u32 s17, s17, 0\n\t"
    "v_cvt_pk_bf16_f32 v17, v2, v3\n\t"
    "v_cvt_pk_bf16_f32 v18, v4, v5\n\t"
    "v_mfma_f32_16x16x32_bf16 v[8:11], a[192:195], v[96:99], v[28:31]\n\t"
    "ds_read_b64_tr_b16 v[252:253], v50 offset:41736\n\t"
    "v_cvt_pk_bf16_f32 v19, v6, v7\n\t"
    "v_mfma_f32_16x16x32_bf16 v[8:11], a[196:199], v[100:103], v[8:11]\n\t"
    "global_load_dword a255, v55, s[16:17]\n\t"
    "v_mfma_f32_16x16x32_bf16 v[8:11], a[200:203], v[104:107], v[8:11]\n\t"
    "ds_read_b64_tr_b16 v[254:255], v50 offset:45832\n\t"
    "v_mfma_f32_16x16x32_bf16 v[8:11], a[204:207], v[108:111], v[8:11]\n\t"
    "v_mfma_f32_16x16x32_bf16 v[12:15], a[208:211], v[96:99], v[28:31]\n\t"
    "buffer_load_dwordx4 v53, s[36:39], s72 offen offset:1024 lds\n\t"
    "v_mfma_f32_16x16x32_bf16 v[12:15], a[212:215], v[100:103], v[12:15]\n\t"
    "v_mfma_f32_16x16x32_bf16 v[12:15], a[216:219], v[104:107], v[12:15]\n\t"
    "v_mfma_f32_16x16x32_bf16 v[12:15], a[220:223], v[108:111], v[12:15]\n\t"
    "v_exp_f32 v8, v8\n\t"
    "v_mfma_f32_16x16x32_bf16 a[224:227], a[248:251], v[16:19], a[224:227]\n\t"
    "v_exp_f32 v9, v9\n\t"
    "v_mfma_f32_16x16x32_bf16 a[0:3], v[224:227], v[16:19], a[0:3]\n\t"
    "v_exp_f32 v10, v10\n\t"
    "v_mfma_f32_16x16x32_bf16 a[4:7], v[228:231], v[16:19], a[4:7]\n\t"
    "v_exp_f32 v11, v11\n\t"
    "v_mfma_f32_16x16x32_bf16 a[8:11], v[232:235], v[16:19], a[8:11]\n\t"
    "v_exp_f32 v12, v12\n\t"
    "v_mfma_f32_16x16x32_bf16 a[12:15], v[236:239], v[16:19], a[12:15]\n\t"
    "v_exp_f32 v13, v13\n\t"
    "v_mfma_f32_16x16x32_bf16 a[16:19], v[240:243], v[16:19], a[16:19]\n\t"
    "s_waitcnt lgkmcnt(0)\n\t"
    "v_exp_f32 v14, v14\n\t"
    "v_mfma_f32_16x16x32_bf16 a[20:23], v[244:247], v[16:19], a[20:23]\n\t"
    "v_exp_f32 v15, v15\n\t"
    "v_mfma_f32_16x16x32_bf16 a[24:27], v[248:251], v[16:19], a[24:27]\n\t"
    "v_cvt_pk_bf16_f32 v20, v8, v9\n\t"
    "v_cvt_pk_bf16_f32 v21, v10, v11\n\t"
    "v_mfma_f32_16x16x32_bf16 a[28:31], v[252:255], v[16:19], a[28:31]\n\t"
    "s_waitcnt vmcnt(8)\n\t"
    "v_cvt_pk_bf16_f32 v22, v12, v13\n\t"
    "v_cvt_pk_bf16_f32 v23, v14, v15\n\t"
    "v_mfma_f32_16x16x32_bf16 v[0:3], a[192:195], v[112:115], v[32:35]\n\t"
    "s_barrier\n\t"
    "ds_read_b64_tr_b16 v[176:177], v50 offset:49152\n\t"
    "v_mfma_f32_16x16x32_bf16 v[0:3], a[196:199], v[116:119], v[0:3]\n\t"
    "v_mfma_f32_16x16x32_bf16 v[0:3], a[200:203], v[120:123], v[0:3]\n\t"
    "ds_read_b64_tr_b16 v[178:179], v50 offset:53248\n\t"
    "v_mfma_f32_16x16x32_bf16 v[0:3], a[204:207], v[124:127], v[0:3]\n\t"
    "v_mfma_f32_16x16x32_bf16 v[4:7], a[208:211], v[112:115], v[32:35]\n\t"
    "ds_read_b64_tr_b16 v[180:181], v50 offset:49160\n\t"
    "v_mfma_f32_16x16x32_bf16 v[4:7], a[212:215], v[116:119], v[4:7]\n\t"
    "v_mfma_f32_16x16x32_bf16 v[4:7], a[216:219], v[120:123], v[4:7]\n\t"
    "s_add_u32 m0, s51, 147456\n\t"
    "ds_read_b64_tr_b16 v[182:183], v50 offset:53256\n\t"
    "v_mfma_f32_16x16x32_bf16 v[4:7], a[220:223], v[124:127], v[4:7]\n\t"
    "buffer_load_dwordx4 v52, s[40:43], s72 offen offset:0 lds\n\t"
    "v_mfma_f32_16x16x32_bf16 a[228:231], a[248:251], v[20:23], a[228:231]\n\t"
    "v_exp_f32 v0, v0\n\t"
    "v_mfma_f32_16x16x32_bf16 a[32:35], v[224:227], v[20:23], a[32:35]\n\t"
    "v_exp_f32 v1, v1\n\t"
    "v_mfma_f32_16x16x32_bf16 a[36:39], v[228:231], v[20:23], a[36:39]\n\t"
    "v_exp_f32 v2, v2\n\t"
    "v_mfma_f32_16x16x32_bf16 a[40:43], v[232:235], v[20:23], a[40:43]\n\t"
    "v_exp_f32 v3, v3\n\t"
    "v_mfma_f32_16x16x32_bf16 a[44:47], v[236:239], v[20:23], a[44:47]\n\t"
    "v_exp_f32 v4, v4\n\t"
    "v_mfma_f32_16x16x32_bf16 a[48:51], v[240:243], v[20:23], a[48:51]\n\t"
    "v_exp_f32 v5, v5\n\t"
    "v_mfma_f32_16x16x32_bf16 a[52:55], v[244:247], v[20:23], a[52:55]\n\t"
    "v_exp_f32 v6, v6\n\t"
    "v_mfma_f32_16x16x32_bf16 a[56:59], v[248:251], v[20:23], a[56:59]\n\t"
    "v_exp_f32 v7, v7\n\t"
    "v_mfma_f32_16x16x32_bf16 a[60:63], v[252:255], v[20:23], a[60:63]\n\t"
    "v_cvt_pk_bf16_f32 v16, v0, v1\n\t"
    "v_cvt_pk_bf16_f32 v17, v2, v3\n\t"
    "v_mfma_f32_16x16x32_bf16 v[8:11], a[192:195], v[128:131], v[36:39]\n\t"
    "v_cvt_pk_bf16_f32 v18, v4, v5\n\t"
    "v_cvt_pk_bf16_f32 v19, v6, v7\n\t"
    "v_mfma_f32_16x16x32_bf16 v[8:11], a[196:199], v[132:135], v[8:11]\n\t"
    "ds_read_b64_tr_b16 v[184:185], v50 offset:49408\n\t"
    "v_mfma_f32_16x16x32_bf16 v[8:11], a[200:203], v[136:139], v[8:11]\n\t"
    "v_mfma_f32_16x16x32_bf16 v[8:11], a[204:207], v[140:143], v[8:11]\n\t"
    "ds_read_b64_tr_b16 v[186:187], v50 offset:53504\n\t"
    "v_mfma_f32_16x16x32_bf16 v[12:15], a[208:211], v[128:131], v[36:39]\n\t"
    "v_mfma_f32_16x16x32_bf16 v[12:15], a[212:215], v[132:135], v[12:15]\n\t"
    "ds_read_b64_tr_b16 v[188:189], v50 offset:49416\n\t"
    "v_mfma_f32_16x16x32_bf16 v[12:15], a[216:219], v[136:139], v[12:15]\n\t"
    "v_mfma_f32_16x16x32_bf16 v[12:15], a[220:223], v[140:143], v[12:15]\n\t"
    "v_exp_f32 v8, v8\n\t"
    "v_mfma_f32_16x16x32_bf16 a[232:235], a[248:251], v[16:19], a[232:235]\n\t"
    "v_exp_f32 v9, v9\n\t"
    "v_mfma_f32_16x16x32_bf16 a[64:67], v[224:227], v[16:19], a[64:67]\n\t"
    "v_exp_f32 v10, v10\n\t"
    "v_mfma_f32_16x16x32_bf16 a[68:71], v[228:231], v[16:19], a[68:71]\n\t"
    "buffer_load_dwordx4 v53, s[40:43], s72 offen offset:1024 lds\n\t"
    "v_mfma_f32_16x16x32_bf16 a[72:75], v[232:235], v[16:19], a[72:75]\n\t"
    "v_exp_f32 v11, v11\n\t"
    "v_mfma_f32_16x16x32_bf16 a[76:79], v[236:239], v[16:19], a[76:79]\n\t"
    "v_exp_f32 v12, v12\n\t"
    "v_mfma_f32_16x16x32_bf16 a[80:83], v[240:243], v[16:19], a[80:83]\n\t"
    "v_exp_f32 v13, v13\n\t"
    "v_mfma_f32_16x16x32_bf16 a[84:87], v[244:247], v[16:19], a[84:87]\n\t"
    "v_exp_f32 v14, v14\n\t"
    "v_mfma_f32_16x16x32_bf16 a[88:91], v[248:251], v[16:19], a[88:91]\n\t"
    "v_exp_f32 v15, v15\n\t"
    "v_mfma_f32_16x16x32_bf16 a[92:95], v[252:255], v[16:19], a[92:95]\n\t"
    "v_cvt_pk_bf16_f32 v20, v8, v9\n\t"
    "v_cvt_pk_bf16_f32 v21, v10, v11\n\t"
    "v_mfma_f32_16x16x32_bf16 v[0:3], a[192:195], v[144:147], v[40:43]\n\t"
    "v_cvt_pk_bf16_f32 v22, v12, v13\n\t"
    "v_cvt_pk_bf16_f32 v23, v14, v15\n\t"
    "v_mfma_f32_16x16x32_bf16 v[0:3], a[196:199], v[148:151], v[0:3]\n\t"
    "ds_read_b64_tr_b16 v[190:191], v50 offset:53512\n\t"
    "v_mfma_f32_16x16x32_bf16 v[0:3], a[200:203], v[152:155], v[0:3]\n\t"
    "v_mfma_f32_16x16x32_bf16 v[0:3], a[204:207], v[156:159], v[0:3]\n\t"
    "ds_read_b64_tr_b16 v[192:193], v50 offset:49664\n\t"
    "v_mfma_f32_16x16x32_bf16 v[4:7], a[208:211], v[144:147], v[40:43]\n\t"
    "v_mfma_f32_16x16x32_bf16 v[4:7], a[212:215], v[148:151], v[4:7]\n\t"
    "ds_read_b64_tr_b16 v[194:195], v50 offset:53760\n\t"
    "v_mfma_f32_16x16x32_bf16 v[4:7], a[216:219], v[152:155], v[4:7]\n\t"
    "v_mfma_f32_16x16x32_bf16 v[4:7], a[220:223], v[156:159], v[4:7]\n\t"
    "v_exp_f32 v0, v0\n\t"
    "v_mfma_f32_16x16x32_bf16 a[236:239], a[248:251], v[20:23], a[236:239]\n\t"
    "v_exp_f32 v1, v1\n\t"
    "v_mfma_f32_16x16x32_bf16 a[96:99], v[224:227], v[20:23], a[96:99]\n\t"
    "v_exp_f32 v2, v2\n\t"
    "v_mfma_f32_16x16x32_bf16 a[100:103], v[228:231], v[20:23], a[100:103]\n\t"
    "v_exp_f32 v3, v3\n\t"
    "v_mfma_f32_16x16x32_bf16 a[104:107], v[232:235], v[20:23], a[104:107]\n\t"
    "v_exp_f32 v4, v4\n\t"
    "v_mfma_f32_16x16x32_bf16 a[108:111], v[236:239], v[20:23], a[108:111]\n\t"
    "v_exp_f32 v5, v5\n\t"
    "v_mfma_f32_16x16x32_bf16 a[112:115], v[240:243], v[20:23], a[112:115]\n\t"
    "buffer_load_dwordx4 v[208:211], v54, s[28:31], s60 offen offset:0 nt\n\t"
    "v_cvt_pk_bf16_f32 v16, v0, v1\n\t"
    "v_mfma_f32_16x16x32_bf16 a[116:119], v[244:247], v[20:23], a[116:119]\n\t"
    "v_exp_f32 v6, v6\n\t"
    "v_mfma_f32_16x16x32_bf16 a[120:123], v[248:251], v[20:23], a[120:123]\n\t"
    "v_exp_f32 v7, v7\n\t"
    "v_mfma_f32_16x16x32_bf16 a[124:127], v[252:255], v[20:23], a[124:127]\n\t"
    "v_cvt_pk_bf16_f32 v17, v2, v3\n\t"
    "v_cvt_pk_bf16_f32 v18, v4, v5\n\t"
    "v_mfma_f32_16x16x32_bf16 v[8:11], a[192:195], v[160:163], v[44:47]\n\t"
    "ds_read_b128 a[192:195], v48 offset:49152\n\t"
    "v_cvt_pk_bf16_f32 v19, v6, v7\n\t"
    "v_mfma_f32_16x16x32_bf16 v[8:11], a[196:199], v[164:167], v[8:11]\n\t"
    "ds_read_b128 a[196:199], v48 offset:49408\n\t"
    "v_mfma_f32_16x16x32_bf16 v[8:11], a[200:203], v[168:171], v[8:11]\n\t"
    "ds_read_b128 a[200:203], v48 offset:49664\n\t"
    "v_mfma_f32_16x16x32_bf16 v[8:11], a[204:207], v[172:175], v[8:11]\n\t"
    "ds_read_b128 a[204:207], v48 offset:49920\n\t"
    "v_mfma_f32_16x16x32_bf16 v[12:15], a[208:211], v[160:163], v[44:47]\n\t"
    "ds_read_b128 a[208:211], v48 offset:53248\n\t"
    "v_mfma_f32_16x16x32_bf16 v[12:15], a[212:215], v[164:167], v[12:15]\n\t"
    "ds_read_b128 a[212:215], v48 offset:53504\n\t"
    "v_mfma_f32_16x16x32_bf16 v[12:15], a[216:219], v[168:171], v[12:15]\n\t"
    "ds_read_b128 a[216:219], v48 offset:53760\n\t"
    "v_mfma_f32_16x16x32_bf16 v[12:15], a[220:223], v[172:175], v[12:15]\n\t"
    "v_exp_f32 v8, v8\n\t"
    "v_mfma_f32_16x16x32_bf16 a[240:243], a[248:251], v[16:19], a[240:243]\n\t"
    "v_exp_f32 v9, v9\n\t"
    "v_mfma_f32_16x16x32_bf16 a[128:131], v[224:227], v[16:19], a[128:131]\n\t"
    "ds_read_b128 a[220:223], v48 offset:54016\n\t"
    "v_cvt_pk_bf16_f32 v20, v8, v9\n\t"
    "v_mfma_f32_16x16x32_bf16 a[132:135], v[228:231], v[16:19], a[132:135]\n\t"
    "v_exp_f32 v10, v10\n\t"
    "v_mfma_f32_16x16x32_bf16 a[136:139], v[232:235], v[16:19], a[136:139]\n\t"
    "v_exp_f32 v11, v11\n\t"
    "v_mfma_f32_16x16x32_bf16 a[140:143], v[236:239], v[16:19], a[140:143]\n\t"
    "v_exp_f32 v12, v12\n\t"
    "v_mfma_f32_16x16x32_bf16 a[144:147], v[240:243], v[16:19], a[144:147]\n\t"
    "v_exp_f32 v13, v13\n\t"
    "v_mfma_f32_16x16x32_bf16 a[148:151], v[244:247], v[16:19], a[148:151]\n\t"
    "v_exp_f32 v14, v14\n\t"
    "v_mfma_f32_16x16x32_bf16 a[152:155], v[248:251], v[16:19], a[152:155]\n\t"
    "v_exp_f32 v15, v15\n\t"
    "v_mfma_f32_16x16x32_bf16 a[156:159], v[252:255], v[16:19], a[156:159]\n\t"
    "s_waitcnt lgkmcnt(1)\n\t"
    "v_cvt_pk_bf16_f32 v21, v10, v11\n\t"
    "v_cvt_pk_bf16_f32 v22, v12, v13\n\t"
    "v_mfma_f32_16x16x32_bf16 v[0:3], a[192:195], v[80:83], v[24:27]\n\t"
    "ds_read_b64_tr_b16 v[196:197], v50 offset:49672\n\t"
    "v_cvt_pk_bf16_f32 v23, v14, v15\n\t"
    "v_mfma_f32_16x16x32_bf16 v[0:3], a[196:199], v[84:87], v[0:3]\n\t"
    "s_add_u32 m0, s51, 73728\n\t"
    "v_mfma_f32_16x16x32_bf16 v[0:3], a[200:203], v[88:91], v[0:3]\n\t"
    "buffer_load_dwordx4 v52, s[36:39], s73 offen offset:0 lds\n\t"
    "v_mfma_f32_16x16x32_bf16 v[0:3], a[204:207], v[92:95], v[0:3]\n\t"
    "ds_read_b64_tr_b16 v[198:199], v50 offset:53768\n\t"
    "v_mfma_f32_16x16x32_bf16 v[4:7], a[208:211], v[80:83], v[24:27]\n\t"
    "v_mfma_f32_16x16x32_bf16 v[4:7], a[212:215], v[84:87], v[4:7]\n\t"
    "ds_read_b64_tr_b16 v[200:201], v50 offset:49920\n\t"
    "v_mfma_f32_16x16x32_bf16 v[4:7], a[216:219], v[88:91], v[4:7]\n\t"
    "s_waitcnt lgkmcnt(3)\n\t"
    "v_mfma_f32_16x16x32_bf16 v[4:7], a[220:223], v[92:95], v[4:7]\n\t"
    "v_exp_f32 v0, v0\n\t"
    "v_mfma_f32_16x16x32_bf16 a[244:247], a[248:251], v[20:23], a[244:247]\n\t"
    "v_exp_f32 v1, v1\n\t"
    "v_mfma_f32_16x16x32_bf16 a[160:163], v[224:227], v[20:23], a[160:163]\n\t"
    "ds_read_b64_tr_b16 v[202:203], v50 offset:54016\n\t"
    "v_cvt_pk_bf16_f32 v16, v0, v1\n\t"
    "v_mfma_f32_16x16x32_bf16 a[164:167], v[228:231], v[20:23], a[164:167]\n\t"
    "v_exp_f32 v2, v2\n\t"
    "v_mfma_f32_16x16x32_bf16 a[168:171], v[232:235], v[20:23], a[168:171]\n\t"
    "v_exp_f32 v3, v3\n\t"
    "v_mfma_f32_16x16x32_bf16 a[172:175], v[236:239], v[20:23], a[172:175]\n\t"
    "v_exp_f32 v4, v4\n\t"
    "v_mfma_f32_16x16x32_bf16 a[176:179], v[240:243], v[20:23], a[176:179]\n\t"
    "v_exp_f32 v5, v5\n\t"
    "v_mfma_f32_16x16x32_bf16 a[180:183], v[244:247], v[20:23], a[180:183]\n\t"
    "v_exp_f32 v6, v6\n\t"
    "v_mfma_f32_16x16x32_bf16 a[184:187], v[248:251], v[20:23], a[184:187]\n\t"
    "v_exp_f32 v7, v7\n\t"
    "v_mfma_f32_16x16x32_bf16 a[188:191], v[252:255], v[20:23], a[188:191]\n\t"
    "v_cvt_pk_bf16_f32 v17, v2, v3\n\t"
    "v_cvt_pk_bf16_f32 v18, v4, v5\n\t"
    "v_mfma_f32_16x16x32_bf16 v[8:11], a[192:195], v[96:99], v[28:31]\n\t"
    "ds_read_b64_tr_b16 v[204:205], v50 offset:49928\n\t"
    "v_cvt_pk_bf16_f32 v19, v6, v7\n\t"
    "v_mfma_f32_16x16x32_bf16 v[8:11], a[196:199], v[100:103], v[8:11]\n\t"
    "v_mfma_f32_16x16x32_bf16 v[8:11], a[200:203], v[104:107], v[8:11]\n\t"
    "ds_read_b64_tr_b16 v[206:207], v50 offset:54024\n\t"
    "v_mfma_f32_16x16x32_bf16 v[8:11], a[204:207], v[108:111], v[8:11]\n\t"
    "v_mfma_f32_16x16x32_bf16 v[12:15], a[208:211], v[96:99], v[28:31]\n\t"
    "v_mfma_f32_16x16x32_bf16 v[12:15], a[212:215], v[100:103], v[12:15]\n\t"
    "buffer_load_dwordx4 v53, s[36:39], s73 offen offset:1024 lds\n\t"
    "v_mfma_f32_16x16x32_bf16 v[12:15], a[216:219], v[104:107], v[12:15]\n\t"
    "v_mfma_f32_16x16x32_bf16 v[12:15], a[220:223], v[108:111], v[12:15]\n\t"
    "v_exp_f32 v8, v8\n\t"
    "v_mfma_f32_16x16x32_bf16 a[224:227], a[248:251], v[16:19], a[224:227]\n\t"
    "v_exp_f32 v9, v9\n\t"
    "v_mfma_f32_16x16x32_bf16 a[0:3], v[176:179], v[16:19], a[0:3]\n\t"
    "s_add_u32 s16, s16, 2048\n\ts_addc_u32 s17, s17, 0\n\t"
    "v_exp_f32 v10, v10\n\t"
    "v_mfma_f32_16x16x32_bf16 a[4:7], v[180:183], v[16:19], a[4:7]\n\t"
    "v_exp_f32 v11, v11\n\t"
    "v_mfma_f32_16x16x32_bf16 a[8:11], v[184:187], v[16:19], a[8:11]\n\t"
    "global_load_dword a255, v55, s[16:17]\n\t"
    "v_cvt_pk_bf16_f32 v20, v8, v9\n\t"
    "v_mfma_f32_16x16x32_bf16 a[12:15], v[188:191], v[16:19], a[12:15]\n\t"
    "v_exp_f32 v12, v12\n\t"
    "v_mfma_f32_16x16x32_bf16 a[16:19], v[192:195], v[16:19], a[16:19]\n\t"
    "s_waitcnt lgkmcnt(0)\n\t"
    "v_exp_f32 v13, v13\n\t"
    "v_mfma_f32_16x16x32_bf16 a[20:23], v[196:199], v[16:19], a[20:23]\n\t"
    "v_exp_f32 v14, v14\n\t"
    "v_mfma_f32_16x16x32_bf16 a[24:27], v[200:203], v[16:19], a[24:27]\n\t"
    "v_exp_f32 v15, v15\n\t"
    "v_mfma_f32_16x16x32_bf16 a[28:31], v[204:207], v[16:19], a[28:31]\n\t"
    "s_waitcnt vmcnt(9)\n\t"
    "v_cvt_pk_bf16_f32 v21, v10, v11\n\t"
    "v_cvt_pk_bf16_f32 v22, v12, v13\n\t"
    "v_mfma_f32_16x16x32_bf16 v[0:3], a[192:195], v[112:115], v[32:35]\n\t"
    "s_barrier\n\t"
    "ds_read_b64_tr_b16 v[224:225], v50 offset:57344\n\t"
    "v_cvt_pk_bf16_f32 v23, v14, v15\n\t"
    "v_mfma_f32_16x16x32_bf16 v[0:3], a[196:199], v[116:119], v[0:3]\n\t"
    "v_mfma_f32_16x16x32_bf16 v[0:3], a[200:203], v[120:123], v[0:3]\n\t"
    "ds_read_b64_tr_b16 v[226:227], v50 offset:61440\n\t"
    "v_mfma_f32_16x16x32_bf16 v[0:3], a[204:207], v[124:127], v[0:3]\n\t"
    "v_mfma_f32_16x16x32_bf16 v[4:7], a[208:211], v[112:115], v[32:35]\n\t"
    "ds_read_b64_tr_b16 v[228:229], v50 offset:57352\n\t"
    "v_mfma_f32_16x16x32_bf16 v[4:7], a[212:215], v[116:119], v[4:7]\n\t"
    "v_mfma_f32_16x16x32_bf16 v[4:7], a[216:219], v[120:123], v[4:7]\n\t"
    "ds_read_b64_tr_b16 v[230:231], v50 offset:61448\n\t"
    "v_mfma_f32_16x16x32_bf16 v[4:7], a[220:223], v[124:127], v[4:7]\n\t"
    "s_add_u32 m0, s51, 155648\n\t"
    "v_exp_f32 v0, v0\n\t"
    "v_mfma_f32_16x16x32_bf16 a[228:231], a[248:251], v[20:23], a[228:231]\n\t"
    "buffer_load_dwordx4 v52, s[40:43], s73 offen offset:0 lds\n\t"
    "v_mfma_f32_16x16x32_bf16 a[32:35], v[176:179], v[20:23], a[32:35]\n\t"
    "v_exp_f32 v1, v1\n\t"
    "v_mfma_f32_16x16x32_bf16 a[36:39], v[180:183], v[20:23], a[36:39]\n\t"
    "v_exp_f32 v2, v2\n\t"
    "v_mfma_f32_16x16x32_bf16 a[40:43], v[184:187], v[20:23], a[40:43]\n\t"
    "v_exp_f32 v3, v3\n\t"
    "v_mfma_f32_16x16x32_bf16 a[44:47], v[188:191], v[20:23], a[44:47]\n\t"
    "v_exp_f32 v4, v4\n\t"
    "v_mfma_f32_16x16x32_bf16 a[48:51], v[192:195], v[20:23], a[48:51]\n\t"
    "v_exp_f32 v5, v5\n\t"
    "v_mfma_f32_16x16x32_bf16 a[52:55], v[196:199], v[20:23], a[52:55]\n\t"
    "v_exp_f32 v6, v6\n\t"
    "v_mfma_f32_16x16x32_bf16 a[56:59], v[200:203], v[20:23], a[56:59]\n\t"
    "v_exp_f32 v7, v7\n\t"
    "v_mfma_f32_16x16x32_bf16 a[60:63], v[204:207], v[20:23], a[60:63]\n\t"
    "v_cvt_pk_bf16_f32 v16, v0, v1\n\t"
    "v_cvt_pk_bf16_f32 v17, v2, v3\n\t"
    "v_mfma_f32_16x16x32_bf16 v[8:11], a[192:195], v[128:131], v[36:39]\n\t"
    "v_cvt_pk_bf16_f32 v18, v4, v5\n\t"
    "v_cvt_pk_bf16_f32 v19, v6, v7\n\t"
    "v_mfma_f32_16x16x32_bf16 v[8:11], a[196:199], v[132:135], v[8:11]\n\t"
    "ds_read_b64_tr_b16 v[232:233], v50 offset:57600\n\t"
    "v_mfma_f32_16x16x32_bf16 v[8:11], a[200:203], v[136:139], v[8:11]\n\t"
    "v_mfma_f32_16x16x32_bf16 v[8:11], a[204:207], v[140:143], v[8:11]\n\t"
    "ds_read_b64_tr_b16 v[234:235], v50 offset:61696\n\t"
    "v_mfma_f32_16x16x32_bf16 v[12:15], a[208:211], v[128:131], v[36:39]\n\t"
    "v_mfma_f32_16x16x32_bf16 v[12:15], a[212:215], v[132:135], v[12:15]\n\t"
    "ds_read_b64_tr_b16 v[236:237], v50 offset:57608\n\t"
    "v_mfma_f32_16x16x32_bf16 v[12:15], a[216:219], v[136:139], v[12:15]\n\t"
    "v_mfma_f32_16x16x32_bf16 v[12:15], a[220:223], v[140:143], v[12:15]\n\t"
    "v_exp_f32 v8, v8\n\t"
    "v_mfma_f32_16x16x32_bf16 a[232:235], a[248:251], v[16:19], a[232:235]\n\t"
    "v_exp_f32 v9, v9\n\t"
    "v_mfma_f32_16x16x32_bf16 a[64:67], v[176:179], v[16:19], a[64:67]\n\t"
    "v_exp_f32 v10, v10\n\t"
    "v_mfma_f32_16x16x32_bf16 a[68:71], v[180:183], v[16:19], a[68:71]\n\t"
    "v_exp_f32 v11, v11\n\t"
    "v_mfma_f32_16x16x32_bf16 a[72:75], v[184:187], v[16:19], a[72:75]\n\t"
    "buffer_load_dwordx4 v53, s[40:43], s73 offen offset:1024 lds\n\t"
    "v_mfma_f32_16x16x32_bf16 a[76:79], v[188:191], v[16:19], a[76:79]\n\t"
    "v_exp_f32 v12, v12\n\t"
    "v_mfma_f32_16x16x32_bf16 a[80:83], v[192:195], v[16:19], a[80:83]\n\t"
    "v_exp_f32 v13, v13\n\t"
    "v_mfma_f32_16x16x32_bf16 a[84:87], v[196:199], v[16:19], a[84:87]\n\t"
    "v_exp_f32 v14, v14\n\t"
    "v_mfma_f32_16x16x32_bf16 a[88:91], v[200:203], v[16:19], a[88:91]\n\t"
    "v_exp_f32 v15, v15\n\t"
    "v_mfma_f32_16x16x32_bf16 a[92:95], v[204:207], v[16:19], a[92:95]\n\t"
    "v_cvt_pk_bf16_f32 v20, v8, v9\n\t"
    "v_cvt_pk_bf16_f32 v21, v10, v11\n\t"
    "v_mfma_f32_16x16x32_bf16 v[0:3], a[192:195], v[144:147], v[40:43]\n\t"
    "v_cvt_pk_bf16_f32 v22, v12, v13\n\t"
    "v_cvt_pk_bf16_f32 v23, v14, v15\n\t"
    "v_mfma_f32_16x16x32_bf16 v[0:3], a[196:199], v[148:151], v[0:3]\n\t"
    "ds_read_b64_tr_b16 v[238:239], v50 offset:61704\n\t"
    "v_mfma_f32_16x16x32_bf16 v[0:3], a[200:203], v[152:155], v[0:3]\n\t"
    "v_mfma_f32_16x16x32_bf16 v[0:3], a[204:207], v[156:159], v[0:3]\n\t"
    "ds_read_b64_tr_b16 v[240:241], v50 offset:57856\n\t"
    "v_mfma_f32_16x16x32_bf16 v[4:7], a[208:211], v[144:147], v[40:43]\n\t"
    "v_mfma_f32_16x16x32_bf16 v[4:7], a[212:215], v[148:151], v[4:7]\n\t"
    "ds_read_b64_tr_b16 v[242:243], v50 offset:61952\n\t"
    "v_mfma_f32_16x16x32_bf16 v[4:7], a[216:219], v[152:155], v[4:7]\n\t"
    "v_mfma_f32_16x16x32_bf16 v[4:7], a[220:223], v[156:159], v[4:7]\n\t"
    "v_exp_f32 v0, v0\n\t"
    "v_mfma_f32_16x16x32_bf16 a[236:239], a[248:251], v[20:23], a[236:239]\n\t"
    "v_exp_f32 v1, v1\n\t"
    "v_mfma_f32_16x16x32_bf16 a[96:99], v[176:179], v[20:23], a[96:99]\n\t"
    "v_exp_f32 v2, v2\n\t"
    "v_mfma_f32_16x16x32_bf16 a[100:103], v[180:183], v[20:23], a[100:103]\n\t"
    "v_exp_f32 v3, v3\n\t"
    "v_mfma_f32_16x16x32_bf16 a[104:107], v[184:187], v[20:23], a[104:107]\n\t"
    "v_exp_f32 v4, v4\n\t"
    "v_mfma_f32_16x16x32_bf16 a[108:111], v[188:191], v[20:23], a[108:111]\n\t"
    "v_exp_f32 v5, v5\n\t"
    "v_mfma_f32_16x16x32_bf16 a[112:115], v[192:195], v[20:23], a[112:115]\n\t"
    "v_exp_f32 v6, v6\n\t"
    "v_mfma_f32_16x16x32_bf16 a[116:119], v[196:199], v[20:23], a[116:119]\n\t"
    "buffer_load_dwordx4 v[212:215], v54, s[28:31], s60 offen offset:64 nt\n\t"
    "v_cvt_pk_bf16_f32 v16, v0, v1\n\t"
    "v_mfma_f32_16x16x32_bf16 a[120:123], v[200:203], v[20:23], a[120:123]\n\t"
    "v_exp_f32 v7, v7\n\t"
    "v_mfma_f32_16x16x32_bf16 a[124:127], v[204:207], v[20:23], a[124:127]\n\t"
    "v_cvt_pk_bf16_f32 v17, v2, v3\n\t"
    "v_cvt_pk_bf16_f32 v18, v4, v5\n\t"
    "v_mfma_f32_16x16x32_bf16 v[8:11], a[192:195], v[160:163], v[44:47]\n\t"
    "ds_read_b128 a[192:195], v48 offset:57344\n\t"
    "v_cvt_pk_bf16_f32 v19, v6, v7\n\t"
    "v_mfma_f32_16x16x32_bf16 v[8:11], a[196:199], v[164:167], v[8:11]\n\t"
    "ds_read_b128 a[196:199], v48 offset:57600\n\t"
    "v_mfma_f32_16x16x32_bf16 v[8:11], a[200:203], v[168:171], v[8:11]\n\t"
    "ds_read_b128 a[200:203], v48 offset:57856\n\t"
    "v_mfma_f32_16x16x32_bf16 v[8:11], a[204:207], v[172:175], v[8:11]\n\t"
    "ds_read_b128 a[204:207], v48 offset:58112\n\t"
    "v_mfma_f32_16x16x32_bf16 v[12:15], a[208:211], v[160:163], v[44:47]\n\t"
    "ds_read_b128 a[208:211], v48 offset:61440\n\t"
    "v_mfma_f32_16x16x32_bf16 v[12:15], a[212:215], v[164:167], v[12:15]\n\t"
    "ds_read_b128 a[212:215], v48 offset:61696\n\t"
    "v_mfma_f32_16x16x32_bf16 v[12:15], a[216:219], v[168:171], v[12:15]\n\t"
    "ds_read_b128 a[216:219], v48 offset:61952\n\t"
    "v_mfma_f32_16x16x32_bf16 v[12:15], a[220:223], v[172:175], v[12:15]\n\t"
    "v_exp_f32 v8, v8\n\t"
    "v_mfma_f32_16x16x32_bf16 a[240:243], a[248:251], v[16:19], a[240:243]\n\t"
    "v_exp_f32 v9, v9\n\t"
    "v_mfma_f32_16x16x32_bf16 a[128:131], v[176:179], v[16:19], a[128:131]\n\t"
    "ds_read_b128 a[220:223], v48 offset:62208\n\t"
    "v_cvt_pk_bf16_f32 v20, v8, v9\n\t"
    "v_mfma_f32_16x16x32_bf16 a[132:135], v[180:183], v[16:19], a[132:135]\n\t"
    "v_exp_f32 v10, v10\n\t"
    "v_mfma_f32_16x16x32_bf16 a[136:139], v[184:187], v[16:19], a[136:139]\n\t"
    "v_exp_f32 v11, v11\n\t"
    "v_mfma_f32_16x16x32_bf16 a[140:143], v[188:191], v[16:19], a[140:143]\n\t"
    "v_exp_f32 v12, v12\n\t"
    "v_mfma_f32_16x16x32_bf16 a[144:147], v[192:195], v[16:19], a[144:147]\n\t"
    "v_exp_f32 v13, v13\n\t"
    "v_mfma_f32_16x16x32_bf16 a[148:151], v[196:199], v[16:19], a[148:151]\n\t"
    "v_exp_f32 v14, v14\n\t"
    "v_mfma_f32_16x16x32_bf16 a[152:155], v[200:203], v[16:19], a[152:155]\n\t"
    "v_exp_f32 v15, v15\n\t"
    "v_mfma_f32_16x16x32_bf16 a[156:159], v[204:207], v[16:19], a[156:159]\n\t"
    "s_waitcnt lgkmcnt(1)\n\t"
    "v_cvt_pk_bf16_f32 v21, v10, v11\n\t"
    "v_cvt_pk_bf16_f32 v22, v12, v13\n\t"
    "v_mfma_f32_16x16x32_bf16 v[0:3], a[192:195], v[80:83], v[24:27]\n\t"
    "ds_read_b64_tr_b16 v[244:245], v50 offset:57864\n\t"
    "v_cvt_pk_bf16_f32 v23, v14, v15\n\t"
    "v_mfma_f32_16x16x32_bf16 v[0:3], a[196:199], v[84:87], v[0:3]\n\t"
    "v_mfma_f32_16x16x32_bf16 v[0:3], a[200:203], v[88:91], v[0:3]\n\t"
    "s_add_u32 m0, s51, 40960\n\t"
    "ds_read_b64_tr_b16 v[246:247], v50 offset:61960\n\t"
    "v_mfma_f32_16x16x32_bf16 v[0:3], a[204:207], v[92:95], v[0:3]\n\t"
    "buffer_load_dwordx4 v52, s[36:39], s74 offen offset:0 lds\n\t"
    "v_mfma_f32_16x16x32_bf16 v[4:7], a[208:211], v[80:83], v[24:27]\n\t"
    "ds_read_b64_tr_b16 v[248:249], v50 offset:58112\n\t"
    "v_mfma_f32_16x16x32_bf16 v[4:7], a[212:215], v[84:87], v[4:7]\n\t"
    "v_mfma_f32_16x16x32_bf16 v[4:7], a[216:219], v[88:91], v[4:7]\n\t"
    "s_waitcnt lgkmcnt(3)\n\t"
    "ds_read_b64_tr_b16 v[250:251], v50 offset:62208\n\t"
    "v_mfma_f32_16x16x32_bf16 v[4:7], a[220:223], v[92:95], v[4:7]\n\t"
    "v_exp_f32 v0, v0\n\t"
    "v_mfma_f32_16x16x32_bf16 a[244:247], a[248:251], v[20:23], a[244:247]\n\t"
    "v_exp_f32 v1, v1\n\t"
    "v_mfma_f32_16x16x32_bf16 a[160:163], v[176:179], v[20:23], a[160:163]\n\t"
    "v_exp_f32 v2, v2\n\t"
    "v_mfma_f32_16x16x32_bf16 a[164:167], v[180:183], v[20:23], a[164:167]\n\t"
    "v_exp_f32 v3, v3\n\t"
    "v_mfma_f32_16x16x32_bf16 a[168:171], v[184:187], v[20:23], a[168:171]\n\t"
    "v_exp_f32 v4, v4\n\t"
    "v_mfma_f32_16x16x32_bf16 a[172:175], v[188:191], v[20:23], a[172:175]\n\t"
    "v_exp_f32 v5, v5\n\t"
    "v_mfma_f32_16x16x32_bf16 a[176:179], v[192:195], v[20:23], a[176:179]\n\t"
    "v_exp_f32 v6, v6\n\t"
    "v_mfma_f32_16x16x32_bf16 a[180:183], v[196:199], v[20:23], a[180:183]\n\t"
    "v_exp_f32 v7, v7\n\t"
    "v_mfma_f32_16x16x32_bf16 a[184:187], v[200:203], v[20:23], a[184:187]\n\t"
    "v_cvt_pk_bf16_f32 v16, v0, v1\n\t"
    "v_cvt_pk_bf16_f32 v17, v2, v3\n\t"
    "v_mfma_f32_16x16x32_bf16 a[188:191], v[204:207], v[20:23], a[188:191]\n\t"
    "v_cvt_pk_bf16_f32 v18, v4, v5\n\t"
    "v_cvt_pk_bf16_f32 v19, v6, v7\n\t"
    "v_mfma_f32_16x16x32_bf16 v[8:11], a[192:195], v[96:99], v[28:31]\n\t"
    "ds_read_b64_tr_b16 v[252:253], v50 offset:58120\n\t"
    "v_mfma_f32_16x16x32_bf16 v[8:11], a[196:199], v[100:103], v[8:11]\n\t"
    "v_mfma_f32_16x16x32_bf16 v[8:11], a[200:203], v[104:107], v[8:11]\n\t"
    "ds_read_b64_tr_b16 v[254:255], v50 offset:62216\n\t"
    "v_mfma_f32_16x16x32_bf16 v[8:11], a[204:207], v[108:111], v[8:11]\n\t"
    "v_mfma_f32_16x16x32_bf16 v[12:15], a[208:211], v[96:99], v[28:31]\n\t"
    "v_mfma_f32_16x16x32_bf16 v[12:15], a[212:215], v[100:103], v[12:15]\n\t"
    "v_mfma_f32_16x16x32_bf16 v[12:15], a[216:219], v[104:107], v[12:15]\n\t"
    "buffer_load_dwordx4 v53, s[36:39], s74 offen offset:1024 lds\n\t"
    "v_mfma_f32_16x16x32_bf16 v[12:15], a[220:223], v[108:111], v[12:15]\n\t"
    "v_exp_f32 v8, v8\n\t"
    "v_mfma_f32_16x16x32_bf16 a[224:227], a[248:251], v[16:19], a[224:227]\n\t"
    "v_exp_f32 v9, v9\n\t"
    "v_mfma_f32_16x16x32_bf16 a[0:3], v[224:227], v[16:19], a[0:3]\n\t"
    "v_exp_f32 v10, v10\n\t"
    "v_mfma_f32_16x16x32_bf16 a[4:7], v[228:231], v[16:19], a[4:7]\n\t"
    "v_exp_f32 v11, v11\n\t"
    "v_mfma_f32_16x16x32_bf16 a[8:11], v[232:235], v[16:19], a[8:11]\n\t"
    "v_exp_f32 v12, v12\n\t"
    "v_mfma_f32_16x16x32_bf16 a[12:15], v[236:239], v[16:19], a[12:15]\n\t"
    "v_exp_f32 v13, v13\n\t"
    "v_mfma_f32_16x16x32_bf16 a[16:19], v[240:243], v[16:19], a[16:19]\n\t"
    "s_waitcnt lgkmcnt(0)\n\t"
    "v_exp_f32 v14, v14\n\t"
    "v_mfma_f32_16x16x32_bf16 a[20:23], v[244:247], v[16:19], a[20:23]\n\t"
    "v_exp_f32 v15, v15\n\t"
    "v_mfma_f32_16x16x32_bf16 a[24:27], v[248:251], v[16:19], a[24:27]\n\t"
    "v_cvt_pk_bf16_f32 v20, v8, v9\n\t"
    "v_cvt_pk_bf16_f32 v21, v10, v11\n\t"
    "v_mfma_f32_16x16x32_bf16 a[28:31], v[252:255], v[16:19], a[28:31]\n\t"
    "s_waitcnt vmcnt(9)\n\t"
    "v_cvt_pk_bf16_f32 v22, v12, v13\n\t"
    "v_cvt_pk_bf16_f32 v23, v14, v15\n\t"
    "v_mfma_f32_16x16x32_bf16 v[0:3], a[192:195], v[112:115], v[32:35]\n\t"
    "s_barrier\n\t"
    "ds_read_b64_tr_b16 v[176:177], v51 offset:0\n\t"
    "v_mfma_f32_16x16x32_bf16 v[0:3], a[196:199], v[116:119], v[0:3]\n\t"
    "v_mfma_f32_16x16x32_bf16 v[0:3], a[200:203], v[120:123], v[0:3]\n\t"
    "s_add_u32 s16, s16, 2048\n\ts_addc_u32 s17, s17, 0\n\t"
    "ds_read_b64_tr_b16 v[178:179], v51 offset:4096\n\t"
    "v_mfma_f32_16x16x32_bf16 v[0:3], a[204:207], v[124:127], v[0:3]\n\t"
    "v_mfma_f32_16x16x32_bf16 v[4:7], a[208:211], v[112:115], v[32:35]\n\t"
    "ds_read_b64_tr_b16 v[180:181], v51 offset:8\n\t"
    "global_load_dword a255, v55, s[16:17]\n\t"
    "v_mfma_f32_16x16x32_bf16 v[4:7], a[212:215], v[116:119], v[4:7]\n\t"
    "v_mfma_f32_16x16x32_bf16 v[4:7], a[216:219], v[120:123], v[4:7]\n\t"
    "ds_read_b64_tr_b16 v[182:183], v51 offset:4104\n\t"
    "v_mfma_f32_16x16x32_bf16 v[4:7], a[220:223], v[124:127], v[4:7]\n\t"
    "v_exp_f32 v0, v0\n\t"
    "v_mfma_f32_16x16x32_bf16 a[228:231], a[248:251], v[20:23], a[228:231]\n\t"
    "s_add_u32 m0, s51, 122880\n\t"
    "v_exp_f32 v1, v1\n\t"
    "v_mfma_f32_16x16x32_bf16 a[32:35], v[224:227], v[20:23], a[32:35]\n\t"
    "buffer_load_dwordx4 v52, s[40:43], s74 offen offset:0 lds\n\t"
    "v_mfma_f32_16x16x32_bf16 a[36:39], v[228:231], v[20:23], a[36:39]\n\t"
    "v_exp_f32 v2, v2\n\t"
    "v_mfma_f32_16x16x32_bf16 a[40:43], v[232:235], v[20:23], a[40:43]\n\t"
    "v_exp_f32 v3, v3\n\t"
    "v_mfma_f32_16x16x32_bf16 a[44:47], v[236:239], v[20:23], a[44:47]\n\t"
    "v_exp_f32 v4, v4\n\t"
    "v_mfma_f32_16x16x32_bf16 a[48:51], v[240:243], v[20:23], a[48:51]\n\t"
    "v_exp_f32 v5, v5\n\t"
    "v_mfma_f32_16x16x32_bf16 a[52:55], v[244:247], v[20:23], a[52:55]\n\t"
    "v_exp_f32 v6, v6\n\t"
    "v_mfma_f32_16x16x32_bf16 a[56:59], v[248:251], v[20:23], a[56:59]\n\t"
    "v_exp_f32 v7, v7\n\t"
    "v_mfma_f32_16x16x32_bf16 a[60:63], v[252:255], v[20:23], a[60:63]\n\t"
    "v_cvt_pk_bf16_f32 v16, v0, v1\n\t"
    "v_cvt_pk_bf16_f32 v17, v2, v3\n\t"
    "v_mfma_f32_16x16x32_bf16 v[8:11], a[192:195], v[128:131], v[36:39]\n\t"
    "v_cvt_pk_bf16_f32 v18, v4, v5\n\t"
    "v_cvt_pk_bf16_f32 v19, v6, v7\n\t"
    "v_mfma_f32_16x16x32_bf16 v[8:11], a[196:199], v[132:135], v[8:11]\n\t"
    "ds_read_b64_tr_b16 v[184:185], v51 offset:256\n\t"
    "v_mfma_f32_16x16x32_bf16 v[8:11], a[200:203], v[136:139], v[8:11]\n\t"
    "v_mfma_f32_16x16x32_bf16 v[8:11], a[204:207], v[140:143], v[8:11]\n\t"
    "ds_read_b64_tr_b16 v[186:187], v51 offset:4352\n\t"
    "v_mfma_f32_16x16x32_bf16 v[12:15], a[208:211], v[128:131], v[36:39]\n\t"
    "v_mfma_f32_16x16x32_bf16 v[12:15], a[212:215], v[132:135], v[12:15]\n\t"
    "ds_read_b64_tr_b16 v[188:189], v51 offset:264\n\t"
    "v_mfma_f32_16x16x32_bf16 v[12:15], a[216:219], v[136:139], v[12:15]\n\t"
    "s_waitcnt vmcnt(10)\n\t"
    "v_lshlrev_b32 v64, 16, v208\n\t"
    "v_and_b32 v65, s49, v208\n\t"
    "v_mfma_f32_16x16x32_bf16 v[12:15], a[220:223], v[140:143], v[12:15]\n\t"
    "v_exp_f32 v8, v8\n\t"
    "v_mfma_f32_16x16x32_bf16 a[232:235], a[248:251], v[16:19], a[232:235]\n\t"
    "v_exp_f32 v9, v9\n\t"
    "v_mfma_f32_16x16x32_bf16 a[64:67], v[224:227], v[16:19], a[64:67]\n\t"
    "v_exp_f32 v10, v10\n\t"
    "v_mfma_f32_16x16x32_bf16 a[68:71], v[228:231], v[16:19], a[68:71]\n\t"
    "v_exp_f32 v11, v11\n\t"
    "v_mfma_f32_16x16x32_bf16 a[72:75], v[232:235], v[16:19], a[72:75]\n\t"
    "v_exp_f32 v12, v12\n\t"
    "v_mfma_f32_16x16x32_bf16 a[76:79], v[236:239], v[16:19], a[76:79]\n\t"
    "buffer_load_dwordx4 v53, s[40:43], s74 offen offset:1024 lds\n\t"
    "v_mfma_f32_16x16x32_bf16 a[80:83], v[240:243], v[16:19], a[80:83]\n\t"
    "v_exp_f32 v13, v13\n\t"
    "v_mfma_f32_16x16x32_bf16 a[84:87], v[244:247], v[16:19], a[84:87]\n\t"
    "v_exp_f32 v14, v14\n\t"
    "v_mfma_f32_16x16x32_bf16 a[88:91], v[248:251], v[16:19], a[88:91]\n\t"
    "v_exp_f32 v15, v15\n\t"
    "v_mfma_f32_16x16x32_bf16 a[92:95], v[252:255], v[16:19], a[92:95]\n\t"
    "v_cvt_pk_bf16_f32 v20, v8, v9\n\t"
    "v_cvt_pk_bf16_f32 v21, v10, v11\n\t"
    "v_mfma_f32_16x16x32_bf16 v[0:3], a[192:195], v[144:147], v[40:43]\n\t"
    "v_cvt_pk_bf16_f32 v22, v12, v13\n\t"
    "v_cvt_pk_bf16_f32 v23, v14, v15\n\t"
    "v_mfma_f32_16x16x32_bf16 v[0:3], a[196:199], v[148:151], v[0:3]\n\t"
    "v_mul_f32 v64, s48, v64\n\t"
    "v_mul_f32 v65, s48, v65\n\t"
    "v_mfma_f32_16x16x32_bf16 v[0:3], a[200:203], v[152:155], v[0:3]\n\t"
    "v_cvt_pk_bf16_f32 v208, v64, v65\n\t"
    "v_lshlrev_b32 v66, 16, v209\n\t"
    "v_mfma_f32_16x16x32_bf16 v[0:3], a[204:207], v[156:159], v[0:3]\n\t"
    "v_and_b32 v67, s49, v209\n\t"
    "v_mul_f32 v66, s48, v66\n\t"
    "v_mfma_f32_16x16x32_bf16 v[4:7], a[208:211], v[144:147], v[40:43]\n\t"
    "ds_read_b64_tr_b16 v[190:191], v51 offset:4360\n\t"
    "v_mul_f32 v67, s48, v67\n\t"
    "v_mfma_f32_16x16x32_bf16 v[4:7], a[212:215], v[148:151], v[4:7]\n\t"
    "v_cvt_pk_bf16_f32 v209, v66, v67\n\t"
    "v_lshlrev_b32 v68, 16, v210\n\t"
    "v_mfma_f32_16x16x32_bf16 v[4:7], a[216:219], v[152:155], v[4:7]\n\t"
    "ds_read_b64_tr_b16 v[192:193], v51 offset:512\n\t"
    "v_and_b32 v69, s49, v210\n\t"
    "v_mfma_f32_16x16x32_bf16 v[4:7], a[220:223], v[156:159], v[4:7]\n\t"
    "v_exp_f32 v0, v0\n\t"
    "v_mfma_f32_16x16x32_bf16 a[236:239], a[248:251], v[20:23], a[236:239]\n\t"
    "v_exp_f32 v1, v1\n\t"
    "v_mfma_f32_16x16x32_bf16 a[96:99], v[224:227], v[20:23], a[96:99]\n\t"
    "v_exp_f32 v2, v2\n\t"
    "v_mfma_f32_16x16x32_bf16 a[100:103], v[228:231], v[20:23], a[100:103]\n\t"
    "v_exp_f32 v3, v3\n\t"
    "v_mfma_f32_16x16x32_bf16 a[104:107], v[232:235], v[20:23], a[104:107]\n\t"
    "v_exp_f32 v4, v4\n\t"
    "v_mfma_f32_16x16x32_bf16 a[108:111], v[236:239], v[20:23], a[108:111]\n\t"
    "v_exp_f32 v5, v5\n\t"
    "v_mfma_f32_16x16x32_bf16 a[112:115], v[240:243], v[20:23], a[112:115]\n\t"
    "v_exp_f32 v6, v6\n\t"
    "v_mfma_f32_16x16x32_bf16 a[116:119], v[244:247], v[20:23], a[116:119]\n\t"
    "v_exp_f32 v7, v7\n\t"
    "v_mfma_f32_16x16x32_bf16 a[120:123], v[248:251], v[20:23], a[120:123]\n\t"
    "PAD3_%=:\n\ts_cbranch_vccnz PAD7_%=\n\t"
    "buffer_load_dwordx4 v[216:219], v54, s[28:31], s60 offen offset:128 nt\n\t"
    "v_cvt_pk_bf16_f32 v16, v0, v1\n\t"
    "v_mfma_f32_16x16x32_bf16 a[124:127], v[252:255], v[20:23], a[124:127]\n\t"
    "v_cvt_pk_bf16_f32 v17, v2, v3\n\t"
    "v_cvt_pk_bf16_f32 v18, v4, v5\n\t"
    "v_mfma_f32_16x16x32_bf16 v[8:11], a[192:195], v[160:163], v[44:47]\n\t"
    "ds_read_b128 a[192:195], v49 offset:0\n\t"
    "v_cvt_pk_bf16_f32 v19, v6, v7\n\t"
    "v_mfma_f32_16x16x32_bf16 v[8:11], a[196:199], v[164:167], v[8:11]\n\t"
    "ds_read_b128 a[196:199], v49 offset:256\n\t"
    "v_mul_f32 v68, s48, v68\n\t"
    "v_mfma_f32_16x16x32_bf16 v[8:11], a[200:203], v[168:171], v[8:11]\n\t"
    "ds_read_b128 a[200:203], v49 offset:512\n\t"
    "v_mul_f32 v69, s48, v69\n\t"
    "v_mfma_f32_16x16x32_bf16 v[8:11], a[204:207], v[172:175], v[8:11]\n\t"
    "ds_read_b128 a[204:207], v49 offset:768\n\t"
    "v_cvt_pk_bf16_f32 v210, v68, v69\n\t"
    "v_mfma_f32_16x16x32_bf16 v[12:15], a[208:211], v[160:163], v[44:47]\n\t"
    "ds_read_b128 a[208:211], v49 offset:4096\n\t"
    "v_lshlrev_b32 v70, 16, v211\n\t"
    "v_mfma_f32_16x16x32_bf16 v[12:15], a[212:215], v[164:167], v[12:15]\n\t"
    "ds_read_b128 a[212:215], v49 offset:4352\n\t"
    "v_and_b32 v71, s49, v211\n\t"
    "v_mfma_f32_16x16x32_bf16 v[12:15], a[216:219], v[168:171], v[12:15]\n\t"
    "ds_read_b128 a[216:219], v49 offset:4608\n\t"
    "v_mul_f32 v70, s48, v70\n\t"
    "v_mfma_f32_16x16x32_bf16 v[12:15], a[220:223], v[172:175], v[12:15]\n\t"
    "ds_read_b128 a[220:223], v49 offset:4864\n\t"
    "v_mul_f32 v71, s48, v71\n\t"
    "v_mfma_f32_16x16x32_bf16 a[240:243], a[248:251], v[16:19], a[240:243]\n\t"
    "v_exp_f32 v8, v8\n\t"
    "v_mfma_f32_16x16x32_bf16 a[128:131], v[224:227], v[16:19], a[128:131]\n\t"
    "v_exp_f32 v9, v9\n\t"
    "v_mfma_f32_16x16x32_bf16 a[132:135], v[228:231], v[16:19], a[132:135]\n\t"
    "v_exp_f32 v10, v10\n\t"
    "v_mfma_f32_16x16x32_bf16 a[136:139], v[232:235], v[16:19], a[136:139]\n\t"
    "v_exp_f32 v11, v11\n\t"
    "v_mfma_f32_16x16x32_bf16 a[140:143], v[236:239], v[16:19], a[140:143]\n\t"
    "v_exp_f32 v12, v12\n\t"
    "v_mfma_f32_16x16x32_bf16 a[144:147], v[240:243], v[16:19], a[144:147]\n\t"
    "v_exp_f32 v13, v13\n\t"
    "v_mfma_f32_16x16x32_bf16 a[148:151], v[244:247], v[16:19], a[148:151]\n\t"
    "v_exp_f32 v14, v14\n\t"
    "v_mfma_f32_16x16x32_bf16 a[152:155], v[248:251], v[16:19], a[152:155]\n\t"
    "v_exp_f32 v15, v15\n\t"
    "v_mfma_f32_16x16x32_bf16 a[156:159], v[252:255], v[16:19], a[156:159]\n\t"
    "s_waitcnt lgkmcnt(0)\n\t"
    "v_cvt_pk_bf16_f32 v20, v8, v9\n\t"
    "v_cvt_pk_bf16_f32 v21, v10, v11\n\t"
    "v_mfma_f32_16x16x32_bf16 v[0:3], a[192:195], v[80:83], v[24:27]\n\t"
    "v_cvt_pk_bf16_f32 v22, v12, v13\n\t"
    "v_cvt_pk_bf16_f32 v23, v14, v15\n\t"
    "v_mfma_f32_16x16x32_bf16 v[0:3], a[196:199], v[84:87], v[0:3]\n\t"
    "ds_read_b64_tr_b16 v[194:195], v51 offset:4608\n\t"
    "v_cvt_pk_bf16_f32 v211, v70, v71\n\t"
    "v_mfma_f32_16x16x32_bf16 v[0:3], a[200:203], v[88:91], v[0:3]\n\t"
    "v_mfma_f32_16x16x32_bf16 v[0:3], a[204:207], v[92:95], v[0:3]\n\t"
    "s_add_u32 m0, s51, 49152\n\t"
    "ds_read_b64_tr_b16 v[196:197], v51 offset:520\n\t"
    "v_mfma_f32_16x16x32_bf16 v[4:7], a[208:211], v[80:83], v[24:27]\n\t"
    "buffer_load_dwordx4 v52, s[36:39], s75 offen offset:0 nt lds\n\t"
    "v_mfma_f32_16x16x32_bf16 v[4:7], a[212:215], v[84:87], v[4:7]\n\t"
    "ds_read_b64_tr_b16 v[198:199], v51 offset:4616\n\t"
    "v_mfma_f32_16x16x32_bf16 v[4:7], a[216:219], v[88:91], v[4:7]\n\t"
    "v_mfma_f32_16x16x32_bf16 v[4:7], a[220:223], v[92:95], v[4:7]\n\t"
    "v_exp_f32 v0, v0\n\t"
    "v_mfma_f32_16x16x32_bf16 a[244:247], a[248:251], v[20:23], a[244:247]\n\t"
    "v_exp_f32 v1, v1\n\t"
    "v_mfma_f32_16x16x32_bf16 a[160:163], v[224:227], v[20:23], a[160:163]\n\t"
    "ds_read_b64_tr_b16 v[200:201], v51 offset:768\n\t"
    "v_cvt_pk_bf16_f32 v16, v0, v1\n\t"
    "v_mfma_f32_16x16x32_bf16 a[164:167], v[228:231], v[20:23], a[164:167]\n\t"
    "v_exp_f32 v2, v2\n\t"
    "v_mfma_f32_16x16x32_bf16 a[168:171], v[232:235], v[20:23], a[168:171]\n\t"
    "v_exp_f32 v3, v3\n\t"
    "v_mfma_f32_16x16x32_bf16 a[172:175], v[236:239], v[20:23], a[172:175]\n\t"
    "ds_read_b64_tr_b16 v[202:203], v51 offset:4864\n\t"
    "v_cvt_pk_bf16_f32 v17, v2, v3\n\t"
    "v_mfma_f32_16x16x32_bf16 a[176:179], v[240:243], v[20:23], a[176:179]\n\t"
    "v_exp_f32 v4, v4\n\t"
    "v_mfma_f32_16x16x32_bf16 a[180:183], v[244:247], v[20:23], a[180:183]\n\t"
    "v_exp_f32 v5, v5\n\t"
    "v_mfma_f32_16x16x32_bf16 a[184:187], v[248:251], v[20:23], a[184:187]\n\t"
    "v_exp_f32 v6, v6\n\t"
    "v_mfma_f32_16x16x32_bf16 a[188:191], v[252:255], v[20:23], a[188:191]\n\t"
    "v_exp_f32 v7, v7\n\t"
    "v_mfma_f32_16x16x32_bf16 v[8:11], a[192:195], v[96:99], v[28:31]\n\t"
    "v_cvt_pk_bf16_f32 v18, v4, v5\n\t"
    "v_cvt_pk_bf16_f32 v19, v6, v7\n\t"
    "v_mfma_f32_16x16x32_bf16 v[8:11], a[196:199], v[100:103], v[8:11]\n\t"
    "ds_read_b64_tr_b16 v[204:205], v51 offset:776\n\t"
    "v_mfma_f32_16x16x32_bf16 v[8:11], a[200:203], v[104:107], v[8:11]\n\t"
    "v_mfma_f32_16x16x32_bf16 v[8:11], a[204:207], v[108:111], v[8:11]\n\t"
    "ds_read_b64_tr_b16 v[206:207], v51 offset:4872\n\t"
    "v_mfma_f32_16x16x32_bf16 v[12:15], a[208:211], v[96:99], v[28:31]\n\t"
    "v_mfma_f32_16x16x32_bf16 v[12:15], a[212:215], v[100:103], v[12:15]\n\t"
    "v_mfma_f32_16x16x32_bf16 v[12:15], a[216:219], v[104:107], v[12:15]\n\t"
    "v_mfma_f32_16x16x32_bf16 v[12:15], a[220:223], v[108:111], v[12:15]\n\t"
    "buffer_load_dwordx4 v53, s[36:39], s75 offen offset:1024 nt lds\n\t"
    "v_mfma_f32_16x16x32_bf16 a[224:227], a[248:251], v[16:19], a[224:227]\n\t"
    "v_exp_f32 v8, v8\n\t"
    "v_mfma_f32_16x16x32_bf16 a[0:3], v[176:179], v[16:19], a[0:3]\n\t"
    "v_exp_f32 v9, v9\n\t"
    "v_mfma_f32_16x16x32_bf16 a[4:7], v[180:183], v[16:19], a[4:7]\n\t"
    "v_exp_f32 v10, v10\n\t"
    "v_mfma_f32_16x16x32_bf16 a[8:11], v[184:187], v[16:19], a[8:11]\n\t"
    "v_exp_f32 v11, v11\n\t"
    "v_mfma_f32_16x16x32_bf16 a[12:15], v[188:191], v[16:19], a[12:15]\n\t"
    "s_waitcnt lgkmcnt(0)\n\t"
    "v_exp_f32 v12, v12\n\t"
    "v_mfma_f32_16x16x32_bf16 a[16:19], v[192:195], v[16:19], a[16:19]\n\t"
    "v_exp_f32 v13, v13\n\t"
    "v_mfma_f32_16x16x32_bf16 a[20:23], v[196:199], v[16:19], a[20:23]\n\t"
    "v_exp_f32 v14, v14\n\t"
    "v_mfma_f32_16x16x32_bf16 a[24:27], v[200:203], v[16:19], a[24:27]\n\t"
    "v_exp_f32 v15, v15\n\t"
    "v_mfma_f32_16x16x32_bf16 a[28:31], v[204:207], v[16:19], a[28:31]\n\t"
    "s_waitcnt vmcnt(9)\n\t"
    "v_cvt_pk_bf16_f32 v20, v8, v9\n\t"
    "v_cvt_pk_bf16_f32 v21, v10, v11\n\t"
    "v_mfma_f32_16x16x32_bf16 v[0:3], a[192:195], v[112:115], v[32:35]\n\t"
    "s_barrier\n\t"
    "v_cvt_pk_bf16_f32 v22, v12, v13\n\t"
    "v_cvt_pk_bf16_f32 v23, v14, v15\n\t"
    "v_mfma_f32_16x16x32_bf16 v[0:3], a[196:199], v[116:119], v[0:3]\n\t"
    "ds_read_b64_tr_b16 v[224:225], v51 offset:8192\n\t"
    "v_mfma_f32_16x16x32_bf16 v[0:3], a[200:203], v[120:123], v[0:3]\n\t"
    "v_mfma_f32_16x16x32_bf16 v[0:3], a[204:207], v[124:127], v[0:3]\n\t"
    "ds_read_b64_tr_b16 v[226:227], v51 offset:12288\n\t"
    "v_mfma_f32_16x16x32_bf16 v[4:7], a[208:211], v[112:115], v[32:35]\n\t"
    "v_mfma_f32_16x16x32_bf16 v[4:7], a[212:215], v[116:119], v[4:7]\n\t"
    "ds_read_b64_tr_b16 v[228:229], v51 offset:8200\n\t"
    "v_mfma_f32_16x16x32_bf16 v[4:7], a[216:219], v[120:123], v[4:7]\n\t"
    "v_mfma_f32_16x16x32_bf16 v[4:7], a[220:223], v[124:127], v[4:7]\n\t"
    "v_exp_f32 v0, v0\n\t"
    "v_mfma_f32_16x16x32_bf16 a[228:231], a[248:251], v[20:23], a[228:231]\n\t"
    "v_exp_f32 v1, v1\n\t"
    "v_mfma_f32_16x16x32_bf16 a[32:35], v[176:179], v[20:23], a[32:35]\n\t"
    "s_add_u32 m0, s51, 131072\n\t"
    "v_exp_f32 v2, v2\n\t"
    "v_mfma_f32_16x16x32_bf16 a[36:39], v[180:183], v[20:23], a[36:39]\n\t"
    "buffer_load_dwordx4 v52, s[40:43], s75 offen offset:0 nt lds\n\t"
    "v_mfma_f32_16x16x32_bf16 a[40:43], v[184:187], v[20:23], a[40:43]\n\t"
    "v_exp_f32 v3, v3\n\t"
    "v_mfma_f32_16x16x32_bf16 a[44:47], v[188:191], v[20:23], a[44:47]\n\t"
    "s_add_u32 s16, s16, 2048\n\ts_addc_u32 s17, s17, 0\n\t"
    "v_exp_f32 v4, v4\n\t"
    "v_mfma_f32_16x16x32_bf16 a[48:51], v[192:195], v[20:23], a[48:51]\n\t"
    "v_exp_f32 v5, v5\n\t"
    "v_mfma_f32_16x16x32_bf16 a[52:55], v[196:199], v[20:23], a[52:55]\n\t"
    "v_exp_f32 v6, v6\n\t"
    "v_mfma_f32_16x16x32_bf16 a[56:59], v[200:203], v[20:23], a[56:59]\n\t"
    "v_exp_f32 v7, v7\n\t"
    "v_mfma_f32_16x16x32_bf16 a[60:63], v[204:207], v[20:23], a[60:63]\n\t"
    "global_load_dword a255, v55, s[16:17]\n\t"
    "v_cvt_pk_bf16_f32 v16, v0, v1\n\t"
    "v_mfma_f32_16x16x32_bf16 v[8:11], a[192:195], v[128:131], v[36:39]\n\t"
    "v_cvt_pk_bf16_f32 v17, v2, v3\n\t"
    "v_cvt_pk_bf16_f32 v18, v4, v5\n\t"
    "v_mfma_f32_16x16x32_bf16 v[8:11], a[196:199], v[132:135], v[8:11]\n\t"
    "ds_read_b64_tr_b16 v[230:231], v51 offset:12296\n\t"
    "v_cvt_pk_bf16_f32 v19, v6, v7\n\t"
    "v_mfma_f32_16x16x32_bf16 v[8:11], a[200:203], v[136:139], v[8:11]\n\t"
    "v_mfma_f32_16x16x32_bf16 v[8:11], a[204:207], v[140:143], v[8:11]\n\t"
    "ds_read_b64_tr_b16 v[232:233], v51 offset:8448\n\t"
    "v_mfma_f32_16x16x32_bf16 v[12:15], a[208:211], v[128:131], v[36:39]\n\t"
    "v_mfma_f32_16x16x32_bf16 v[12:15], a[212:215], v[132:135], v[12:15]\n\t"
    "ds_read_b64_tr_b16 v[234:235], v51 offset:12544\n\t"
    "v_mfma_f32_16x16x32_bf16 v[12:15], a[216:219], v[136:139], v[12:15]\n\t"
    "v_mfma_f32_16x16x32_bf16 v[12:15], a[220:223], v[140:143], v[12:15]\n\t"
    "v_exp_f32 v8, v8\n\t"
    "v_mfma_f32_16x16x32_bf16 a[232:235], a[248:251], v[16:19], a[232:235]\n\t"
    "v_exp_f32 v9, v9\n\t"
    "v_mfma_f32_16x16x32_bf16 a[64:67], v[176:179], v[16:19], a[64:67]\n\t"
    "v_exp_f32 v10, v10\n\t"
    "v_mfma_f32_16x16x32_bf16 a[68:71], v[180:183], v[16:19], a[68:71]\n\t"
    "v_exp_f32 v11, v11\n\t"
    "v_mfma_f32_16x16x32_bf16 a[72:75], v[184:187], v[16:19], a[72:75]\n\t"
    "v_exp_f32 v12, v12\n\t"
    "v_mfma_f32_16x16x32_bf16 a[76:79], v[188:191], v[16:19], a[76:79]\n\t"
    "v_exp_f32 v13, v13\n\t"
    "v_mfma_f32_16x16x32_bf16 a[80:83], v[192:195], v[16:19], a[80:83]\n\t"
    "buffer_load_dwordx4 v53, s[40:43], s75 offen offset:1024 nt lds\n\t"
    "v_mfma_f32_16x16x32_bf16 a[84:87], v[196:199], v[16:19], a[84:87]\n\t"
    "v_exp_f32 v14, v14\n\t"
    "v_mfma_f32_16x16x32_bf16 a[88:91], v[200:203], v[16:19], a[88:91]\n\t"
    "v_exp_f32 v15, v15\n\t"
    "v_mfma_f32_16x16x32_bf16 a[92:95], v[204:207], v[16:19], a[92:95]\n\t"
    "v_cvt_pk_bf16_f32 v20, v8, v9\n\t"
    "v_cvt_pk_bf16_f32 v21, v10, v11\n\t"
    "v_mfma_f32_16x16x32_bf16 v[0:3], a[192:195], v[144:147], v[40:43]\n\t"
    "v_cvt_pk_bf16_f32 v22, v12, v13\n\t"
    "v_cvt_pk_bf16_f32 v23, v14, v15\n\t"
    "v_mfma_f32_16x16x32_bf16 v[0:3], a[196:199], v[148:151], v[0:3]\n\t"
    "s_waitcnt vmcnt(11)\n\t"
    "v_lshlrev_b32 v64, 16, v212\n\t"
    "v_and_b32 v65, s49, v212\n\t"
    "v_mfma_f32_16x16x32_bf16 v[0:3], a[200:203], v[152:155], v[0:3]\n\t"
    "ds_read_b64_tr_b16 v[236:237], v51 offset:8456\n\t"
    "v_mul_f32 v64, s48, v64\n\t"
    "v_mfma_f32_16x16x32_bf16 v[0:3], a[204:207], v[156:159], v[0:3]\n\t"
    "v_mul_f32 v65, s48, v65\n\t"
    "v_cvt_pk_bf16_f32 v212, v64, v65\n\t"
    "v_mfma_f32_16x16x32_bf16 v[4:7], a[208:211], v[144:147], v[40:43]\n\t"
    "ds_read_b64_tr_b16 v[238:239], v51 offset:12552\n\t"
    "v_lshlrev_b32 v66, 16, v213\n\t"
    "v_mfma_f32_16x16x32_bf16 v[4:7], a[212:215], v[148:151], v[4:7]\n\t"
    "v_and_b32 v67, s49, v213\n\t"
    "v_mul_f32 v66, s48, v66\n\t"
    "v_mfma_f32_16x16x32_bf16 v[4:7], a[216:219], v[152:155], v[4:7]\n\t"
    "ds_read_b64_tr_b16 v[240:241], v51 offset:8704\n\t"
    "v_mul_f32 v67, s48, v67\n\t"
    "v_mfma_f32_16x16x32_bf16 v[4:7], a[220:223], v[156:159], v[4:7]\n\t"
    "v_exp_f32 v0, v0\n\t"
    "v_mfma_f32_16x16x32_bf16 a[236:239], a[248:251], v[20:23], a[236:239]\n\t"
    "v_exp_f32 v1, v1\n\t"
    "v_mfma_f32_16x16x32_bf16 a[96:99], v[176:179], v[20:23], a[96:99]\n\t"
    "v_exp_f32 v2, v2\n\t"
    "v_mfma_f32_16x16x32_bf16 a[100:103], v[180:183], v[20:23], a[100:103]\n\t"
    "v_exp_f32 v3, v3\n\t"
    "v_mfma_f32_16x16x32_bf16 a[104:107], v[184:187], v[20:23], a[104:107]\n\t"
    "v_exp_f32 v4, v4\n\t"
    "v_mfma_f32_16x16x32_bf16 a[108:111], v[188:191], v[20:23], a[108:111]\n\t"
    "v_exp_f32 v5, v5\n\t"
    "v_mfma_f32_16x16x32_bf16 a[112:115], v[192:195], v[20:23], a[112:115]\n\t"
    "v_exp_f32 v6, v6\n\t"
    "v_mfma_f32_16x16x32_bf16 a[116:119], v[196:199], v[20:23], a[116:119]\n\t"
    "v_exp_f32 v7, v7\n\t"
    "v_mfma_f32_16x16x32_bf16 a[120:123], v[200:203], v[20:23], a[120:123]\n\t"
    "v_cvt_pk_bf16_f32 v16, v0, v1\n\t"
    "v_cvt_pk_bf16_f32 v17, v2, v3\n\t"
    "v_mfma_f32_16x16x32_bf16 a[124:127], v[204:207], v[20:23], a[124:127]\n\t"
    "buffer_load_dwordx4 v[220:223], v54, s[28:31], s60 offen offset:192 nt\n\t"
    "v_cvt_pk_bf16_f32 v18, v4, v5\n\t"
    "v_mfma_f32_16x16x32_bf16 v[8:11], a[192:195], v[160:163], v[44:47]\n\t"
    "ds_read_b128 a[192:195], v49 offset:8192\n\t"
    "v_cvt_pk_bf16_f32 v19, v6, v7\n\t"
    "v_mfma_f32_16x16x32_bf16 v[8:11], a[196:199], v[164:167], v[8:11]\n\t"
    "ds_read_b128 a[196:199], v49 offset:8448\n\t"
    "v_cvt_pk_bf16_f32 v213, v66, v67\n\t"
    "v_mfma_f32_16x16x32_bf16 v[8:11], a[200:203], v[168:171], v[8:11]\n\t"
    "ds_read_b128 a[200:203], v49 offset:8704\n\t"
    "v_lshlrev_b32 v68, 16, v214\n\t"
    "v_mfma_f32_16x16x32_bf16 v[8:11], a[204:207], v[172:175], v[8:11]\n\t"
    "ds_read_b128 a[204:207], v49 offset:8960\n\t"
    "v_and_b32 v69, s49, v214\n\t"
    "v_mfma_f32_16x16x32_bf16 v[12:15], a[208:211], v[160:163], v[44:47]\n\t"
    "ds_read_b128 a[208:211], v49 offset:12288\n\t"
    "v_mul_f32 v68, s48, v68\n\t"
    "v_mfma_f32_16x16x32_bf16 v[12:15], a[212:215], v[164:167], v[12:15]\n\t"
    "ds_read_b128 a[212:215], v49 offset:12544\n\t"
    "v_mul_f32 v69, s48, v69\n\t"
    "v_mfma_f32_16x16x32_bf16 v[12:15], a[216:219], v[168:171], v[12:15]\n\t"
    "ds_read_b128 a[216:219], v49 offset:12800\n\t"
    "v_cvt_pk_bf16_f32 v214, v68, v69\n\t"
    "v_mfma_f32_16x16x32_bf16 v[12:15], a[220:223], v[172:175], v[12:15]\n\t"
    "ds_read_b128 a[220:223], v49 offset:13056\n\t"
    "v_lshlrev_b32 v70, 16, v215\n\t"
    "v_mfma_f32_16x16x32_bf16 a[240:243], a[248:251], v[16:19], a[240:243]\n\t"
    "v_exp_f32 v8, v8\n\t"
    "v_mfma_f32_16x16x32_bf16 a[128:131], v[176:179], v[16:19], a[128:131]\n\t"
    "v_exp_f32 v9, v9\n\t"
    "v_mfma_f32_16x16x32_bf16 a[132:135], v[180:183], v[16:19], a[132:135]\n\t"
    "v_exp_f32 v10, v10\n\t"
    "v_mfma_f32_16x16x32_bf16 a[136:139], v[184:187], v[16:19], a[136:139]\n\t"
    "v_exp_f32 v11, v11\n\t"
    "v_mfma_f32_16x16x32_bf16 a[140:143], v[188:191], v[16:19], a[140:143]\n\t"
    "v_exp_f32 v12, v12\n\t"
    "v_mfma_f32_16x16x32_bf16 a[144:147], v[192:195], v[16:19], a[144:147]\n\t"
    "v_exp_f32 v13, v13\n\t"
    "v_mfma_f32_16x16x32_bf16 a[148:151], v[196:199], v[16:19], a[148:151]\n\t"
    "v_exp_f32 v14, v14\n\t"
    "v_mfma_f32_16x16x32_bf16 a[152:155], v[200:203], v[16:19], a[152:155]\n\t"
    "v_exp_f32 v15, v15\n\t"
    "v_mfma_f32_16x16x32_bf16 a[156:159], v[204:207], v[16:19], a[156:159]\n\t"
    "s_waitcnt lgkmcnt(0)\n\t"
    "v_cvt_pk_bf16_f32 v20, v8, v9\n\t"
    "v_cvt_pk_bf16_f32 v21, v10, v11\n\t"
    "v_mfma_f32_16x16x32_bf16 v[0:3], a[192:195], v[80:83], v[24:27]\n\t"
    "v_cvt_pk_bf16_f32 v22, v12, v13\n\t"
    "v_cvt_pk_bf16_f32 v23, v14, v15\n\t"
    "v_mfma_f32_16x16x32_bf16 v[0:3], a[196:199], v[84:87], v[0:3]\n\t"
    "ds_read_b64_tr_b16 v[242:243], v51 offset:12800\n\t"
    "v_and_b32 v71, s49, v215\n\t"
    "v_mfma_f32_16x16x32_bf16 v[0:3], a[200:203], v[88:91], v[0:3]\n\t"
    "v_mul_f32 v70, s48, v70\n\t"
    "v_mul_f32 v71, s48, v71\n\t"
    "v_mfma_f32_16x16x32_bf16 v[0:3], a[204:207], v[92:95], v[0:3]\n\t"
    "ds_read_b64_tr_b16 v[244:245], v51 offset:8712\n\t"
    "v_cvt_pk_bf16_f32 v215, v70, v71\n\t"
    "v_mfma_f32_16x16x32_bf16 v[4:7], a[208:211], v[80:83], v[24:27]\n\t"
    "s_add_u32 m0, s51, 57344\n\t"
    "v_mfma_f32_16x16x32_bf16 v[4:7], a[212:215], v[84:87], v[4:7]\n\t"
    "buffer_load_dwordx4 v52, s[36:39], s76 offen offset:0 nt lds\n\t"
    "v_mfma_f32_16x16x32_bf16 v[4:7], a[216:219], v[88:91], v[4:7]\n\t"
    "ds_read_b64_tr_b16 v[246:247], v51 offset:12808\n\t"
    "v_mfma_f32_16x16x32_bf16 v[4:7], a[220:223], v[92:95], v[4:7]\n\t"
    "v_exp_f32 v0, v0\n\t"
    "v_mfma_f32_16x16x32_bf16 a[244:247], a[248:251], v[20:23], a[244:247]\n\t"
    "v_exp_f32 v1, v1\n\t"
    "v_mfma_f32_16x16x32_bf16 a[160:163], v[176:179], v[20:23], a[160:163]\n\t"
    "ds_read_b64_tr_b16 v[248:249], v51 offset:8960\n\t"
    "v_cvt_pk_bf16_f32 v16, v0, v1\n\t"
    "v_mfma_f32_16x16x32_bf16 a[164:167], v[180:183], v[20:23], a[164:167]\n\t"
    "v_exp_f32 v2, v2\n\t"
    "v_mfma_f32_16x16x32_bf16 a[168:171], v[184:187], v[20:23], a[168:171]\n\t"
    "v_exp_f32 v3, v3\n\t"
    "v_mfma_f32_16x16x32_bf16 a[172:175], v[188:191], v[20:23], a[172:175]\n\t"
    "ds_read_b64_tr_b16 v[250:251], v51 offset:13056\n\t"
    "v_cvt_pk_bf16_f32 v17, v2, v3\n\t"
    "v_mfma_f32_16x16x32_bf16 a[176:179], v[192:195], v[20:23], a[176:179]\n\t"
    "v_exp_f32 v4, v4\n\t"
    "v_mfma_f32_16x16x32_bf16 a[180:183], v[196:199], v[20:23], a[180:183]\n\t"
    "v_exp_f32 v5, v5\n\t"
    "v_mfma_f32_16x16x32_bf16 a[184:187], v[200:203], v[20:23], a[184:187]\n\t"
    "v_exp_f32 v6, v6\n\t"
    "v_mfma_f32_16x16x32_bf16 a[188:191], v[204:207], v[20:23], a[188:191]\n\t"
    "v_exp_f32 v7, v7\n\t"
    "v_mfma_f32_16x16x32_bf16 v[8:11], a[192:195], v[96:99], v[28:31]\n\t"
    "v_cvt_pk_bf16_f32 v18, v4, v5\n\t"
    "v_cvt_pk_bf16_f32 v19, v6, v7\n\t"
    "v_mfma_f32_16x16x32_bf16 v[8:11], a[196:199], v[100:103], v[8:11]\n\t"
    "ds_read_b64_tr_b16 v[252:253], v51 offset:8968\n\t"
    "v_mfma_f32_16x16x32_bf16 v[8:11], a[200:203], v[104:107], v[8:11]\n\t"
    "v_mfma_f32_16x16x32_bf16 v[8:11], a[204:207], v[108:111], v[8:11]\n\t"
    "ds_read_b64_tr_b16 v[254:255], v51 offset:13064\n\t"
    "v_mfma_f32_16x16x32_bf16 v[12:15], a[208:211], v[96:99], v[28:31]\n\t"
    "v_mfma_f32_16x16x32_bf16 v[12:15], a[212:215], v[100:103], v[12:15]\n\t"
    "v_mfma_f32_16x16x32_bf16 v[12:15], a[216:219], v[104:107], v[12:15]\n\t"
    "v_mfma_f32_16x16x32_bf16 v[12:15], a[220:223], v[108:111], v[12:15]\n\t"
    "v_exp_f32 v8, v8\n\t"
    "v_mfma_f32_16x16x32_bf16 a[224:227], a[248:251], v[16:19], a[224:227]\n\t"
    "buffer_load_dwordx4 v53, s[36:39], s76 offen offset:1024 nt lds\n\t"
    "v_mfma_f32_16x16x32_bf16 a[0:3], v[224:227], v[16:19], a[0:3]\n\t"
    "v_exp_f32 v9, v9\n\t"
    "v_mfma_f32_16x16x32_bf16 a[4:7], v[228:231], v[16:19], a[4:7]\n\t"
    "v_exp_f32 v10, v10\n\t"
    "v_mfma_f32_16x16x32_bf16 a[8:11], v[232:235], v[16:19], a[8:11]\n\t"
    "v_exp_f32 v11, v11\n\t"
    "v_mfma_f32_16x16x32_bf16 a[12:15], v[236:239], v[16:19], a[12:15]\n\t"
    "s_waitcnt lgkmcnt(0)\n\t"
    "v_exp_f32 v12, v12\n\t"
    "v_mfma_f32_16x16x32_bf16 a[16:19], v[240:243], v[16:19], a[16:19]\n\t"
    "v_exp_f32 v13, v13\n\t"
    "v_mfma_f32_16x16x32_bf16 a[20:23], v[244:247], v[16:19], a[20:23]\n\t"
    "v_exp_f32 v14, v14\n\t"
    "v_mfma_f32_16x16x32_bf16 a[24:27], v[248:251], v[16:19], a[24:27]\n\t"
    "v_exp_f32 v15, v15\n\t"
    "v_mfma_f32_16x16x32_bf16 a[28:31], v[252:255], v[16:19], a[28:31]\n\t"
    "v_cvt_pk_bf16_f32 v20, v8, v9\n\t"
    "v_cvt_pk_bf16_f32 v21, v10, v11\n\t"
    "v_mfma_f32_16x16x32_bf16 v[0:3], a[192:195], v[112:115], v[32:35]\n\t"
    "v_cvt_pk_bf16_f32 v22, v12, v13\n\t"
    "v_cvt_pk_bf16_f32 v23, v14, v15\n\t"
    "v_mfma_f32_16x16x32_bf16 v[0:3], a[196:199], v[116:119], v[0:3]\n\t"
    "v_mfma_f32_16x16x32_bf16 v[0:3], a[200:203], v[120:123], v[0:3]\n\t"
    "v_mfma_f32_16x16x32_bf16 v[0:3], a[204:207], v[124:127], v[0:3]\n\t"
    "v_mfma_f32_16x16x32_bf16 v[4:7], a[208:211], v[112:115], v[32:35]\n\t"
    "v_mfma_f32_16x16x32_bf16 v[4:7], a[212:215], v[116:119], v[4:7]\n\t"
    "v_mfma_f32_16x16x32_bf16 v[4:7], a[216:219], v[120:123], v[4:7]\n\t"
    "v_mfma_f32_16x16x32_bf16 v[4:7], a[220:223], v[124:127], v[4:7]\n\t"
    "v_exp_f32 v0, v0\n\t"
    "v_mfma_f32_16x16x32_bf16 a[228:231], a[248:251], v[20:23], a[228:231]\n\t"
    "v_exp_f32 v1, v1\n\t"
    "v_mfma_f32_16x16x32_bf16 a[32:35], v[224:227], v[20:23], a[32:35]\n\t"
    "v_exp_f32 v2, v2\n\t"
    "v_mfma_f32_16x16x32_bf16 a[36:39], v[228:231], v[20:23], a[36:39]\n\t"
    "s_add_u32 m0, s51, 139264\n\t"
    "v_exp_f32 v3, v3\n\t"
    "v_mfma_f32_16x16x32_bf16 a[40:43], v[232:235], v[20:23], a[40:43]\n\t"
    "buffer_load_dwordx4 v52, s[40:43], s76 offen offset:0 nt lds\n\t"
    "v_mfma_f32_16x16x32_bf16 a[44:47], v[236:239], v[20:23], a[44:47]\n\t"
    "v_exp_f32 v4, v4\n\t"
    "v_mfma_f32_16x16x32_bf16 a[48:51], v[240:243], v[20:23], a[48:51]\n\t"
    "v_exp_f32 v5, v5\n\t"
    "v_mfma_f32_16x16x32_bf16 a[52:55], v[244:247], v[20:23], a[52:55]\n\t"
    "v_exp_f32 v6, v6\n\t"
    "v_mfma_f32_16x16x32_bf16 a[56:59], v[248:251], v[20:23], a[56:59]\n\t"
    "v_exp_f32 v7, v7\n\t"
    "v_mfma_f32_16x16x32_bf16 a[60:63], v[252:255], v[20:23], a[60:63]\n\t"
    "v_cvt_pk_bf16_f32 v16, v0, v1\n\t"
    "v_cvt_pk_bf16_f32 v17, v2, v3\n\t"
    "v_mfma_f32_16x16x32_bf16 v[8:11], a[192:195], v[128:131], v[36:39]\n\t"
    "v_cvt_pk_bf16_f32 v18, v4, v5\n\t"
    "v_cvt_pk_bf16_f32 v19, v6, v7\n\t"
    "v_mfma_f32_16x16x32_bf16 v[8:11], a[196:199], v[132:135], v[8:11]\n\t"
    "v_mfma_f32_16x16x32_bf16 v[8:11], a[200:203], v[136:139], v[8:11]\n\t"
    "v_mfma_f32_16x16x32_bf16 v[8:11], a[204:207], v[140:143], v[8:11]\n\t"
    "v_mfma_f32_16x16x32_bf16 v[12:15], a[208:211], v[128:131], v[36:39]\n\t"
    "v_mfma_f32_16x16x32_bf16 v[12:15], a[212:215], v[132:135], v[12:15]\n\t"
    "s_add_u32 s16, s16, 2048\n\ts_addc_u32 s17, s17, 0\n\t"
    "v_mfma_f32_16x16x32_bf16 v[12:15], a[216:219], v[136:139], v[12:15]\n\t"
    "v_mfma_f32_16x16x32_bf16 v[12:15], a[220:223], v[140:143], v[12:15]\n\t"
    "v_exp_f32 v8, v8\n\t"
    "v_mfma_f32_16x16x32_bf16 a[232:235], a[248:251], v[16:19], a[232:235]\n\t"
    "s_waitcnt vmcnt(9)\n\t"
    "global_load_dword a255, v55, s[16:17]\n\t"
    "v_lshlrev_b32 v64, 16, v216\n\t"
    "v_mfma_f32_16x16x32_bf16 a[64:67], v[224:227], v[16:19], a[64:67]\n\t"
    "v_exp_f32 v9, v9\n\t"
    "v_mfma_f32_16x16x32_bf16 a[68:71], v[228:231], v[16:19], a[68:71]\n\t"
    "v_exp_f32 v10, v10\n\t"
    "v_mfma_f32_16x16x32_bf16 a[72:75], v[232:235], v[16:19], a[72:75]\n\t"
    "v_exp_f32 v11, v11\n\t"
    "v_mfma_f32_16x16x32_bf16 a[76:79], v[236:239], v[16:19], a[76:79]\n\t"
    "v_exp_f32 v12, v12\n\t"
    "v_mfma_f32_16x16x32_bf16 a[80:83], v[240:243], v[16:19], a[80:83]\n\t"
    "v_exp_f32 v13, v13\n\t"
    "v_mfma_f32_16x16x32_bf16 a[84:87], v[244:247], v[16:19], a[84:87]\n\t"
    "buffer_load_dwordx4 v53, s[40:43], s76 offen offset:1024 nt lds\n\t"
    "v_mfma_f32_16x16x32_bf16 a[88:91], v[248:251], v[16:19], a[88:91]\n\t"
    "v_exp_f32 v14, v14\n\t"
    "v_mfma_f32_16x16x32_bf16 a[92:95], v[252:255], v[16:19], a[92:95]\n\t"
    "v_exp_f32 v15, v15\n\t"
    "v_mfma_f32_16x16x32_bf16 v[0:3], a[192:195], v[144:147], v[40:43]\n\t"
    "v_cvt_pk_bf16_f32 v20, v8, v9\n\t"
    "v_cvt_pk_bf16_f32 v21, v10, v11\n\t"
    "v_mfma_f32_16x16x32_bf16 v[0:3], a[196:199], v[148:151], v[0:3]\n\t"
    "v_cvt_pk_bf16_f32 v22, v12, v13\n\t"
    "v_cvt_pk_bf16_f32 v23, v14, v15\n\t"
    "v_mfma_f32_16x16x32_bf16 v[0:3], a[200:203], v[152:155], v[0:3]\n\t"
    "v_and_b32 v65, s49, v216\n\t"
    "v_mul_f32 v64, s48, v64\n\t"
    "v_mfma_f32_16x16x32_bf16 v[0:3], a[204:207], v[156:159], v[0:3]\n\t"
    "v_mul_f32 v65, s48, v65\n\t"
    "v_cvt_pk_bf16_f32 v216, v64, v65\n\t"
    "v_mfma_f32_16x16x32_bf16 v[4:7], a[208:211], v[144:147], v[40:43]\n\t"
    "v_lshlrev_b32 v66, 16, v217\n\t"
    "v_and_b32 v67, s49, v217\n\t"
    "v_mfma_f32_16x16x32_bf16 v[4:7], a[212:215], v[148:151], v[4:7]\n\t"
    "v_mul_f32 v66, s48, v66\n\t"
    "v_mul_f32 v67, s48, v67\n\t"
    "v_mfma_f32_16x16x32_bf16 v[4:7], a[216:219], v[152:155], v[4:7]\n\t"
    "v_cvt_pk_bf16_f32 v217, v66, v67\n\t"
    "v_lshlrev_b32 v68, 16, v218\n\t"
    "v_mfma_f32_16x16x32_bf16 v[4:7], a[220:223], v[156:159], v[4:7]\n\t"
    "v_exp_f32 v0, v0\n\t"
    "v_mfma_f32_16x16x32_bf16 a[236:239], a[248:251], v[20:23], a[236:239]\n\t"
    "v_exp_f32 v1, v1\n\t"
    "v_mfma_f32_16x16x32_bf16 a[96:99], v[224:227], v[20:23], a[96:99]\n\t"
    "v_exp_f32 v2, v2\n\t"
    "v_mfma_f32_16x16x32_bf16 a[100:103], v[228:231], v[20:23], a[100:103]\n\t"
    "v_exp_f32 v3, v3\n\t"
    "v_mfma_f32_16x16x32_bf16 a[104:107], v[232:235], v[20:23], a[104:107]\n\t"
    "v_exp_f32 v4, v4\n\t"
    "v_mfma_f32_16x16x32_bf16 a[108:111], v[236:239], v[20:23], a[108:111]\n\t"
    "v_exp_f32 v5, v5\n\t"
    "v_mfma_f32_16x16x32_bf16 a[112:115], v[240:243], v[20:23], a[112:115]\n\t"
    "v_exp_f32 v6, v6\n\t"
    "v_mfma_f32_16x16x32_bf16 a[116:119], v[244:247], v[20:23], a[116:119]\n\t"
    "v_exp_f32 v7, v7\n\t"
    "v_mfma_f32_16x16x32_bf16 a[120:123], v[248:251], v[20:23], a[120:123]\n\t"
    "v_cvt_pk_bf16_f32 v16, v0, v1\n\t"
    "v_cvt_pk_bf16_f32 v17, v2, v3\n\t"
    "v_mfma_f32_16x16x32_bf16 a[124:127], v[252:255], v[20:23], a[124:127]\n\t"
    "v_cvt_pk_bf16_f32 v18, v4, v5\n\t"
    "v_cvt_pk_bf16_f32 v19, v6, v7\n\t"
    "v_mfma_f32_16x16x32_bf16 v[8:11], a[192:195], v[160:163], v[44:47]\n\t"
    "s_barrier\n\t"
    "buffer_load_dwordx4 v[176:179], v54, s[28:31], s58 offen offset:0 nt\n\t"
    "ds_read_b128 a[192:195], v48 offset:40960\n\t"
    "v_mfma_f32_16x16x32_bf16 v[8:11], a[196:199], v[164:167], v[8:11]\n\t"
    "ds_read_b128 a[196:199], v48 offset:41216\n\t"
    "v_and_b32 v69, s49, v218\n\t"
    "v_mfma_f32_16x16x32_bf16 v[8:11], a[200:203], v[168:171], v[8:11]\n\t"
    "ds_read_b128 a[200:203], v48 offset:41472\n\t"
    "v_mul_f32 v68, s48, v68\n\t"
    "v_mfma_f32_16x16x32_bf16 v[8:11], a[204:207], v[172:175], v[8:11]\n\t"
    "ds_read_b128 a[204:207], v48 offset:41728\n\t"
    "v_mul_f32 v69, s48, v69\n\t"
    "v_mfma_f32_16x16x32_bf16 v[12:15], a[208:211], v[160:163], v[44:47]\n\t"
    "ds_read_b128 a[208:211], v48 offset:45056\n\t"
    "v_cvt_pk_bf16_f32 v218, v68, v69\n\t"
    "v_mfma_f32_16x16x32_bf16 v[12:15], a[212:215], v[164:167], v[12:15]\n\t"
    "ds_read_b128 a[212:215], v48 offset:45312\n\t"
    "v_lshlrev_b32 v70, 16, v219\n\t"
    "v_mfma_f32_16x16x32_bf16 v[12:15], a[216:219], v[168:171], v[12:15]\n\t"
    "ds_read_b128 a[216:219], v48 offset:45568\n\t"
    "v_and_b32 v71, s49, v219\n\t"
    "v_mfma_f32_16x16x32_bf16 v[12:15], a[220:223], v[172:175], v[12:15]\n\t"
    "ds_read_b128 a[220:223], v48 offset:45824\n\t"
    "v_mul_f32 v70, s48, v70\n\t"
    "v_mfma_f32_16x16x32_bf16 a[240:243], a[248:251], v[16:19], a[240:243]\n\t"
    "v_exp_f32 v8, v8\n\t"
    "v_mfma_f32_16x16x32_bf16 a[128:131], v[224:227], v[16:19], a[128:131]\n\t"
    "v_exp_f32 v9, v9\n\t"
    "v_mfma_f32_16x16x32_bf16 a[132:135], v[228:231], v[16:19], a[132:135]\n\t"
    "v_exp_f32 v10, v10\n\t"
    "v_mfma_f32_16x16x32_bf16 a[136:139], v[232:235], v[16:19], a[136:139]\n\t"
    "v_exp_f32 v11, v11\n\t"
    "v_mfma_f32_16x16x32_bf16 a[140:143], v[236:239], v[16:19], a[140:143]\n\t"
    "v_exp_f32 v12, v12\n\t"
    "v_mfma_f32_16x16x32_bf16 a[144:147], v[240:243], v[16:19], a[144:147]\n\t"
    "v_exp_f32 v13, v13\n\t"
    "v_mfma_f32_16x16x32_bf16 a[148:151], v[244:247], v[16:19], a[148:151]\n\t"
    "v_exp_f32 v14, v14\n\t"
    "v_mfma_f32_16x16x32_bf16 a[152:155], v[248:251], v[16:19], a[152:155]\n\t"
    "v_exp_f32 v15, v15\n\t"
    "v_mfma_f32_16x16x32_bf16 a[156:159], v[252:255], v[16:19], a[156:159]\n\t"
    "s_waitcnt lgkmcnt(0)\n\t"
    "v_cvt_pk_bf16_f32 v20, v8, v9\n\t"
    "v_cvt_pk_bf16_f32 v21, v10, v11\n\t"
    "v_mfma_f32_16x16x32_bf16 v[0:3], a[192:195], v[80:83], v[24:27]\n\t"
    "v_cvt_pk_bf16_f32 v22, v12, v13\n\t"
    "v_cvt_pk_bf16_f32 v23, v14, v15\n\t"
    "v_mfma_f32_16x16x32_bf16 v[0:3], a[196:199], v[84:87], v[0:3]\n\t"
    "v_mul_f32 v71, s48, v71\n\t"
    "v_cvt_pk_bf16_f32 v219, v70, v71\n\t"
    "v_mfma_f32_16x16x32_bf16 v[0:3], a[200:203], v[88:91], v[0:3]\n\t"
    "v_mfma_f32_16x16x32_bf16 v[0:3], a[204:207], v[92:95], v[0:3]\n\t"
    "v_mfma_f32_16x16x32_bf16 v[4:7], a[208:211], v[80:83], v[24:27]\n\t"
    "v_mfma_f32_16x16x32_bf16 v[4:7], a[212:215], v[84:87], v[4:7]\n\t"
    "v_mfma_f32_16x16x32_bf16 v[4:7], a[216:219], v[88:91], v[4:7]\n\t"
    "buffer_load_dwordx4 v[180:183], v54, s[28:31], s58 offen offset:64 nt\n\t"
    "v_mfma_f32_16x16x32_bf16 v[4:7], a[220:223], v[92:95], v[4:7]\n\t"
    "v_exp_f32 v0, v0\n\t"
    "v_mfma_f32_16x16x32_bf16 a[244:247], a[248:251], v[20:23], a[244:247]\n\t"
    "v_exp_f32 v1, v1\n\t"
    "v_mfma_f32_16x16x32_bf16 a[160:163], v[224:227], v[20:23], a[160:163]\n\t"
    "ds_read_b64_tr_b16 v[224:225], v50 offset:40960\n\t"
    "ds_read_b64_tr_b16 v[226:227], v50 offset:45056\n\t"
    "v_mfma_f32_16x16x32_bf16 a[164:167], v[228:231], v[20:23], a[164:167]\n\t"
    "ds_read_b64_tr_b16 v[228:229], v50 offset:40968\n\t"
    "ds_read_b64_tr_b16 v[230:231], v50 offset:45064\n\t"
    "v_mfma_f32_16x16x32_bf16 a[168:171], v[232:235], v[20:23], a[168:171]\n\t"
    "ds_read_b64_tr_b16 v[232:233], v50 offset:41216\n\t"
    "ds_read_b64_tr_b16 v[234:235], v50 offset:45312\n\t"
    "v_mfma_f32_16x16x32_bf16 a[172:175], v[236:239], v[20:23], a[172:175]\n\t"
    "ds_read_b64_tr_b16 v[236:237], v50 offset:41224\n\t"
    "ds_read_b64_tr_b16 v[238:239], v50 offset:45320\n\t"
    "v_mfma_f32_16x16x32_bf16 a[176:179], v[240:243], v[20:23], a[176:179]\n\t"
    "v_exp_f32 v2, v2\n\t"
    "v_mfma_f32_16x16x32_bf16 a[180:183], v[244:247], v[20:23], a[180:183]\n\t"
    "v_exp_f32 v3, v3\n\t"
    "v_mfma_f32_16x16x32_bf16 a[184:187], v[248:251], v[20:23], a[184:187]\n\t"
    "v_exp_f32 v4, v4\n\t"
    "v_mfma_f32_16x16x32_bf16 a[188:191], v[252:255], v[20:23], a[188:191]\n\t"
    "v_exp_f32 v5, v5\n\t"
    "v_mfma_f32_16x16x32_bf16 v[8:11], a[192:195], v[96:99], v[28:31]\n\t"
    "v_exp_f32 v6, v6\n\t"
    "v_mfma_f32_16x16x32_bf16 v[8:11], a[196:199], v[100:103], v[8:11]\n\t"
    "v_exp_f32 v7, v7\n\t"
    "v_mfma_f32_16x16x32_bf16 v[8:11], a[200:203], v[104:107], v[8:11]\n\t"
    "ds_read_b64_tr_b16 v[240:241], v50 offset:41472\n\t"
    "ds_read_b64_tr_b16 v[242:243], v50 offset:45568\n\t"
    "v_mfma_f32_16x16x32_bf16 v[8:11], a[204:207], v[108:111], v[8:11]\n\t"
    "v_cvt_pk_bf16_f32 v16, v0, v1\n\t"
    "v_cvt_pk_bf16_f32 v17, v2, v3\n\t"
    "v_mfma_f32_16x16x32_bf16 v[12:15], a[208:211], v[96:99], v[28:31]\n\t"
    "v_cvt_pk_bf16_f32 v18, v4, v5\n\t"
    "v_cvt_pk_bf16_f32 v19, v6, v7\n\t"
    "v_mfma_f32_16x16x32_bf16 v[12:15], a[212:215], v[100:103], v[12:15]\n\t"
    "ds_read_b64_tr_b16 v[244:245], v50 offset:41480\n\t"
    "ds_read_b64_tr_b16 v[246:247], v50 offset:45576\n\t"
    "v_mfma_f32_16x16x32_bf16 v[12:15], a[216:219], v[104:107], v[12:15]\n\t"
    "ds_read_b64_tr_b16 v[248:249], v50 offset:41728\n\t"
    "ds_read_b64_tr_b16 v[250:251], v50 offset:45824\n\t"
    "v_mfma_f32_16x16x32_bf16 v[12:15], a[220:223], v[108:111], v[12:15]\n\t"
    "ds_read_b64_tr_b16 v[252:253], v50 offset:41736\n\t"
    "ds_read_b64_tr_b16 v[254:255], v50 offset:45832\n\t"
    "v_mfma_f32_16x16x32_bf16 a[224:227], a[248:251], v[16:19], a[224:227]\n\t"
    "s_waitcnt lgkmcnt(8)\n\t"
    "v_exp_f32 v8, v8\n\t"
    "v_mfma_f32_16x16x32_bf16 a[0:3], v[224:227], v[16:19], a[0:3]\n\t"
    "v_exp_f32 v9, v9\n\t"
    "v_mfma_f32_16x16x32_bf16 a[4:7], v[228:231], v[16:19], a[4:7]\n\t"
    "v_exp_f32 v10, v10\n\t"
    "v_mfma_f32_16x16x32_bf16 a[8:11], v[232:235], v[16:19], a[8:11]\n\t"
    "s_add_u32 m0, s51, 65536\n\t"
    "v_exp_f32 v11, v11\n\t"
    "v_mfma_f32_16x16x32_bf16 a[12:15], v[236:239], v[16:19], a[12:15]\n\t"
    "s_waitcnt lgkmcnt(6)\n\t"
    "buffer_load_dwordx4 v52, s[36:39], s77 offen offset:0 nt lds\n\t"
    "v_mfma_f32_16x16x32_bf16 a[16:19], v[240:243], v[16:19], a[16:19]\n\t"
    "s_waitcnt lgkmcnt(4)\n\t"
    "v_exp_f32 v12, v12\n\t"
    "v_mfma_f32_16x16x32_bf16 a[20:23], v[244:247], v[16:19], a[20:23]\n\t"
    "s_waitcnt lgkmcnt(2)\n\t"
    "v_exp_f32 v13, v13\n\t"
    "v_mfma_f32_16x16x32_bf16 a[24:27], v[248:251], v[16:19], a[24:27]\n\t"
    "s_waitcnt lgkmcnt(0)\n\t"
    "v_exp_f32 v14, v14\n\t"
    "v_mfma_f32_16x16x32_bf16 a[28:31], v[252:255], v[16:19], a[28:31]\n\t"
    "v_exp_f32 v15, v15\n\t"
    "v_mfma_f32_16x16x32_bf16 v[0:3], a[192:195], v[112:115], v[32:35]\n\t"
    "v_cvt_pk_bf16_f32 v20, v8, v9\n\t"
    "v_cvt_pk_bf16_f32 v21, v10, v11\n\t"
    "v_mfma_f32_16x16x32_bf16 v[0:3], a[196:199], v[116:119], v[0:3]\n\t"
    "v_cvt_pk_bf16_f32 v22, v12, v13\n\t"
    "v_cvt_pk_bf16_f32 v23, v14, v15\n\t"
    "v_mfma_f32_16x16x32_bf16 v[0:3], a[200:203], v[120:123], v[0:3]\n\t"
    "v_mfma_f32_16x16x32_bf16 v[0:3], a[204:207], v[124:127], v[0:3]\n\t"
    "v_mfma_f32_16x16x32_bf16 v[4:7], a[208:211], v[112:115], v[32:35]\n\t"
    "v_mfma_f32_16x16x32_bf16 v[4:7], a[212:215], v[116:119], v[4:7]\n\t"
    "v_mfma_f32_16x16x32_bf16 v[4:7], a[216:219], v[120:123], v[4:7]\n\t"
    "v_mfma_f32_16x16x32_bf16 v[4:7], a[220:223], v[124:127], v[4:7]\n\t"
    "PAD4_%=:\n\ts_cbranch_vccnz PAD8_%=\n\t"
    "v_exp_f32 v0, v0\n\t"
    "v_mfma_f32_16x16x32_bf16 a[228:231], a[248:251], v[20:23], a[228:231]\n\t"
    "v_exp_f32 v1, v1\n\t"
    "v_mfma_f32_16x16x32_bf16 a[32:35], v[224:227], v[20:23], a[32:35]\n\t"
    "v_exp_f32 v2, v2\n\t"
    "v_mfma_f32_16x16x32_bf16 a[36:39], v[228:231], v[20:23], a[36:39]\n\t"
    "v_exp_f32 v3, v3\n\t"
    "v_mfma_f32_16x16x32_bf16 a[40:43], v[232:235], v[20:23], a[40:43]\n\t"
    "v_exp_f32 v4, v4\n\t"
    "v_mfma_f32_16x16x32_bf16 a[44:47], v[236:239], v[20:23], a[44:47]\n\t"
    "v_exp_f32 v5, v5\n\t"
    "v_mfma_f32_16x16x32_bf16 a[48:51], v[240:243], v[20:23], a[48:51]\n\t"
    "v_exp_f32 v6, v6\n\t"
    "v_mfma_f32_16x16x32_bf16 a[52:55], v[244:247], v[20:23], a[52:55]\n\t"
    "v_exp_f32 v7, v7\n\t"
    "v_mfma_f32_16x16x32_bf16 a[56:59], v[248:251], v[20:23], a[56:59]\n\t"
    "buffer_load_dwordx4 v53, s[36:39], s77 offen offset:1024 nt lds\n\t"
    "v_mfma_f32_16x16x32_bf16 a[60:63], v[252:255], v[20:23], a[60:63]\n\t"
    "v_cvt_pk_bf16_f32 v16, v0, v1\n\t"
    "v_cvt_pk_bf16_f32 v17, v2, v3\n\t"
    "v_mfma_f32_16x16x32_bf16 v[8:11], a[192:195], v[128:131], v[36:39]\n\t"
    "v_cvt_pk_bf16_f32 v18, v4, v5\n\t"
    "v_cvt_pk_bf16_f32 v19, v6, v7\n\t"
    "v_mfma_f32_16x16x32_bf16 v[8:11], a[196:199], v[132:135], v[8:11]\n\t"
    "v_mfma_f32_16x16x32_bf16 v[8:11], a[200:203], v[136:139], v[8:11]\n\t"
    "v_mfma_f32_16x16x32_bf16 v[8:11], a[204:207], v[140:143], v[8:11]\n\t"
    "v_mfma_f32_16x16x32_bf16 v[12:15], a[208:211], v[128:131], v[36:39]\n\t"
    "v_mfma_f32_16x16x32_bf16 v[12:15], a[212:215], v[132:135], v[12:15]\n\t"
    "v_mfma_f32_16x16x32_bf16 v[12:15], a[216:219], v[136:139], v[12:15]\n\t"
    "v_mfma_f32_16x16x32_bf16 v[12:15], a[220:223], v[140:143], v[12:15]\n\t"
    "v_exp_f32 v8, v8\n\t"
    "v_mfma_f32_16x16x32_bf16 a[232:235], a[248:251], v[16:19], a[232:235]\n\t"
    "v_exp_f32 v9, v9\n\t"
    "v_mfma_f32_16x16x32_bf16 a[64:67], v[224:227], v[16:19], a[64:67]\n\t"
    "v_exp_f32 v10, v10\n\t"
    "v_mfma_f32_16x16x32_bf16 a[68:71], v[228:231], v[16:19], a[68:71]\n\t"
    "v_exp_f32 v11, v11\n\t"
    "v_mfma_f32_16x16x32_bf16 a[72:75], v[232:235], v[16:19], a[72:75]\n\t"
    "v_exp_f32 v12, v12\n\t"
    "v_mfma_f32_16x16x32_bf16 a[76:79], v[236:239], v[16:19], a[76:79]\n\t"
    "v_exp_f32 v13, v13\n\t"
    "v_mfma_f32_16x16x32_bf16 a[80:83], v[240:243], v[16:19], a[80:83]\n\t"
    "v_exp_f32 v14, v14\n\t"
    "v_mfma_f32_16x16x32_bf16 a[84:87], v[244:247], v[16:19], a[84:87]\n\t"
    "v_exp_f32 v15, v15\n\t"
    "v_mfma_f32_16x16x32_bf16 a[88:91], v[248:251], v[16:19], a[88:91]\n\t"
    "s_add_u32 s16, s16, 2048\n\ts_addc_u32 s17, s17, 0\n\t"
    "v_cvt_pk_bf16_f32 v20, v8, v9\n\t"
    "v_cvt_pk_bf16_f32 v21, v10, v11\n\t"
    "v_mfma_f32_16x16x32_bf16 a[92:95], v[252:255], v[16:19], a[92:95]\n\t"
    "v_cvt_pk_bf16_f32 v22, v12, v13\n\t"
    "v_cvt_pk_bf16_f32 v23, v14, v15\n\t"
    "v_mfma_f32_16x16x32_bf16 v[0:3], a[192:195], v[144:147], v[40:43]\n\t"
    "s_waitcnt vmcnt(9)\n\t"
    "s_add_u32 m0, s51, 147456\n\t"
    "global_load_dword a255, v55, s[16:17]\n\t"
    "v_lshlrev_b32 v64, 16, v220\n\t"
    "v_mfma_f32_16x16x32_bf16 v[0:3], a[196:199], v[148:151], v[0:3]\n\t"
    "buffer_load_dwordx4 v52, s[40:43], s77 offen offset:0 nt lds\n\t"
    "v_mfma_f32_16x16x32_bf16 v[0:3], a[200:203], v[152:155], v[0:3]\n\t"
    "v_and_b32 v65, s49, v220\n\t"
    "v_mul_f32 v64, s48, v64\n\t"
    "v_mfma_f32_16x16x32_bf16 v[0:3], a[204:207], v[156:159], v[0:3]\n\t"
    "v_mul_f32 v65, s48, v65\n\t"
    "v_cvt_pk_bf16_f32 v220, v64, v65\n\t"
    "v_mfma_f32_16x16x32_bf16 v[4:7], a[208:211], v[144:147], v[40:43]\n\t"
    "v_lshlrev_b32 v66, 16, v221\n\t"
    "v_and_b32 v67, s49, v221\n\t"
    "v_mfma_f32_16x16x32_bf16 v[4:7], a[212:215], v[148:151], v[4:7]\n\t"
    "v_mul_f32 v66, s48, v66\n\t"
    "v_mul_f32 v67, s48, v67\n\t"
    "v_mfma_f32_16x16x32_bf16 v[4:7], a[216:219], v[152:155], v[4:7]\n\t"
    "v_cvt_pk_bf16_f32 v221, v66, v67\n\t"
    "v_lshlrev_b32 v68, 16, v222\n\t"
    "v_mfma_f32_16x16x32_bf16 v[4:7], a[220:223], v[156:159], v[4:7]\n\t"
    "v_exp_f32 v0, v0\n\t"
    "v_mfma_f32_16x16x32_bf16 a[236:239], a[248:251], v[20:23], a[236:239]\n\t"
    "v_exp_f32 v1, v1\n\t"
    "v_mfma_f32_16x16x32_bf16 a[96:99], v[224:227], v[20:23], a[96:99]\n\t"
    "v_exp_f32 v2, v2\n\t"
    "v_mfma_f32_16x16x32_bf16 a[100:103], v[228:231], v[20:23], a[100:103]\n\t"
    "v_exp_f32 v3, v3\n\t"
    "v_mfma_f32_16x16x32_bf16 a[104:107], v[232:235], v[20:23], a[104:107]\n\t"
    "v_exp_f32 v4, v4\n\t"
    "v_mfma_f32_16x16x32_bf16 a[108:111], v[236:239], v[20:23], a[108:111]\n\t"
    "v_exp_f32 v5, v5\n\t"
    "v_mfma_f32_16x16x32_bf16 a[112:115], v[240:243], v[20:23], a[112:115]\n\t"
    "v_exp_f32 v6, v6\n\t"
    "v_mfma_f32_16x16x32_bf16 a[116:119], v[244:247], v[20:23], a[116:119]\n\t"
    "v_exp_f32 v7, v7\n\t"
    "v_mfma_f32_16x16x32_bf16 a[120:123], v[248:251], v[20:23], a[120:123]\n\t"
    "v_cvt_pk_bf16_f32 v16, v0, v1\n\t"
    "v_cvt_pk_bf16_f32 v17, v2, v3\n\t"
    "v_mfma_f32_16x16x32_bf16 a[124:127], v[252:255], v[20:23], a[124:127]\n\t"
    "v_cvt_pk_bf16_f32 v18, v4, v5\n\t"
    "v_cvt_pk_bf16_f32 v19, v6, v7\n\t"
    "v_mfma_f32_16x16x32_bf16 v[8:11], a[192:195], v[160:163], v[44:47]\n\t"
    "s_barrier\n\t"
    "ds_read_b128 a[192:195], v48 offset:49152\n\t"
    "v_and_b32 v69, s49, v222\n\t"
    "v_mfma_f32_16x16x32_bf16 v[8:11], a[196:199], v[164:167], v[8:11]\n\t"
    "ds_read_b128 a[196:199], v48 offset:49408\n\t"
    "v_mul_f32 v68, s48, v68\n\t"
    "v_mfma_f32_16x16x32_bf16 v[8:11], a[200:203], v[168:171], v[8:11]\n\t"
    "ds_read_b128 a[200:203], v48 offset:49664\n\t"
    "v_mul_f32 v69, s48, v69\n\t"
    "v_mfma_f32_16x16x32_bf16 v[8:11], a[204:207], v[172:175], v[8:11]\n\t"
    "ds_read_b128 a[204:207], v48 offset:49920\n\t"
    "v_cvt_pk_bf16_f32 v222, v68, v69\n\t"
    "v_mfma_f32_16x16x32_bf16 v[12:15], a[208:211], v[160:163], v[44:47]\n\t"
    "buffer_load_dwordx4 v53, s[40:43], s77 offen offset:1024 nt lds\n\t"
    "v_mfma_f32_16x16x32_bf16 v[12:15], a[212:215], v[164:167], v[12:15]\n\t"
    "ds_read_b128 a[208:211], v48 offset:53248\n\t"
    "v_lshlrev_b32 v70, 16, v223\n\t"
    "v_mfma_f32_16x16x32_bf16 v[12:15], a[216:219], v[168:171], v[12:15]\n\t"
    "ds_read_b128 a[212:215], v48 offset:53504\n\t"
    "v_and_b32 v71, s49, v223\n\t"
    "v_mfma_f32_16x16x32_bf16 v[12:15], a[220:223], v[172:175], v[12:15]\n\t"
    "ds_read_b128 a[216:219], v48 offset:53760\n\t"
    "v_mul_f32 v70, s48, v70\n\t"
    "v_mfma_f32_16x16x32_bf16 a[240:243], a[248:251], v[16:19], a[240:243]\n\t"
    "ds_read_b128 a[220:223], v48 offset:54016\n\t"
    "v_mul_f32 v71, s48, v71\n\t"
    "v_mfma_f32_16x16x32_bf16 a[128:131], v[224:227], v[16:19], a[128:131]\n\t"
    "v_exp_f32 v8, v8\n\t"
    "v_mfma_f32_16x16x32_bf16 a[132:135], v[228:231], v[16:19], a[132:135]\n\t"
    "v_exp_f32 v9, v9\n\t"
    "v_mfma_f32_16x16x32_bf16 a[136:139], v[232:235], v[16:19], a[136:139]\n\t"
    "v_exp_f32 v10, v10\n\t"
    "v_mfma_f32_16x16x32_bf16 a[140:143], v[236:239], v[16:19], a[140:143]\n\t"
    "v_exp_f32 v11, v11\n\t"
    "v_mfma_f32_16x16x32_bf16 a[144:147], v[240:243], v[16:19], a[144:147]\n\t"
    "v_exp_f32 v12, v12\n\t"
    "v_mfma_f32_16x16x32_bf16 a[148:151], v[244:247], v[16:19], a[148:151]\n\t"
    "v_exp_f32 v13, v13\n\t"
    "v_mfma_f32_16x16x32_bf16 a[152:155], v[248:251], v[16:19], a[152:155]\n\t"
    "v_exp_f32 v14, v14\n\t"
    "v_mfma_f32_16x16x32_bf16 a[156:159], v[252:255], v[16:19], a[156:159]\n\t"
    "s_waitcnt lgkmcnt(0)\n\t"
    "v_exp_f32 v15, v15\n\t"
    "v_mfma_f32_16x16x32_bf16 v[0:3], a[192:195], v[80:83], v[24:27]\n\t"
    "v_cvt_pk_bf16_f32 v20, v8, v9\n\t"
    "v_cvt_pk_bf16_f32 v21, v10, v11\n\t"
    "v_mfma_f32_16x16x32_bf16 v[0:3], a[196:199], v[84:87], v[0:3]\n\t"
    "v_cvt_pk_bf16_f32 v22, v12, v13\n\t"
    "v_cvt_pk_bf16_f32 v23, v14, v15\n\t"
    "v_mfma_f32_16x16x32_bf16 v[0:3], a[200:203], v[88:91], v[0:3]\n\t"
    "v_cvt_pk_bf16_f32 v223, v70, v71\n\t"
    "v_mfma_f32_16x16x32_bf16 v[0:3], a[204:207], v[92:95], v[0:3]\n\t"
    "v_mfma_f32_16x16x32_bf16 v[4:7], a[208:211], v[80:83], v[24:27]\n\t"
    "v_mfma_f32_16x16x32_bf16 v[4:7], a[212:215], v[84:87], v[4:7]\n\t"
    "v_mfma_f32_16x16x32_bf16 v[4:7], a[216:219], v[88:91], v[4:7]\n\t"
    "v_mfma_f32_16x16x32_bf16 v[4:7], a[220:223], v[92:95], v[4:7]\n\t"
    "v_exp_f32 v0, v0\n\t"
    "v_mfma_f32_16x16x32_bf16 a[244:247], a[248:251], v[20:23], a[244:247]\n\t"
    "v_exp_f32 v1, v1\n\t"
    "v_mfma_f32_16x16x32_bf16 a[160:163], v[224:227], v[20:23], a[160:163]\n\t"
    "buffer_load_dwordx4 v[184:187], v54, s[28:31], s58 offen offset:128 nt\n\t"
    "ds_read_b64_tr_b16 v[224:225], v50 offset:49152\n\t"
    "v_mfma_f32_16x16x32_bf16 a[164:167], v[228:231], v[20:23], a[164:167]\n\t"
    "ds_read_b64_tr_b16 v[226:227], v50 offset:53248\n\t"
    "ds_read_b64_tr_b16 v[228:229], v50 offset:49160\n\t"
    "v_mfma_f32_16x16x32_bf16 a[168:171], v[232:235], v[20:23], a[168:171]\n\t"
    "ds_read_b64_tr_b16 v[230:231], v50 offset:53256\n\t"
    "ds_read_b64_tr_b16 v[232:233], v50 offset:49408\n\t"
    "v_mfma_f32_16x16x32_bf16 a[172:175], v[236:239], v[20:23], a[172:175]\n\t"
    "ds_read_b64_tr_b16 v[234:235], v50 offset:53504\n\t"
    "ds_read_b64_tr_b16 v[236:237], v50 offset:49416\n\t"
    "v_mfma_f32_16x16x32_bf16 a[176:179], v[240:243], v[20:23], a[176:179]\n\t"
    "ds_read_b64_tr_b16 v[238:239], v50 offset:53512\n\t"
    "ds_read_b64_tr_b16 v[240:241], v50 offset:49664\n\t"
    "v_mfma_f32_16x16x32_bf16 a[180:183], v[244:247], v[20:23], a[180:183]\n\t"
    "v_exp_f32 v2, v2\n\t"
    "v_mfma_f32_16x16x32_bf16 a[184:187], v[248:251], v[20:23], a[184:187]\n\t"
    "v_exp_f32 v3, v3\n\t"
    "v_mfma_f32_16x16x32_bf16 a[188:191], v[252:255], v[20:23], a[188:191]\n\t"
    "v_exp_f32 v4, v4\n\t"
    "v_mfma_f32_16x16x32_bf16 v[8:11], a[192:195], v[96:99], v[28:31]\n\t"
    "v_exp_f32 v5, v5\n\t"
    "v_mfma_f32_16x16x32_bf16 v[8:11], a[196:199], v[100:103], v[8:11]\n\t"
    "v_exp_f32 v6, v6\n\t"
    "v_mfma_f32_16x16x32_bf16 v[8:11], a[200:203], v[104:107], v[8:11]\n\t"
    "v_exp_f32 v7, v7\n\t"
    "v_mfma_f32_16x16x32_bf16 v[8:11], a[204:207], v[108:111], v[8:11]\n\t"
    "ds_read_b64_tr_b16 v[242:243], v50 offset:53760\n\t"
    "v_cvt_pk_bf16_f32 v16, v0, v1\n\t"
    "v_mfma_f32_16x16x32_bf16 v[12:15], a[208:211], v[96:99], v[28:31]\n\t"
    "v_cvt_pk_bf16_f32 v17, v2, v3\n\t"
    "v_cvt_pk_bf16_f32 v18, v4, v5\n\t"
    "v_mfma_f32_16x16x32_bf16 v[12:15], a[212:215], v[100:103], v[12:15]\n\t"
    "ds_read_b64_tr_b16 v[244:245], v50 offset:49672\n\t"
    "v_cvt_pk_bf16_f32 v19, v6, v7\n\t"
    "v_mfma_f32_16x16x32_bf16 v[12:15], a[216:219], v[104:107], v[12:15]\n\t"
    "ds_read_b64_tr_b16 v[246:247], v50 offset:53768\n\t"
    "ds_read_b64_tr_b16 v[248:249], v50 offset:49920\n\t"
    "v_mfma_f32_16x16x32_bf16 v[12:15], a[220:223], v[108:111], v[12:15]\n\t"
    "ds_read_b64_tr_b16 v[250:251], v50 offset:54016\n\t"
    "ds_read_b64_tr_b16 v[252:253], v50 offset:49928\n\t"
    "v_mfma_f32_16x16x32_bf16 a[224:227], a[248:251], v[16:19], a[224:227]\n\t"
    "s_waitcnt lgkmcnt(6)\n\t"
    "v_exp_f32 v8, v8\n\t"
    "v_mfma_f32_16x16x32_bf16 a[0:3], v[224:227], v[16:19], a[0:3]\n\t"
    "ds_read_b64_tr_b16 v[254:255], v50 offset:54024\n\t"
    "v_mfma_f32_16x16x32_bf16 a[4:7], v[228:231], v[16:19], a[4:7]\n\t"
    "v_exp_f32 v9, v9\n\t"
    "v_mfma_f32_16x16x32_bf16 a[8:11], v[232:235], v[16:19], a[8:11]\n\t"
    "v_exp_f32 v10, v10\n\t"
    "v_mfma_f32_16x16x32_bf16 a[12:15], v[236:239], v[16:19], a[12:15]\n\t"
    "s_waitcnt lgkmcnt(6)\n\t"
    "v_exp_f32 v11, v11\n\t"
    "v_mfma_f32_16x16x32_bf16 a[16:19], v[240:243], v[16:19], a[16:19]\n\t"
    "s_waitcnt lgkmcnt(4)\n\t"
    "buffer_load_dwordx4 v[188:191], v54, s[28:31], s58 offen offset:192 nt\n\t"
    "v_cvt_pk_bf16_f32 v20, v8, v9\n\t"
    "v_mfma_f32_16x16x32_bf16 a[20:23], v[244:247], v[16:19], a[20:23]\n\t"
    "s_waitcnt lgkmcnt(2)\n\t"
    "v_exp_f32 v12, v12\n\t"
    "v_mfma_f32_16x16x32_bf16 a[24:27], v[248:251], v[16:19], a[24:27]\n\t"
    "s_waitcnt lgkmcnt(0)\n\t"
    "v_exp_f32 v13, v13\n\t"
    "v_mfma_f32_16x16x32_bf16 a[28:31], v[252:255], v[16:19], a[28:31]\n\t"
    "v_exp_f32 v14, v14\n\t"
    "v_mfma_f32_16x16x32_bf16 v[0:3], a[192:195], v[112:115], v[32:35]\n\t"
    "v_exp_f32 v15, v15\n\t"
    "v_mfma_f32_16x16x32_bf16 v[0:3], a[196:199], v[116:119], v[0:3]\n\t"
    "v_cvt_pk_bf16_f32 v21, v10, v11\n\t"
    "v_cvt_pk_bf16_f32 v22, v12, v13\n\t"
    "v_mfma_f32_16x16x32_bf16 v[0:3], a[200:203], v[120:123], v[0:3]\n\t"
    "v_cvt_pk_bf16_f32 v23, v14, v15\n\t"
    "v_mfma_f32_16x16x32_bf16 v[0:3], a[204:207], v[124:127], v[0:3]\n\t"
    "v_mfma_f32_16x16x32_bf16 v[4:7], a[208:211], v[112:115], v[32:35]\n\t"
    "v_mfma_f32_16x16x32_bf16 v[4:7], a[212:215], v[116:119], v[4:7]\n\t"
    "v_mfma_f32_16x16x32_bf16 v[4:7], a[216:219], v[120:123], v[4:7]\n\t"
    "v_mfma_f32_16x16x32_bf16 v[4:7], a[220:223], v[124:127], v[4:7]\n\t"
    "v_exp_f32 v0, v0\n\t"
    "v_mfma_f32_16x16x32_bf16 a[228:231], a[248:251], v[20:23], a[228:231]\n\t"
    "v_exp_f32 v1, v1\n\t"
    "v_mfma_f32_16x16x32_bf16 a[32:35], v[224:227], v[20:23], a[32:35]\n\t"
    "v_exp_f32 v2, v2\n\t"
    "v_mfma_f32_16x16x32_bf16 a[36:39], v[228:231], v[20:23], a[36:39]\n\t"
    "v_exp_f32 v3, v3\n\t"
    "v_mfma_f32_16x16x32_bf16 a[40:43], v[232:235], v[20:23], a[40:43]\n\t"
    "v_exp_f32 v4, v4\n\t"
    "v_mfma_f32_16x16x32_bf16 a[44:47], v[236:239], v[20:23], a[44:47]\n\t"
    "v_exp_f32 v5, v5\n\t"
    "v_mfma_f32_16x16x32_bf16 a[48:51], v[240:243], v[20:23], a[48:51]\n\t"
    "v_exp_f32 v6, v6\n\t"
    "v_mfma_f32_16x16x32_bf16 a[52:55], v[244:247], v[20:23], a[52:55]\n\t"
    "v_exp_f32 v7, v7\n\t"
    "v_mfma_f32_16x16x32_bf16 a[56:59], v[248:251], v[20:23], a[56:59]\n\t"
    "v_cvt_pk_bf16_f32 v16, v0, v1\n\t"
    "v_cvt_pk_bf16_f32 v17, v2, v3\n\t"
    "v_mfma_f32_16x16x32_bf16 a[60:63], v[252:255], v[20:23], a[60:63]\n\t"
    "v_cvt_pk_bf16_f32 v18, v4, v5\n\t"
    "v_cvt_pk_bf16_f32 v19, v6, v7\n\t"
    "v_mfma_f32_16x16x32_bf16 v[8:11], a[192:195], v[128:131], v[36:39]\n\t"
    "v_mfma_f32_16x16x32_bf16 v[8:11], a[196:199], v[132:135], v[8:11]\n\t"
    "s_add_u32 m0, s51, 73728\n\t"
    "v_mfma_f32_16x16x32_bf16 v[8:11], a[200:203], v[136:139], v[8:11]\n\t"
    "buffer_load_dwordx4 v52, s[36:39], s78 offen offset:0 nt lds\n\t"
    "v_mfma_f32_16x16x32_bf16 v[8:11], a[204:207], v[140:143], v[8:11]\n\t"
    "v_mfma_f32_16x16x32_bf16 v[12:15], a[208:211], v[128:131], v[36:39]\n\t"
    "v_mfma_f32_16x16x32_bf16 v[12:15], a[212:215], v[132:135], v[12:15]\n\t"
    "v_mfma_f32_16x16x32_bf16 v[12:15], a[216:219], v[136:139], v[12:15]\n\t"
    "v_mfma_f32_16x16x32_bf16 v[12:15], a[220:223], v[140:143], v[12:15]\n\t"
    "v_exp_f32 v8, v8\n\t"
    "v_mfma_f32_16x16x32_bf16 a[232:235], a[248:251], v[16:19], a[232:235]\n\t"
    "v_exp_f32 v9, v9\n\t"
    "v_mfma_f32_16x16x32_bf16 a[64:67], v[224:227], v[16:19], a[64:67]\n\t"
    "v_exp_f32 v10, v10\n\t"
    "v_mfma_f32_16x16x32_bf16 a[68:71], v[228:231], v[16:19], a[68:71]\n\t"
    "v_exp_f32 v11, v11\n\t"
    "v_mfma_f32_16x16x32_bf16 a[72:75], v[232:235], v[16:19], a[72:75]\n\t"
    "v_exp_f32 v12, v12\n\t"
    "v_mfma_f32_16x16x32_bf16 a[76:79], v[236:239], v[16:19], a[76:79]\n\t"
    "v_exp_f32 v13, v13\n\t"
    "v_mfma_f32_16x16x32_bf16 a[80:83], v[240:243], v[16:19], a[80:83]\n\t"
    "v_exp_f32 v14, v14\n\t"
    "v_mfma_f32_16x16x32_bf16 a[84:87], v[244:247], v[16:19], a[84:87]\n\t"
    "v_exp_f32 v15, v15\n\t"
    "v_mfma_f32_16x16x32_bf16 a[88:91], v[248:251], v[16:19], a[88:91]\n\t"
    "v_cvt_pk_bf16_f32 v20, v8, v9\n\t"
    "v_cvt_pk_bf16_f32 v21, v10, v11\n\t"
    "v_mfma_f32_16x16x32_bf16 a[92:95], v[252:255], v[16:19], a[92:95]\n\t"
    "v_cvt_pk_bf16_f32 v22, v12, v13\n\t"
    "v_cvt_pk_bf16_f32 v23, v14, v15\n\t"
    "v_mfma_f32_16x16x32_bf16 v[0:3], a[192:195], v[144:147], v[40:43]\n\t"
    "s_waitcnt vmcnt(9)\n\t"
    "v_lshlrev_b32 v64, 16, v176\n\t"
    "v_and_b32 v65, s49, v176\n\t"
    "v_mfma_f32_16x16x32_bf16 v[0:3], a[196:199], v[148:151], v[0:3]\n\t"
    "v_mul_f32 v64, s48, v64\n\t"
    "v_mul_f32 v65, s48, v65\n\t"
    "v_mfma_f32_16x16x32_bf16 v[0:3], a[200:203], v[152:155], v[0:3]\n\t"
    "v_cvt_pk_bf16_f32 v176, v64, v65\n\t"
    "v_lshlrev_b32 v66, 16, v177\n\t"
    "v_mfma_f32_16x16x32_bf16 v[0:3], a[204:207], v[156:159], v[0:3]\n\t"
    "v_and_b32 v67, s49, v177\n\t"
    "v_mul_f32 v66, s48, v66\n\t"
    "v_mfma_f32_16x16x32_bf16 v[4:7], a[208:211], v[144:147], v[40:43]\n\t"
    "v_mul_f32 v67, s48, v67\n\t"
    "v_cvt_pk_bf16_f32 v177, v66, v67\n\t"
    "v_mfma_f32_16x16x32_bf16 v[4:7], a[212:215], v[148:151], v[4:7]\n\t"
    "buffer_load_dwordx4 v53, s[36:39], s78 offen offset:1024 nt lds\n\t"
    "v_mfma_f32_16x16x32_bf16 v[4:7], a[216:219], v[152:155], v[4:7]\n\t"
    "v_lshlrev_b32 v68, 16, v178\n\t"
    "v_and_b32 v69, s49, v178\n\t"
    "v_mfma_f32_16x16x32_bf16 v[4:7], a[220:223], v[156:159], v[4:7]\n\t"
    "v_exp_f32 v0, v0\n\t"
    "v_mfma_f32_16x16x32_bf16 a[236:239], a[248:251], v[20:23], a[236:239]\n\t"
    "s_add_u32 s16, s16, 2048\n\ts_addc_u32 s17, s17, 0\n\t"
    "v_exp_f32 v1, v1\n\t"
    "v_mfma_f32_16x16x32_bf16 a[96:99], v[224:227], v[20:23], a[96:99]\n\t"
    "v_exp_f32 v2, v2\n\t"
    "v_mfma_f32_16x16x32_bf16 a[100:103], v[228:231], v[20:23], a[100:103]\n\t"
    "global_load_dword a255, v55, s[16:17]\n\t"
    "v_cvt_pk_bf16_f32 v16, v0, v1\n\t"
    "v_mfma_f32_16x16x32_bf16 a[104:107], v[232:235], v[20:23], a[104:107]\n\t"
    "v_exp_f32 v3, v3\n\t"
    "v_mfma_f32_16x16x32_bf16 a[108:111], v[236:239], v[20:23], a[108:111]\n\t"
    "v_exp_f32 v4, v4\n\t"
    "v_mfma_f32_16x16x32_bf16 a[112:115], v[240:243], v[20:23], a[112:115]\n\t"
    "v_exp_f32 v5, v5\n\t"
    "v_mfma_f32_16x16x32_bf16 a[116:119], v[244:247], v[20:23], a[116:119]\n\t"
    "v_exp_f32 v6, v6\n\t"
    "v_mfma_f32_16x16x32_bf16 a[120:123], v[248:251], v[20:23], a[120:123]\n\t"
    "v_exp_f32 v7, v7\n\t"
    "v_mfma_f32_16x16x32_bf16 a[124:127], v[252:255], v[20:23], a[124:127]\n\t"
    "v_cvt_pk_bf16_f32 v17, v2, v3\n\t"
    "v_cvt_pk_bf16_f32 v18, v4, v5\n\t"
    "v_mfma_f32_16x16x32_bf16 v[8:11], a[192:195], v[160:163], v[44:47]\n\t"
    "s_barrier\n\t"
    "ds_read_b128 a[192:195], v48 offset:57344\n\t"
    "v_cvt_pk_bf16_f32 v19, v6, v7\n\t"
    "v_mfma_f32_16x16x32_bf16 v[8:11], a[196:199], v[164:167], v[8:11]\n\t"
    "ds_read_b128 a[196:199], v48 offset:57600\n\t"
    "v_mul_f32 v68, s48, v68\n\t"
    "v_mfma_f32_16x16x32_bf16 v[8:11], a[200:203], v[168:171], v[8:11]\n\t"
    "ds_read_b128 a[200:203], v48 offset:57856\n\t"
    "v_mul_f32 v69, s48, v69\n\t"
    "v_mfma_f32_16x16x32_bf16 v[8:11], a[204:207], v[172:175], v[8:11]\n\t"
    "ds_read_b128 a[204:207], v48 offset:58112\n\t"
    "v_cvt_pk_bf16_f32 v178, v68, v69\n\t"
    "v_mfma_f32_16x16x32_bf16 v[12:15], a[208:211], v[160:163], v[44:47]\n\t"
    "ds_read_b128 a[208:211], v48 offset:61440\n\t"
    "v_lshlrev_b32 v70, 16, v179\n\t"
    "v_mfma_f32_16x16x32_bf16 v[12:15], a[212:215], v[164:167], v[12:15]\n\t"
    "ds_read_b128 a[212:215], v48 offset:61696\n\t"
    "v_and_b32 v71, s49, v179\n\t"
    "v_mfma_f32_16x16x32_bf16 v[12:15], a[216:219], v[168:171], v[12:15]\n\t"
    "ds_read_b128 a[216:219], v48 offset:61952\n\t"
    "v_mul_f32 v70, s48, v70\n\t"
    "v_mfma_f32_16x16x32_bf16 v[12:15], a[220:223], v[172:175], v[12:15]\n\t"
    "s_add_u32 m0, s51, 155648\n\t"
    "ds_read_b128 a[220:223], v48 offset:62208\n\t"
    "v_mul_f32 v71, s48, v71\n\t"
    "v_mfma_f32_16x16x32_bf16 a[240:243], a[248:251], v[16:19], a[240:243]\n\t"
    "buffer_load_dwordx4 v52, s[40:43], s78 offen offset:0 nt lds\n\t"
    "v_mfma_f32_16x16x32_bf16 a[128:131], v[224:227], v[16:19], a[128:131]\n\t"
    "v_exp_f32 v8, v8\n\t"
    "v_mfma_f32_16x16x32_bf16 a[132:135], v[228:231], v[16:19], a[132:135]\n\t"
    "v_exp_f32 v9, v9\n\t"
    "v_mfma_f32_16x16x32_bf16 a[136:139], v[232:235], v[16:19], a[136:139]\n\t"
    "v_exp_f32 v10, v10\n\t"
    "v_mfma_f32_16x16x32_bf16 a[140:143], v[236:239], v[16:19], a[140:143]\n\t"
    "v_exp_f32 v11, v11\n\t"
    "v_mfma_f32_16x16x32_bf16 a[144:147], v[240:243], v[16:19], a[144:147]\n\t"
    "v_exp_f32 v12, v12\n\t"
    "v_mfma_f32_16x16x32_bf16 a[148:151], v[244:247], v[16:19], a[148:151]\n\t"
    "v_exp_f32 v13, v13\n\t"
    "v_mfma_f32_16x16x32_bf16 a[152:155], v[248:251], v[16:19], a[152:155]\n\t"
    "v_exp_f32 v14, v14\n\t"
    "v_mfma_f32_16x16x32_bf16 a[156:159], v[252:255], v[16:19], a[156:159]\n\t"
    "s_waitcnt lgkmcnt(0)\n\t"
    "v_exp_f32 v15, v15\n\t"
    "v_mfma_f32_16x16x32_bf16 v[0:3], a[192:195], v[80:83], v[24:27]\n\t"
    "v_cvt_pk_bf16_f32 v20, v8, v9\n\t"
    "v_cvt_pk_bf16_f32 v21, v10, v11\n\t"
    "v_mfma_f32_16x16x32_bf16 v[0:3], a[196:199], v[84:87], v[0:3]\n\t"
    "v_cvt_pk_bf16_f32 v22, v12, v13\n\t"
    "v_cvt_pk_bf16_f32 v23, v14, v15\n\t"
    "v_mfma_f32_16x16x32_bf16 v[0:3], a[200:203], v[88:91], v[0:3]\n\t"
    "s_waitcnt vmcnt(11)\n\t"
    "v_cvt_pk_bf16_f32 v179, v70, v71\n\t"
    "v_lshlrev_b32 v64, 16, v180\n\t"
    "v_mfma_f32_16x16x32_bf16 v[0:3], a[204:207], v[92:95], v[0:3]\n\t"
    "v_and_b32 v65, s49, v180\n\t"
    "v_mul_f32 v64, s48, v64\n\t"
    "v_mfma_f32_16x16x32_bf16 v[4:7], a[208:211], v[80:83], v[24:27]\n\t"
    "v_mul_f32 v65, s48, v65\n\t"
    "v_cvt_pk_bf16_f32 v180, v64, v65\n\t"
    "v_mfma_f32_16x16x32_bf16 v[4:7], a[212:215], v[84:87], v[4:7]\n\t"
    "v_lshlrev_b32 v66, 16, v181\n\t"
    "v_and_b32 v67, s49, v181\n\t"
    "v_mfma_f32_16x16x32_bf16 v[4:7], a[216:219], v[88:91], v[4:7]\n\t"
    "v_mul_f32 v66, s48, v66\n\t"
    "v_mul_f32 v67, s48, v67\n\t"
    "v_mfma_f32_16x16x32_bf16 v[4:7], a[220:223], v[92:95], v[4:7]\n\t"
    "v_exp_f32 v0, v0\n\t"
    "v_mfma_f32_16x16x32_bf16 a[244:247], a[248:251], v[20:23], a[244:247]\n\t"
    "v_exp_f32 v1, v1\n\t"
    "v_mfma_f32_16x16x32_bf16 a[160:163], v[224:227], v[20:23], a[160:163]\n\t"
    "ds_read_b64_tr_b16 v[224:225], v50 offset:57344\n\t"
    "ds_read_b64_tr_b16 v[226:227], v50 offset:61440\n\t"
    "v_mfma_f32_16x16x32_bf16 a[164:167], v[228:231], v[20:23], a[164:167]\n\t"
    "ds_read_b64_tr_b16 v[228:229], v50 offset:57352\n\t"
    "ds_read_b64_tr_b16 v[230:231], v50 offset:61448\n\t"
    "v_mfma_f32_16x16x32_bf16 a[168:171], v[232:235], v[20:23], a[168:171]\n\t"
    "buffer_load_dwordx4 v53, s[40:43], s78 offen offset:1024 nt lds\n\t"
    "v_mfma_f32_16x16x32_bf16 a[172:175], v[236:239], v[20:23], a[172:175]\n\t"
    "ds_read_b64_tr_b16 v[232:233], v50 offset:57600\n\t"
    "ds_read_b64_tr_b16 v[234:235], v50 offset:61696\n\t"
    "v_mfma_f32_16x16x32_bf16 a[176:179], v[240:243], v[20:23], a[176:179]\n\t"
    "ds_read_b64_tr_b16 v[236:237], v50 offset:57608\n\t"
    "ds_read_b64_tr_b16 v[238:239], v50 offset:61704\n\t"
    "v_mfma_f32_16x16x32_bf16 a[180:183], v[244:247], v[20:23], a[180:183]\n\t"
    "v_exp_f32 v2, v2\n\t"
    "v_mfma_f32_16x16x32_bf16 a[184:187], v[248:251], v[20:23], a[184:187]\n\t"
    "v_exp_f32 v3, v3\n\t"
    "v_mfma_f32_16x16x32_bf16 a[188:191], v[252:255], v[20:23], a[188:191]\n\t"
    "v_exp_f32 v4, v4\n\t"
    "v_mfma_f32_16x16x32_bf16 v[8:11], a[192:195], v[96:99], v[28:31]\n\t"
    "v_exp_f32 v5, v5\n\t"
    "v_mfma_f32_16x16x32_bf16 v[8:11], a[196:199], v[100:103], v[8:11]\n\t"
    "v_exp_f32 v6, v6\n\t"
    "v_mfma_f32_16x16x32_bf16 v[8:11], a[200:203], v[104:107], v[8:11]\n\t"
    "v_exp_f32 v7, v7\n\t"
    "v_mfma_f32_16x16x32_bf16 v[8:11], a[204:207], v[108:111], v[8:11]\n\t"
    "ds_read_b64_tr_b16 v[240:241], v50 offset:57856\n\t"
    "ds_read_b64_tr_b16 v[242:243], v50 offset:61952\n\t"
    "v_mfma_f32_16x16x32_bf16 v[12:15], a[208:211], v[96:99], v[28:31]\n\t"
    "v_cvt_pk_bf16_f32 v16, v0, v1\n\t"
    "v_cvt_pk_bf16_f32 v17, v2, v3\n\t"
    "v_mfma_f32_16x16x32_bf16 v[12:15], a[212:215], v[100:103], v[12:15]\n\t"
    "v_cvt_pk_bf16_f32 v18, v4, v5\n\t"
    "v_cvt_pk_bf16_f32 v19, v6, v7\n\t"
    "v_mfma_f32_16x16x32_bf16 v[12:15], a[216:219], v[104:107], v[12:15]\n\t"
    "ds_read_b64_tr_b16 v[244:245], v50 offset:57864\n\t"
    "ds_read_b64_tr_b16 v[246:247], v50 offset:61960\n\t"
    "v_mfma_f32_16x16x32_bf16 v[12:15], a[220:223], v[108:111], v[12:15]\n\t"
    "ds_read_b64_tr_b16 v[248:249], v50 offset:58112\n\t"
    "ds_read_b64_tr_b16 v[250:251], v50 offset:62208\n\t"
    "v_mfma_f32_16x16x32_bf16 a[224:227], a[248:251], v[16:19], a[224:227]\n\t"
    "s_waitcnt lgkmcnt(6)\n\t"
    "ds_read_b64_tr_b16 v[252:253], v50 offset:58120\n\t"
    "ds_read_b64_tr_b16 v[254:255], v50 offset:62216\n\t"
    "v_mfma_f32_16x16x32_bf16 a[0:3], v[224:227], v[16:19], a[0:3]\n\t"
    "v_exp_f32 v8, v8\n\t"
    "v_mfma_f32_16x16x32_bf16 a[4:7], v[228:231], v[16:19], a[4:7]\n\t"
    "v_exp_f32 v9, v9\n\t"
    "v_mfma_f32_16x16x32_bf16 a[8:11], v[232:235], v[16:19], a[8:11]\n\t"
    "v_exp_f32 v10, v10\n\t"
    "v_mfma_f32_16x16x32_bf16 a[12:15], v[236:239], v[16:19], a[12:15]\n\t"
    "s_waitcnt lgkmcnt(6)\n\t"
    "v_exp_f32 v11, v11\n\t"
    "v_mfma_f32_16x16x32_bf16 a[16:19], v[240:243], v[16:19], a[16:19]\n\t"
    "s_waitcnt lgkmcnt(4)\n\t"
    "v_exp_f32 v12, v12\n\t"
    "v_mfma_f32_16x16x32_bf16 a[20:23], v[244:247], v[16:19], a[20:23]\n\t"
    "s_waitcnt lgkmcnt(2)\n\t"
    "buffer_load_dwordx4 v[192:195], v54, s[28:31], s59 offen offset:0 nt\n\t"
    "v_cvt_pk_bf16_f32 v20, v8, v9\n\t"
    "v_mfma_f32_16x16x32_bf16 a[24:27], v[248:251], v[16:19], a[24:27]\n\t"
    "s_waitcnt lgkmcnt(0)\n\t"
    "v_exp_f32 v13, v13\n\t"
    "v_mfma_f32_16x16x32_bf16 a[28:31], v[252:255], v[16:19], a[28:31]\n\t"
    "v_exp_f32 v14, v14\n\t"
    "v_mfma_f32_16x16x32_bf16 v[0:3], a[192:195], v[112:115], v[32:35]\n\t"
    "v_exp_f32 v15, v15\n\t"
    "v_mfma_f32_16x16x32_bf16 v[0:3], a[196:199], v[116:119], v[0:3]\n\t"
    "v_cvt_pk_bf16_f32 v21, v10, v11\n\t"
    "v_cvt_pk_bf16_f32 v22, v12, v13\n\t"
    "v_mfma_f32_16x16x32_bf16 v[0:3], a[200:203], v[120:123], v[0:3]\n\t"
    "v_cvt_pk_bf16_f32 v181, v66, v67\n\t"
    "v_cvt_pk_bf16_f32 v23, v14, v15\n\t"
    "v_mfma_f32_16x16x32_bf16 v[0:3], a[204:207], v[124:127], v[0:3]\n\t"
    "v_lshlrev_b32 v68, 16, v182\n\t"
    "v_and_b32 v69, s49, v182\n\t"
    "v_mfma_f32_16x16x32_bf16 v[4:7], a[208:211], v[112:115], v[32:35]\n\t"
    "v_mul_f32 v68, s48, v68\n\t"
    "v_mul_f32 v69, s48, v69\n\t"
    "v_mfma_f32_16x16x32_bf16 v[4:7], a[212:215], v[116:119], v[4:7]\n\t"
    "v_cvt_pk_bf16_f32 v182, v68, v69\n\t"
    "v_lshlrev_b32 v70, 16, v183\n\t"
    "v_mfma_f32_16x16x32_bf16 v[4:7], a[216:219], v[120:123], v[4:7]\n\t"
    "v_and_b32 v71, s49, v183\n\t"
    "v_mul_f32 v70, s48, v70\n\t"
    "v_mfma_f32_16x16x32_bf16 v[4:7], a[220:223], v[124:127], v[4:7]\n\t"
    "v_exp_f32 v0, v0\n\t"
    "v_mfma_f32_16x16x32_bf16 a[228:231], a[248:251], v[20:23], a[228:231]\n\t"
    "v_exp_f32 v1, v1\n\t"
    "v_mfma_f32_16x16x32_bf16 a[32:35], v[224:227], v[20:23], a[32:35]\n\t"
    "v_exp_f32 v2, v2\n\t"
    "v_mfma_f32_16x16x32_bf16 a[36:39], v[228:231], v[20:23], a[36:39]\n\t"
    "v_exp_f32 v3, v3\n\t"
    "v_mfma_f32_16x16x32_bf16 a[40:43], v[232:235], v[20:23], a[40:43]\n\t"
    "v_exp_f32 v4, v4\n\t"
    "v_mfma_f32_16x16x32_bf16 a[44:47], v[236:239], v[20:23], a[44:47]\n\t"
    "v_exp_f32 v5, v5\n\t"
    "v_mfma_f32_16x16x32_bf16 a[48:51], v[240:243], v[20:23], a[48:51]\n\t"
    "v_exp_f32 v6, v6\n\t"
    "v_mfma_f32_16x16x32_bf16 a[52:55], v[244:247], v[20:23], a[52:55]\n\t"
    "v_exp_f32 v7, v7\n\t"
    "v_mfma_f32_16x16x32_bf16 a[56:59], v[248:251], v[20:23], a[56:59]\n\t"
    "v_cvt_pk_bf16_f32 v16, v0, v1\n\t"
    "v_cvt_pk_bf16_f32 v17, v2, v3\n\t"
    "v_mfma_f32_16x16x32_bf16 a[60:63], v[252:255], v[20:23], a[60:63]\n\t"
    "v_cvt_pk_bf16_f32 v18, v4, v5\n\t"
    "v_cvt_pk_bf16_f32 v19, v6, v7\n\t"
    "v_mfma_f32_16x16x32_bf16 v[8:11], a[192:195], v[128:131], v[36:39]\n\t"
    "v_mul_f32 v71, s48, v71\n\t"
    "v_cvt_pk_bf16_f32 v183, v70, v71\n\t"
    "v_mfma_f32_16x16x32_bf16 v[8:11], a[196:199], v[132:135], v[8:11]\n\t"
    "v_mfma_f32_16x16x32_bf16 v[8:11], a[200:203], v[136:139], v[8:11]\n\t"
    "v_mfma_f32_16x16x32_bf16 v[8:11], a[204:207], v[140:143], v[8:11]\n\t"
    "buffer_load_dwordx4 v[196:199], v54, s[28:31], s59 offen offset:64 nt\n\t"
    "v_mfma_f32_16x16x32_bf16 v[12:15], a[208:211], v[128:131], v[36:39]\n\t"
    "v_mfma_f32_16x16x32_bf16 v[12:15], a[212:215], v[132:135], v[12:15]\n\t"
    "v_mfma_f32_16x16x32_bf16 v[12:15], a[216:219], v[136:139], v[12:15]\n\t"
    "v_mfma_f32_16x16x32_bf16 v[12:15], a[220:223], v[140:143], v[12:15]\n\t"
    "v_exp_f32 v8, v8\n\t"
    "v_mfma_f32_16x16x32_bf16 a[232:235], a[248:251], v[16:19], a[232:235]\n\t"
    "v_exp_f32 v9, v9\n\t"
    "v_mfma_f32_16x16x32_bf16 a[64:67], v[224:227], v[16:19], a[64:67]\n\t"
    "v_exp_f32 v10, v10\n\t"
    "v_mfma_f32_16x16x32_bf16 a[68:71], v[228:231], v[16:19], a[68:71]\n\t"
    "v_exp_f32 v11, v11\n\t"
    "v_mfma_f32_16x16x32_bf16 a[72:75], v[232:235], v[16:19], a[72:75]\n\t"
    "v_exp_f32 v12, v12\n\t"
    "v_mfma_f32_16x16x32_bf16 a[76:79], v[236:239], v[16:19], a[76:79]\n\t"
    "v_exp_f32 v13, v13\n\t"
    "v_mfma_f32_16x16x32_bf16 a[80:83], v[240:243], v[16:19], a[80:83]\n\t"
    "v_exp_f32 v14, v14\n\t"
    "v_mfma_f32_16x16x32_bf16 a[84:87], v[244:247], v[16:19], a[84:87]\n\t"
    "v_exp_f32 v15, v15\n\t"
    "v_mfma_f32_16x16x32_bf16 a[88:91], v[248:251], v[16:19], a[88:91]\n\t"
    "v_cvt_pk_bf16_f32 v20, v8, v9\n\t"
    "v_cvt_pk_bf16_f32 v21, v10, v11\n\t"
    "v_mfma_f32_16x16x32_bf16 a[92:95], v[252:255], v[16:19], a[92:95]\n\t"
    "v_cvt_pk_bf16_f32 v22, v12, v13\n\t"
    "v_cvt_pk_bf16_f32 v23, v14, v15\n\t"
    "v_mfma_f32_16x16x32_bf16 v[0:3], a[192:195], v[144:147], v[40:43]\n\t"
    "v_mfma_f32_16x16x32_bf16 v[0:3], a[196:199], v[148:151], v[0:3]\n\t"
    "v_mfma_f32_16x16x32_bf16 v[0:3], a[200:203], v[152:155], v[0:3]\n\t"
    "v_mfma_f32_16x16x32_bf16 v[0:3], a[204:207], v[156:159], v[0:3]\n\t"
    "v_mfma_f32_16x16x32_bf16 v[4:7], a[208:211], v[144:147], v[40:43]\n\t"
    "v_mfma_f32_16x16x32_bf16 v[4:7], a[212:215], v[148:151], v[4:7]\n\t"
    "v_mfma_f32_16x16x32_bf16 v[4:7], a[216:219], v[152:155], v[4:7]\n\t"
    "v_mfma_f32_16x16x32_bf16 v[4:7], a[220:223], v[156:159], v[4:7]\n\t"
    "v_exp_f32 v0, v0\n\t"
    "v_mfma_f32_16x16x32_bf16 a[236:239], a[248:251], v[20:23], a[236:239]\n\t"
    "s_add_u32 m0, s51, 40960\n\t"
    "v_exp_f32 v1, v1\n\t"
    "v_mfma_f32_16x16x32_bf16 a[96:99], v[224:227], v[20:23], a[96:99]\n\t"
    "buffer_load_dwordx4 v52, s[36:39], s79 offen offset:0 nt lds\n\t"
    "v_mfma_f32_16x16x32_bf16 a[100:103], v[228:231], v[20:23], a[100:103]\n\t"
    "v_exp_f32 v2, v2\n\t"
    "v_mfma_f32_16x16x32_bf16 a[104:107], v[232:235], v[20:23], a[104:107]\n\t"
    "v_exp_f32 v3, v3\n\t"
    "v_mfma_f32_16x16x32_bf16 a[108:111], v[236:239], v[20:23], a[108:111]\n\t"
    "v_exp_f32 v4, v4\n\t"
    "v_mfma_f32_16x16x32_bf16 a[112:115], v[240:243], v[20:23], a[112:115]\n\t"
    "v_exp_f32 v5, v5\n\t"
    "v_mfma_f32_16x16x32_bf16 a[116:119], v[244:247], v[20:23], a[116:119]\n\t"
    "v_exp_f32 v6, v6\n\t"
    "v_mfma_f32_16x16x32_bf16 a[120:123], v[248:251], v[20:23], a[120:123]\n\t"
    "v_exp_f32 v7, v7\n\t"
    "v_mfma_f32_16x16x32_bf16 a[124:127], v[252:255], v[20:23], a[124:127]\n\t"
    "s_waitcnt vmcnt(10)\n\t"
    "v_cvt_pk_bf16_f32 v16, v0, v1\n\t"
    "v_cvt_pk_bf16_f32 v17, v2, v3\n\t"
    "v_mfma_f32_16x16x32_bf16 v[8:11], a[192:195], v[160:163], v[44:47]\n\t"
    "s_barrier\n\t"
    "v_cvt_pk_bf16_f32 v18, v4, v5\n\t"
    "v_cvt_pk_bf16_f32 v19, v6, v7\n\t"
    "v_mfma_f32_16x16x32_bf16 v[8:11], a[196:199], v[164:167], v[8:11]\n\t"
    "s_add_u32 s16, s16, 2048\n\ts_addc_u32 s17, s17, 0\n\t"
    "ds_read_b128 a[192:195], v49 offset:0\n\t"
    "v_mfma_f32_16x16x32_bf16 v[8:11], a[200:203], v[168:171], v[8:11]\n\t"
    "s_waitcnt vmcnt(9)\n\t"
    "ds_read_b128 a[196:199], v49 offset:256\n\t"
    "v_lshlrev_b32 v64, 16, v184\n\t"
    "v_mfma_f32_16x16x32_bf16 v[8:11], a[204:207], v[172:175], v[8:11]\n\t"
    "ds_read_b128 a[200:203], v49 offset:512\n\t"
    "global_load_dword a255, v55, s[16:17]\n\t"
    "v_mfma_f32_16x16x32_bf16 v[12:15], a[208:211], v[160:163], v[44:47]\n\t"
    "ds_read_b128 a[204:207], v49 offset:768\n\t"
    "v_and_b32 v65, s49, v184\n\t"
    "v_mfma_f32_16x16x32_bf16 v[12:15], a[212:215], v[164:167], v[12:15]\n\t"
    "ds_read_b128 a[208:211], v49 offset:4096\n\t"
    "v_mul_f32 v64, s48, v64\n\t"
    "v_mfma_f32_16x16x32_bf16 v[12:15], a[216:219], v[168:171], v[12:15]\n\t"
    "ds_read_b128 a[212:215], v49 offset:4352\n\t"
    "v_mul_f32 v65, s48, v65\n\t"
    "v_mfma_f32_16x16x32_bf16 v[12:15], a[220:223], v[172:175], v[12:15]\n\t"
    "ds_read_b128 a[216:219], v49 offset:4608\n\t"
    "v_cvt_pk_bf16_f32 v184, v64, v65\n\t"
    "v_mfma_f32_16x16x32_bf16 a[240:243], a[248:251], v[16:19], a[240:243]\n\t"
    "ds_read_b128 a[220:223], v49 offset:4864\n\t"
    "v_lshlrev_b32 v66, 16, v185\n\t"
    "v_mfma_f32_16x16x32_bf16 a[128:131], v[224:227], v[16:19], a[128:131]\n\t"
    "v_exp_f32 v8, v8\n\t"
    "v_mfma_f32_16x16x32_bf16 a[132:135], v[228:231], v[16:19], a[132:135]\n\t"
    "v_exp_f32 v9, v9\n\t"
    "v_mfma_f32_16x16x32_bf16 a[136:139], v[232:235], v[16:19], a[136:139]\n\t"
    "v_exp_f32 v10, v10\n\t"
    "v_mfma_f32_16x16x32_bf16 a[140:143], v[236:239], v[16:19], a[140:143]\n\t"
    "buffer_load_dwordx4 v53, s[36:39], s79 offen offset:1024 nt lds\n\t"
    "v_mfma_f32_16x16x32_bf16 a[144:147], v[240:243], v[16:19], a[144:147]\n\t"
    "PAD5_%=:\n\ts_cbranch_vccnz PAD9_%=\n\t"
    "v_exp_f32 v11, v11\n\t"
    "v_mfma_f32_16x16x32_bf16 a[148:151], v[244:247], v[16:19], a[148:151]\n\t"
    "v_exp_f32 v12, v12\n\t"
    "v_mfma_f32_16x16x32_bf16 a[152:155], v[248:251], v[16:19], a[152:155]\n\t"
    "v_exp_f32 v13, v13\n\t"
    "v_mfma_f32_16x16x32_bf16 a[156:159], v[252:255], v[16:19], a[156:159]\n\t"
    "s_waitcnt lgkmcnt(0)\n\t"
    "v_exp_f32 v14, v14\n\t"
    "v_mfma_f32_16x16x32_bf16 v[0:3], a[192:195], v[80:83], v[24:27]\n\t"
    "v_exp_f32 v15, v15\n\t"
    "v_mfma_f32_16x16x32_bf16 v[0:3], a[196:199], v[84:87], v[0:3]\n\t"
    "v_cvt_pk_bf16_f32 v20, v8, v9\n\t"
    "v_cvt_pk_bf16_f32 v21, v10, v11\n\t"
    "v_mfma_f32_16x16x32_bf16 v[0:3], a[200:203], v[88:91], v[0:3]\n\t"
    "v_cvt_pk_bf16_f32 v22, v12, v13\n\t"
    "v_cvt_pk_bf16_f32 v23, v14, v15\n\t"
    "v_mfma_f32_16x16x32_bf16 v[0:3], a[204:207], v[92:95], v[0:3]\n\t"
    "v_and_b32 v67, s49, v185\n\t"
    "v_mul_f32 v66, s48, v66\n\t"
    "v_mfma_f32_16x16x32_bf16 v[4:7], a[208:211], v[80:83], v[24:27]\n\t"
    "v_mul_f32 v67, s48, v67\n\t"
    "v_cvt_pk_bf16_f32 v185, v66, v67\n\t"
    "v_mfma_f32_16x16x32_bf16 v[4:7], a[212:215], v[84:87], v[4:7]\n\t"
    "v_lshlrev_b32 v68, 16, v186\n\t"
    "v_and_b32 v69, s49, v186\n\t"
    "v_mfma_f32_16x16x32_bf16 v[4:7], a[216:219], v[88:91], v[4:7]\n\t"
    "v_mul_f32 v68, s48, v68\n\t"
    "v_mul_f32 v69, s48, v69\n\t"
    "v_mfma_f32_16x16x32_bf16 v[4:7], a[220:223], v[92:95], v[4:7]\n\t"
    "v_exp_f32 v0, v0\n\t"
    "v_mfma_f32_16x16x32_bf16 a[244:247], a[248:251], v[20:23], a[244:247]\n\t"
    "v_exp_f32 v1, v1\n\t"
    "v_mfma_f32_16x16x32_bf16 a[160:163], v[224:227], v[20:23], a[160:163]\n\t"
    "ds_read_b64_tr_b16 v[224:225], v51 offset:0\n\t"
    "ds_read_b64_tr_b16 v[226:227], v51 offset:4096\n\t"
    "v_mfma_f32_16x16x32_bf16 a[164:167], v[228:231], v[20:23], a[164:167]\n\t"
    "ds_read_b64_tr_b16 v[228:229], v51 offset:8\n\t"
    "ds_read_b64_tr_b16 v[230:231], v51 offset:4104\n\t"
    "v_mfma_f32_16x16x32_bf16 a[168:171], v[232:235], v[20:23], a[168:171]\n\t"
    "ds_read_b64_tr_b16 v[232:233], v51 offset:256\n\t"
    "ds_read_b64_tr_b16 v[234:235], v51 offset:4352\n\t"
    "v_mfma_f32_16x16x32_bf16 a[172:175], v[236:239], v[20:23], a[172:175]\n\t"
    "ds_read_b64_tr_b16 v[236:237], v51 offset:264\n\t"
    "ds_read_b64_tr_b16 v[238:239], v51 offset:4360\n\t"
    "v_mfma_f32_16x16x32_bf16 a[176:179], v[240:243], v[20:23], a[176:179]\n\t"
    "v_exp_f32 v2, v2\n\t"
    "v_mfma_f32_16x16x32_bf16 a[180:183], v[244:247], v[20:23], a[180:183]\n\t"
    "s_add_u32 m0, s51, 122880\n\t"
    "v_exp_f32 v3, v3\n\t"
    "v_mfma_f32_16x16x32_bf16 a[184:187], v[248:251], v[20:23], a[184:187]\n\t"
    "buffer_load_dwordx4 v52, s[40:43], s79 offen offset:0 nt lds\n\t"
    "v_mfma_f32_16x16x32_bf16 a[188:191], v[252:255], v[20:23], a[188:191]\n\t"
    "v_exp_f32 v4, v4\n\t"
    "v_mfma_f32_16x16x32_bf16 v[8:11], a[192:195], v[96:99], v[28:31]\n\t"
    "v_exp_f32 v5, v5\n\t"
    "v_mfma_f32_16x16x32_bf16 v[8:11], a[196:199], v[100:103], v[8:11]\n\t"
    "v_exp_f32 v6, v6\n\t"
    "v_mfma_f32_16x16x32_bf16 v[8:11], a[200:203], v[104:107], v[8:11]\n\t"
    "v_exp_f32 v7, v7\n\t"
    "v_mfma_f32_16x16x32_bf16 v[8:11], a[204:207], v[108:111], v[8:11]\n\t"
    "ds_read_b64_tr_b16 v[240:241], v51 offset:512\n\t"
    "ds_read_b64_tr_b16 v[242:243], v51 offset:4608\n\t"
    "v_mfma_f32_16x16x32_bf16 v[12:15], a[208:211], v[96:99], v[28:31]\n\t"
    "v_cvt_pk_bf16_f32 v16, v0, v1\n\t"
    "v_cvt_pk_bf16_f32 v17, v2, v3\n\t"
    "v_mfma_f32_16x16x32_bf16 v[12:15], a[212:215], v[100:103], v[12:15]\n\t"
    "v_cvt_pk_bf16_f32 v18, v4, v5\n\t"
    "v_cvt_pk_bf16_f32 v19, v6, v7\n\t"
    "v_mfma_f32_16x16x32_bf16 v[12:15], a[216:219], v[104:107], v[12:15]\n\t"
    "ds_read_b64_tr_b16 v[244:245], v51 offset:520\n\t"
    "ds_read_b64_tr_b16 v[246:247], v51 offset:4616\n\t"
    "v_mfma_f32_16x16x32_bf16 v[12:15], a[220:223], v[108:111], v[12:15]\n\t"
    "ds_read_b64_tr_b16 v[248:249], v51 offset:768\n\t"
    "ds_read_b64_tr_b16 v[250:251], v51 offset:4864\n\t"
    "v_mfma_f32_16x16x32_bf16 a[224:227], a[248:251], v[16:19], a[224:227]\n\t"
    "s_waitcnt lgkmcnt(6)\n\t"
    "ds_read_b64_tr_b16 v[252:253], v51 offset:776\n\t"
    "ds_read_b64_tr_b16 v[254:255], v51 offset:4872\n\t"
    "v_mfma_f32_16x16x32_bf16 a[0:3], v[224:227], v[16:19], a[0:3]\n\t"
    "v_exp_f32 v8, v8\n\t"
    "v_mfma_f32_16x16x32_bf16 a[4:7], v[228:231], v[16:19], a[4:7]\n\t"
    "v_exp_f32 v9, v9\n\t"
    "v_mfma_f32_16x16x32_bf16 a[8:11], v[232:235], v[16:19], a[8:11]\n\t"
    "v_exp_f32 v10, v10\n\t"
    "v_mfma_f32_16x16x32_bf16 a[12:15], v[236:239], v[16:19], a[12:15]\n\t"
    "s_waitcnt lgkmcnt(6)\n\t"
    "v_exp_f32 v11, v11\n\t"
    "v_mfma_f32_16x16x32_bf16 a[16:19], v[240:243], v[16:19], a[16:19]\n\t"
    "s_waitcnt lgkmcnt(4)\n\t"
    "v_exp_f32 v12, v12\n\t"
    "v_mfma_f32_16x16x32_bf16 a[20:23], v[244:247], v[16:19], a[20:23]\n\t"
    "s_waitcnt lgkmcnt(2)\n\t"
    "v_exp_f32 v13, v13\n\t"
    "v_mfma_f32_16x16x32_bf16 a[24:27], v[248:251], v[16:19], a[24:27]\n\t"
    "s_waitcnt lgkmcnt(0)\n\t"
    "v_exp_f32 v14, v14\n\t"
    "v_mfma_f32_16x16x32_bf16 a[28:31], v[252:255], v[16:19], a[28:31]\n\t"
    "v_exp_f32 v15, v15\n\t"
    "v_mfma_f32_16x16x32_bf16 v[0:3], a[192:195], v[112:115], v[32:35]\n\t"
    "v_cvt_pk_bf16_f32 v20, v8, v9\n\t"
    "v_cvt_pk_bf16_f32 v21, v10, v11\n\t"
    "v_mfma_f32_16x16x32_bf16 v[0:3], a[196:199], v[116:119], v[0:3]\n\t"
    "v_cvt_pk_bf16_f32 v22, v12, v13\n\t"
    "v_cvt_pk_bf16_f32 v23, v14, v15\n\t"
    "v_mfma_f32_16x16x32_bf16 v[0:3], a[200:203], v[120:123], v[0:3]\n\t"
    "buffer_load_dwordx4 v53, s[40:43], s79 offen offset:1024 nt lds\n\t"
    "v_mfma_f32_16x16x32_bf16 v[0:3], a[204:207], v[124:127], v[0:3]\n\t"
    "v_cvt_pk_bf16_f32 v186, v68, v69\n\t"
    "v_lshlrev_b32 v70, 16, v187\n\t"
    "v_mfma_f32_16x16x32_bf16 v[4:7], a[208:211], v[112:115], v[32:35]\n\t"
    "v_and_b32 v71, s49, v187\n\t"
    "v_mul_f32 v70, s48, v70\n\t"
    "v_mfma_f32_16x16x32_bf16 v[4:7], a[212:215], v[116:119], v[4:7]\n\t"
    "v_mul_f32 v71, s48, v71\n\t"
    "v_cvt_pk_bf16_f32 v187, v70, v71\n\t"
    "v_mfma_f32_16x16x32_bf16 v[4:7], a[216:219], v[120:123], v[4:7]\n\t"
    "s_waitcnt vmcnt(12)\n\t"
    "v_lshlrev_b32 v64, 16, v188\n\t"
    "v_and_b32 v65, s49, v188\n\t"
    "v_mfma_f32_16x16x32_bf16 v[4:7], a[220:223], v[124:127], v[4:7]\n\t"
    "v_exp_f32 v0, v0\n\t"
    "v_mfma_f32_16x16x32_bf16 a[228:231], a[248:251], v[20:23], a[228:231]\n\t"
    "v_exp_f32 v1, v1\n\t"
    "v_mfma_f32_16x16x32_bf16 a[32:35], v[224:227], v[20:23], a[32:35]\n\t"
    "v_exp_f32 v2, v2\n\t"
    "v_mfma_f32_16x16x32_bf16 a[36:39], v[228:231], v[20:23], a[36:39]\n\t"
    "v_exp_f32 v3, v3\n\t"
    "v_mfma_f32_16x16x32_bf16 a[40:43], v[232:235], v[20:23], a[40:43]\n\t"
    "v_exp_f32 v4, v4\n\t"
    "v_mfma_f32_16x16x32_bf16 a[44:47], v[236:239], v[20:23], a[44:47]\n\t"
    "v_exp_f32 v5, v5\n\t"
    "v_mfma_f32_16x16x32_bf16 a[48:51], v[240:243], v[20:23], a[48:51]\n\t"
    "v_exp_f32 v6, v6\n\t"
    "v_mfma_f32_16x16x32_bf16 a[52:55], v[244:247], v[20:23], a[52:55]\n\t"
    "v_exp_f32 v7, v7\n\t"
    "v_mfma_f32_16x16x32_bf16 a[56:59], v[248:251], v[20:23], a[56:59]\n\t"
    "v_cvt_pk_bf16_f32 v16, v0, v1\n\t"
    "v_cvt_pk_bf16_f32 v17, v2, v3\n\t"
    "v_mfma_f32_16x16x32_bf16 a[60:63], v[252:255], v[20:23], a[60:63]\n\t"
    "v_cvt_pk_bf16_f32 v18, v4, v5\n\t"
    "v_cvt_pk_bf16_f32 v19, v6, v7\n\t"
    "v_mfma_f32_16x16x32_bf16 v[8:11], a[192:195], v[128:131], v[36:39]\n\t"
    "v_mul_f32 v64, s48, v64\n\t"
    "v_mul_f32 v65, s48, v65\n\t"
    "v_mfma_f32_16x16x32_bf16 v[8:11], a[196:199], v[132:135], v[8:11]\n\t"
    "v_cvt_pk_bf16_f32 v188, v64, v65\n\t"
    "v_lshlrev_b32 v66, 16, v189\n\t"
    "v_mfma_f32_16x16x32_bf16 v[8:11], a[200:203], v[136:139], v[8:11]\n\t"
    "v_and_b32 v67, s49, v189\n\t"
    "v_mul_f32 v66, s48, v66\n\t"
    "v_mfma_f32_16x16x32_bf16 v[8:11], a[204:207], v[140:143], v[8:11]\n\t"
    "v_mul_f32 v67, s48, v67\n\t"
    "v_cvt_pk_bf16_f32 v189, v66, v67\n\t"
    "v_mfma_f32_16x16x32_bf16 v[12:15], a[208:211], v[128:131], v[36:39]\n\t"
    "buffer_load_dwordx4 v[200:203], v54, s[28:31], s59 offen offset:128 nt\n\t"
    "v_lshlrev_b32 v68, 16, v190\n\t"
    "v_mfma_f32_16x16x32_bf16 v[12:15], a[212:215], v[132:135], v[12:15]\n\t"
    "v_and_b32 v69, s49, v190\n\t"
    "v_mul_f32 v68, s48, v68\n\t"
    "v_mfma_f32_16x16x32_bf16 v[12:15], a[216:219], v[136:139], v[12:15]\n\t"
    "v_mul_f32 v69, s48, v69\n\t"
    "v_cvt_pk_bf16_f32 v190, v68, v69\n\t"
    "v_mfma_f32_16x16x32_bf16 v[12:15], a[220:223], v[140:143], v[12:15]\n\t"
    "v_exp_f32 v8, v8\n\t"
    "v_mfma_f32_16x16x32_bf16 a[232:235], a[248:251], v[16:19], a[232:235]\n\t"
    "v_exp_f32 v9, v9\n\t"
    "v_mfma_f32_16x16x32_bf16 a[64:67], v[224:227], v[16:19], a[64:67]\n\t"
    "v_exp_f32 v10, v10\n\t"
    "v_mfma_f32_16x16x32_bf16 a[68:71], v[228:231], v[16:19], a[68:71]\n\t"
    "v_exp_f32 v11, v11\n\t"
    "v_mfma_f32_16x16x32_bf16 a[72:75], v[232:235], v[16:19], a[72:75]\n\t"
    "v_exp_f32 v12, v12\n\t"
    "v_mfma_f32_16x16x32_bf16 a[76:79], v[236:239], v[16:19], a[76:79]\n\t"
    "v_exp_f32 v13, v13\n\t"
    "v_mfma_f32_16x16x32_bf16 a[80:83], v[240:243], v[16:19], a[80:83]\n\t"
    "v_exp_f32 v14, v14\n\t"
    "v_mfma_f32_16x16x32_bf16 a[84:87], v[244:247], v[16:19], a[84:87]\n\t"
    "v_exp_f32 v15, v15\n\t"
    "v_mfma_f32_16x16x32_bf16 a[88:91], v[248:251], v[16:19], a[88:91]\n\t"
    "v_cvt_pk_bf16_f32 v20, v8, v9\n\t"
    "v_cvt_pk_bf16_f32 v21, v10, v11\n\t"
    "v_mfma_f32_16x16x32_bf16 a[92:95], v[252:255], v[16:19], a[92:95]\n\t"
    "v_cvt_pk_bf16_f32 v22, v12, v13\n\t"
    "v_cvt_pk_bf16_f32 v23, v14, v15\n\t"
    "v_mfma_f32_16x16x32_bf16 v[0:3], a[192:195], v[144:147], v[40:43]\n\t"
    "v_lshlrev_b32 v70, 16, v191\n\t"
    "v_and_b32 v71, s49, v191\n\t"
    "v_mfma_f32_16x16x32_bf16 v[0:3], a[196:199], v[148:151], v[0:3]\n\t"
    "v_mul_f32 v70, s48, v70\n\t"
    "v_mul_f32 v71, s48, v71\n\t"
    "v_mfma_f32_16x16x32_bf16 v[0:3], a[200:203], v[152:155], v[0:3]\n\t"
    "v_cvt_pk_bf16_f32 v191, v70, v71\n\t"
    "v_mfma_f32_16x16x32_bf16 v[0:3], a[204:207], v[156:159], v[0:3]\n\t"
    "v_mfma_f32_16x16x32_bf16 v[4:7], a[208:211], v[144:147], v[40:43]\n\t"
    "v_mfma_f32_16x16x32_bf16 v[4:7], a[212:215], v[148:151], v[4:7]\n\t"
    "v_mfma_f32_16x16x32_bf16 v[4:7], a[216:219], v[152:155], v[4:7]\n\t"
    "v_mfma_f32_16x16x32_bf16 v[4:7], a[220:223], v[156:159], v[4:7]\n\t"
    "v_exp_f32 v0, v0\n\t"
    "v_mfma_f32_16x16x32_bf16 a[236:239], a[248:251], v[20:23], a[236:239]\n\t"
    "v_exp_f32 v1, v1\n\t"
    "v_mfma_f32_16x16x32_bf16 a[96:99], v[224:227], v[20:23], a[96:99]\n\t"
    "v_exp_f32 v2, v2\n\t"
    "v_mfma_f32_16x16x32_bf16 a[100:103], v[228:231], v[20:23], a[100:103]\n\t"
    "buffer_load_dwordx4 v[204:207], v54, s[28:31], s59 offen offset:192 nt\n\t"
    "v_cvt_pk_bf16_f32 v16, v0, v1\n\t"
    "v_mfma_f32_16x16x32_bf16 a[104:107], v[232:235], v[20:23], a[104:107]\n\t"
    "v_exp_f32 v3, v3\n\t"
    "v_mfma_f32_16x16x32_bf16 a[108:111], v[236:239], v[20:23], a[108:111]\n\t"
    "v_exp_f32 v4, v4\n\t"
    "v_mfma_f32_16x16x32_bf16 a[112:115], v[240:243], v[20:23], a[112:115]\n\t"
    "v_exp_f32 v5, v5\n\t"
    "v_mfma_f32_16x16x32_bf16 a[116:119], v[244:247], v[20:23], a[116:119]\n\t"
    "v_exp_f32 v6, v6\n\t"
    "v_mfma_f32_16x16x32_bf16 a[120:123], v[248:251], v[20:23], a[120:123]\n\t"
    "v_exp_f32 v7, v7\n\t"
    "v_mfma_f32_16x16x32_bf16 a[124:127], v[252:255], v[20:23], a[124:127]\n\t"
    "s_waitcnt vmcnt(9)\n\t"
    "v_cvt_pk_bf16_f32 v17, v2, v3\n\t"
    "v_cvt_pk_bf16_f32 v18, v4, v5\n\t"
    "v_mfma_f32_16x16x32_bf16 v[8:11], a[192:195], v[160:163], v[44:47]\n\t"
    "s_barrier\n\t"
    "ds_read_b128 a[192:195], v49 offset:8192\n\t"
    "v_cvt_pk_bf16_f32 v19, v6, v7\n\t"
    "v_mfma_f32_16x16x32_bf16 v[8:11], a[196:199], v[164:167], v[8:11]\n\t"
    "ds_read_b128 a[196:199], v49 offset:8448\n\t"
    "v_mfma_f32_16x16x32_bf16 v[8:11], a[200:203], v[168:171], v[8:11]\n\t"
    "ds_read_b128 a[200:203], v49 offset:8704\n\t"
    "v_mfma_f32_16x16x32_bf16 v[8:11], a[204:207], v[172:175], v[8:11]\n\t"
    "ds_read_b128 a[204:207], v49 offset:8960\n\t"
    "v_mfma_f32_16x16x32_bf16 v[12:15], a[208:211], v[160:163], v[44:47]\n\t"
    "ds_read_b128 a[208:211], v49 offset:12288\n\t"
    "v_mfma_f32_16x16x32_bf16 v[12:15], a[212:215], v[164:167], v[12:15]\n\t"
    "ds_read_b128 a[212:215], v49 offset:12544\n\t"
    "v_mfma_f32_16x16x32_bf16 v[12:15], a[216:219], v[168:171], v[12:15]\n\t"
    "ds_read_b128 a[216:219], v49 offset:12800\n\t"
    "v_mfma_f32_16x16x32_bf16 v[12:15], a[220:223], v[172:175], v[12:15]\n\t"
    "v_exp_f32 v8, v8\n\t"
    "v_mfma_f32_16x16x32_bf16 a[240:243], a[248:251], v[16:19], a[240:243]\n\t"
    "v_exp_f32 v9, v9\n\t"
    "v_mfma_f32_16x16x32_bf16 a[128:131], v[224:227], v[16:19], a[128:131]\n\t"
    "ds_read_b128 a[220:223], v49 offset:13056\n\t"
    "v_cvt_pk_bf16_f32 v20, v8, v9\n\t"
    "v_mfma_f32_16x16x32_bf16 a[132:135], v[228:231], v[16:19], a[132:135]\n\t"
    "v_exp_f32 v10, v10\n\t"
    "v_mfma_f32_16x16x32_bf16 a[136:139], v[232:235], v[16:19], a[136:139]\n\t"
    "s_add_u32 s16, s16, 2048\n\ts_addc_u32 s17, s17, 0\n\t"
    "v_exp_f32 v11, v11\n\t"
    "v_mfma_f32_16x16x32_bf16 a[140:143], v[236:239], v[16:19], a[140:143]\n\t"
    "v_exp_f32 v12, v12\n\t"
    "v_mfma_f32_16x16x32_bf16 a[144:147], v[240:243], v[16:19], a[144:147]\n\t"
    "global_load_dword a255, v55, s[16:17]\n\t"
    "v_cvt_pk_bf16_f32 v21, v10, v11\n\t"
    "v_mfma_f32_16x16x32_bf16 a[148:151], v[244:247], v[16:19], a[148:151]\n\t"
    "v_exp_f32 v13, v13\n\t"
    "v_mfma_f32_16x16x32_bf16 a[152:155], v[248:251], v[16:19], a[152:155]\n\t"
    "v_exp_f32 v14, v14\n\t"
    "v_mfma_f32_16x16x32_bf16 a[156:159], v[252:255], v[16:19], a[156:159]\n\t"
    "s_waitcnt lgkmcnt(1)\n\t"
    "v_exp_f32 v15, v15\n\t"
    "v_mfma_f32_16x16x32_bf16 v[0:3], a[192:195], v[80:83], v[24:27]\n\t"
    "v_cvt_pk_bf16_f32 v22, v12, v13\n\t"
    "v_cvt_pk_bf16_f32 v23, v14, v15\n\t"
    "v_mfma_f32_16x16x32_bf16 v[0:3], a[196:199], v[84:87], v[0:3]\n\t"
    "v_mfma_f32_16x16x32_bf16 v[0:3], a[200:203], v[88:91], v[0:3]\n\t"
    "v_mfma_f32_16x16x32_bf16 v[0:3], a[204:207], v[92:95], v[0:3]\n\t"
    "v_mfma_f32_16x16x32_bf16 v[4:7], a[208:211], v[80:83], v[24:27]\n\t"
    "v_mfma_f32_16x16x32_bf16 v[4:7], a[212:215], v[84:87], v[4:7]\n\t"
    "v_mfma_f32_16x16x32_bf16 v[4:7], a[216:219], v[88:91], v[4:7]\n\t"
    "s_waitcnt lgkmcnt(0)\n\t"
    "v_mfma_f32_16x16x32_bf16 v[4:7], a[220:223], v[92:95], v[4:7]\n\t"
    "v_exp_f32 v0, v0\n\t"
    "v_mfma_f32_16x16x32_bf16 a[244:247], a[248:251], v[20:23], a[244:247]\n\t"
    "v_exp_f32 v1, v1\n\t"
    "v_mfma_f32_16x16x32_bf16 a[160:163], v[224:227], v[20:23], a[160:163]\n\t"
    "ds_read_b64_tr_b16 v[224:225], v51 offset:8192\n\t"
    "ds_read_b64_tr_b16 v[226:227], v51 offset:12288\n\t"
    "v_mfma_f32_16x16x32_bf16 a[164:167], v[228:231], v[20:23], a[164:167]\n\t"
    "ds_read_b64_tr_b16 v[228:229], v51 offset:8200\n\t"
    "ds_read_b64_tr_b16 v[230:231], v51 offset:12296\n\t"
    "v_mfma_f32_16x16x32_bf16 a[168:171], v[232:235], v[20:23], a[168:171]\n\t"
    "ds_read_b64_tr_b16 v[232:233], v51 offset:8448\n\t"
    "ds_read_b64_tr_b16 v[234:235], v51 offset:12544\n\t"
    "v_mfma_f32_16x16x32_bf16 a[172:175], v[236:239], v[20:23], a[172:175]\n\t"
    "ds_read_b64_tr_b16 v[236:237], v51 offset:8456\n\t"
    "ds_read_b64_tr_b16 v[238:239], v51 offset:12552\n\t"
    "v_mfma_f32_16x16x32_bf16 a[176:179], v[240:243], v[20:23], a[176:179]\n\t"
    "v_exp_f32 v2, v2\n\t"
    "v_mfma_f32_16x16x32_bf16 a[180:183], v[244:247], v[20:23], a[180:183]\n\t"
    "v_exp_f32 v3, v3\n\t"
    "v_mfma_f32_16x16x32_bf16 a[184:187], v[248:251], v[20:23], a[184:187]\n\t"
    "v_exp_f32 v4, v4\n\t"
    "v_mfma_f32_16x16x32_bf16 a[188:191], v[252:255], v[20:23], a[188:191]\n\t"
    "v_exp_f32 v5, v5\n\t"
    "v_mfma_f32_16x16x32_bf16 v[8:11], a[192:195], v[96:99], v[28:31]\n\t"
    "v_exp_f32 v6, v6\n\t"
    "v_mfma_f32_16x16x32_bf16 v[8:11], a[196:199], v[100:103], v[8:11]\n\t"
    "v_exp_f32 v7, v7\n\t"
    "v_mfma_f32_16x16x32_bf16 v[8:11], a[200:203], v[104:107], v[8:11]\n\t"
    "ds_read_b64_tr_b16 v[240:241], v51 offset:8704\n\t"
    "ds_read_b64_tr_b16 v[242:243], v51 offset:12800\n\t"
    "v_mfma_f32_16x16x32_bf16 v[8:11], a[204:207], v[108:111], v[8:11]\n\t"
    "v_cvt_pk_bf16_f32 v16, v0, v1\n\t"
    "v_cvt_pk_bf16_f32 v17, v2, v3\n\t"
    "v_mfma_f32_16x16x32_bf16 v[12:15], a[208:211], v[96:99], v[28:31]\n\t"
    "v_cvt_pk_bf16_f32 v18, v4, v5\n\t"
    "v_cvt_pk_bf16_f32 v19, v6, v7\n\t"
    "v_mfma_f32_16x16x32_bf16 v[12:15], a[212:215], v[100:103], v[12:15]\n\t"
    "ds_read_b64_tr_b16 v[244:245], v51 offset:8712\n\t"
    "ds_read_b64_tr_b16 v[246:247], v51 offset:12808\n\t"
    "v_mfma_f32_16x16x32_bf16 v[12:15], a[216:219], v[104:107], v[12:15]\n\t"
    "ds_read_b64_tr_b16 v[248:249], v51 offset:8960\n\t"
    "ds_read_b64_tr_b16 v[250:251], v51 offset:13056\n\t"
    "v_mfma_f32_16x16x32_bf16 v[12:15], a[220:223], v[108:111], v[12:15]\n\t"
    "ds_read_b64_tr_b16 v[252:253], v51 offset:8968\n\t"
    "ds_read_b64_tr_b16 v[254:255], v51 offset:13064\n\t"
    "v_mfma_f32_16x16x32_bf16 a[224:227], a[248:251], v[16:19], a[224:227]\n\t"
    "s_waitcnt lgkmcnt(8)\n\t"
    "v_exp_f32 v8, v8\n\t"
    "v_mfma_f32_16x16x32_bf16 a[0:3], v[224:227], v[16:19], a[0:3]\n\t"
    "v_exp_f32 v9, v9\n\t"
    "v_mfma_f32_16x16x32_bf16 a[4:7], v[228:231], v[16:19], a[4:7]\n\t"
    "v_exp_f32 v10, v10\n\t"
    "v_mfma_f32_16x16x32_bf16 a[8:11], v[232:235], v[16:19], a[8:11]\n\t"
    "v_exp_f32 v11, v11\n\t"
    "v_mfma_f32_16x16x32_bf16 a[12:15], v[236:239], v[16:19], a[12:15]\n\t"
    "s_waitcnt lgkmcnt(6)\n\t"
    "v_exp_f32 v12, v12\n\t"
    "v_mfma_f32_16x16x32_bf16 a[16:19], v[240:243], v[16:19], a[16:19]\n\t"
    "s_waitcnt lgkmcnt(4)\n\t"
    "v_exp_f32 v13, v13\n\t"
    "v_mfma_f32_16x16x32_bf16 a[20:23], v[244:247], v[16:19], a[20:23]\n\t"
    "s_waitcnt lgkmcnt(2)\n\t"
    "v_exp_f32 v14, v14\n\t"
    "v_mfma_f32_16x16x32_bf16 a[24:27], v[248:251], v[16:19], a[24:27]\n\t"
    "s_waitcnt lgkmcnt(0)\n\t"
    "v_exp_f32 v15, v15\n\t"
    "v_mfma_f32_16x16x32_bf16 a[28:31], v[252:255], v[16:19], a[28:31]\n\t"
    "v_cvt_pk_bf16_f32 v20, v8, v9\n\t"
    "v_cvt_pk_bf16_f32 v21, v10, v11\n\t"
    "v_mfma_f32_16x16x32_bf16 v[0:3], a[192:195], v[112:115], v[32:35]\n\t"
    "v_cvt_pk_bf16_f32 v22, v12, v13\n\t"
    "v_cvt_pk_bf16_f32 v23, v14, v15\n\t"
    "v_mfma_f32_16x16x32_bf16 v[0:3], a[196:199], v[116:119], v[0:3]\n\t"
    "s_waitcnt vmcnt(9)\n\t"
    "v_lshlrev_b32 v64, 16, v192\n\t"
    "v_and_b32 v65, s49, v192\n\t"
    "v_mfma_f32_16x16x32_bf16 v[0:3], a[200:203], v[120:123], v[0:3]\n\t"
    "v_mul_f32 v64, s48, v64\n\t"
    "v_mul_f32 v65, s48, v65\n\t"
    "v_mfma_f32_16x16x32_bf16 v[0:3], a[204:207], v[124:127], v[0:3]\n\t"
    "v_cvt_pk_bf16_f32 v192, v64, v65\n\t"
    "v_lshlrev_b32 v66, 16, v193\n\t"
    "v_mfma_f32_16x16x32_bf16 v[4:7], a[208:211], v[112:115], v[32:35]\n\t"
    "v_and_b32 v67, s49, v193\n\t"
    "v_mul_f32 v66, s48, v66\n\t"
    "v_mfma_f32_16x16x32_bf16 v[4:7], a[212:215], v[116:119], v[4:7]\n\t"
    "v_mul_f32 v67, s48, v67\n\t"
    "v_cvt_pk_bf16_f32 v193, v66, v67\n\t"
    "v_mfma_f32_16x16x32_bf16 v[4:7], a[216:219], v[120:123], v[4:7]\n\t"
    "v_lshlrev_b32 v68, 16, v194\n\t"
    "v_and_b32 v69, s49, v194\n\t"
    "v_mfma_f32_16x16x32_bf16 v[4:7], a[220:223], v[124:127], v[4:7]\n\t"
    "v_exp_f32 v0, v0\n\t"
    "v_mfma_f32_16x16x32_bf16 a[228:231], a[248:251], v[20:23], a[228:231]\n\t"
    "v_exp_f32 v1, v1\n\t"
    "v_mfma_f32_16x16x32_bf16 a[32:35], v[224:227], v[20:23], a[32:35]\n\t"
    "v_exp_f32 v2, v2\n\t"
    "v_mfma_f32_16x16x32_bf16 a[36:39], v[228:231], v[20:23], a[36:39]\n\t"
    "v_exp_f32 v3, v3\n\t"
    "v_mfma_f32_16x16x32_bf16 a[40:43], v[232:235], v[20:23], a[40:43]\n\t"
    "v_exp_f32 v4, v4\n\t"
    "v_mfma_f32_16x16x32_bf16 a[44:47], v[236:239], v[20:23], a[44:47]\n\t"
    "v_exp_f32 v5, v5\n\t"
    "v_mfma_f32_16x16x32_bf16 a[48:51], v[240:243], v[20:23], a[48:51]\n\t"
    "v_exp_f32 v6, v6\n\t"
    "v_mfma_f32_16x16x32_bf16 a[52:55], v[244:247], v[20:23], a[52:55]\n\t"
    "v_exp_f32 v7, v7\n\t"
    "v_mfma_f32_16x16x32_bf16 a[56:59], v[248:251], v[20:23], a[56:59]\n\t"
    "v_cvt_pk_bf16_f32 v16, v0, v1\n\t"
    "v_cvt_pk_bf16_f32 v17, v2, v3\n\t"
    "v_mfma_f32_16x16x32_bf16 a[60:63], v[252:255], v[20:23], a[60:63]\n\t"
    "v_cvt_pk_bf16_f32 v18, v4, v5\n\t"
    "v_cvt_pk_bf16_f32 v19, v6, v7\n\t"
    "v_mfma_f32_16x16x32_bf16 v[8:11], a[192:195], v[128:131], v[36:39]\n\t"
    "v_mul_f32 v68, s48, v68\n\t"
    "v_mul_f32 v69, s48, v69\n\t"
    "v_mfma_f32_16x16x32_bf16 v[8:11], a[196:199], v[132:135], v[8:11]\n\t"
    "v_cvt_pk_bf16_f32 v194, v68, v69\n\t"
    "v_lshlrev_b32 v70, 16, v195\n\t"
    "v_mfma_f32_16x16x32_bf16 v[8:11], a[200:203], v[136:139], v[8:11]\n\t"
    "v_and_b32 v71, s49, v195\n\t"
    "v_mul_f32 v70, s48, v70\n\t"
    "v_mfma_f32_16x16x32_bf16 v[8:11], a[204:207], v[140:143], v[8:11]\n\t"
    "v_mul_f32 v71, s48, v71\n\t"
    "v_cvt_pk_bf16_f32 v195, v70, v71\n\t"
    "v_mfma_f32_16x16x32_bf16 v[12:15], a[208:211], v[128:131], v[36:39]\n\t"
    "s_waitcnt vmcnt(8)\n\t"
    "v_lshlrev_b32 v64, 16, v196\n\t"
    "v_and_b32 v65, s49, v196\n\t"
    "v_mfma_f32_16x16x32_bf16 v[12:15], a[212:215], v[132:135], v[12:15]\n\t"
    "v_mul_f32 v64, s48, v64\n\t"
    "v_mul_f32 v65, s48, v65\n\t"
    "v_mfma_f32_16x16x32_bf16 v[12:15], a[216:219], v[136:139], v[12:15]\n\t"
    "v_cvt_pk_bf16_f32 v196, v64, v65\n\t"
    "v_lshlrev_b32 v66, 16, v197\n\t"
    "v_mfma_f32_16x16x32_bf16 v[12:15], a[220:223], v[140:143], v[12:15]\n\t"
    "v_exp_f32 v8, v8\n\t"
    "v_mfma_f32_16x16x32_bf16 a[232:235], a[248:251], v[16:19], a[232:235]\n\t"
    "v_exp_f32 v9, v9\n\t"
    "v_mfma_f32_16x16x32_bf16 a[64:67], v[224:227], v[16:19], a[64:67]\n\t"
    "v_exp_f32 v10, v10\n\t"
    "v_mfma_f32_16x16x32_bf16 a[68:71], v[228:231], v[16:19], a[68:71]\n\t"
    "v_exp_f32 v11, v11\n\t"
    "v_mfma_f32_16x16x32_bf16 a[72:75], v[232:235], v[16:19], a[72:75]\n\t"
    "v_exp_f32 v12, v12\n\t"
    "v_mfma_f32_16x16x32_bf16 a[76:79], v[236:239], v[16:19], a[76:79]\n\t"
    "v_exp_f32 v13, v13\n\t"
    "v_mfma_f32_16x16x32_bf16 a[80:83], v[240:243], v[16:19], a[80:83]\n\t"
    "v_exp_f32 v14, v14\n\t"
    "v_mfma_f32_16x16x32_bf16 a[84:87], v[244:247], v[16:19], a[84:87]\n\t"
    "v_exp_f32 v15, v15\n\t"
    "v_mfma_f32_16x16x32_bf16 a[88:91], v[248:251], v[16:19], a[88:91]\n\t"
    "v_cvt_pk_bf16_f32 v20, v8, v9\n\t"
    "v_cvt_pk_bf16_f32 v21, v10, v11\n\t"
    "v_mfma_f32_16x16x32_bf16 a[92:95], v[252:255], v[16:19], a[92:95]\n\t"
    "v_cvt_pk_bf16_f32 v22, v12, v13\n\t"
    "v_cvt_pk_bf16_f32 v23, v14, v15\n\t"
    "v_mfma_f32_16x16x32_bf16 v[0:3], a[192:195], v[144:147], v[40:43]\n\t"
    "v_and_b32 v67, s49, v197\n\t"
    "v_mul_f32 v66, s48, v66\n\t"
    "v_mfma_f32_16x16x32_bf16 v[0:3], a[196:199], v[148:151], v[0:3]\n\t"
    "v_mul_f32 v67, s48, v67\n\t"
    "v_cvt_pk_bf16_f32 v197, v66, v67\n\t"
    "v_mfma_f32_16x16x32_bf16 v[0:3], a[200:203], v[152:155], v[0:3]\n\t"
    "v_lshlrev_b32 v68, 16, v198\n\t"
    "v_and_b32 v69, s49, v198\n\t"
    "v_mfma_f32_16x16x32_bf16 v[0:3], a[204:207], v[156:159], v[0:3]\n\t"
    "v_mul_f32 v68, s48, v68\n\t"
    "v_mul_f32 v69, s48, v69\n\t"
    "v_mfma_f32_16x16x32_bf16 v[4:7], a[208:211], v[144:147], v[40:43]\n\t"
    "v_cvt_pk_bf16_f32 v198, v68, v69\n\t"
    "v_lshlrev_b32 v70, 16, v199\n\t"
    "v_mfma_f32_16x16x32_bf16 v[4:7], a[212:215], v[148:151], v[4:7]\n\t"
    "v_and_b32 v71, s49, v199\n\t"
    "v_mul_f32 v70, s48, v70\n\t"
    "v_mfma_f32_16x16x32_bf16 v[4:7], a[216:219], v[152:155], v[4:7]\n\t"
    "v_mul_f32 v71, s48, v71\n\t"
    "v_cvt_pk_bf16_f32 v199, v70, v71\n\t"
    "v_mfma_f32_16x16x32_bf16 v[4:7], a[220:223], v[156:159], v[4:7]\n\t"
    "v_exp_f32 v0, v0\n\t"
    "v_mfma_f32_16x16x32_bf16 a[236:239], a[248:251], v[20:23], a[236:239]\n\t"
    "v_exp_f32 v1, v1\n\t"
    "v_mfma_f32_16x16x32_bf16 a[96:99], v[224:227], v[20:23], a[96:99]\n\t"
    "v_exp_f32 v2, v2\n\t"
    "v_mfma_f32_16x16x32_bf16 a[100:103], v[228:231], v[20:23], a[100:103]\n\t"
    "v_exp_f32 v3, v3\n\t"
    "v_mfma_f32_16x16x32_bf16 a[104:107], v[232:235], v[20:23], a[104:107]\n\t"
    "v_exp_f32 v4, v4\n\t"
    "v_mfma_f32_16x16x32_bf16 a[108:111], v[236:239], v[20:23], a[108:111]\n\t"
    "v_exp_f32 v5, v5\n\t"
    "v_mfma_f32_16x16x32_bf16 a[112:115], v[240:243], v[20:23], a[112:115]\n\t"
    "v_exp_f32 v6, v6\n\t"
    "v_mfma_f32_16x16x32_bf16 a[116:119], v[244:247], v[20:23], a[116:119]\n\t"
    "v_exp_f32 v7, v7\n\t"
    "v_mfma_f32_16x16x32_bf16 a[120:123], v[248:251], v[20:23], a[120:123]\n\t"
    "v_cvt_pk_bf16_f32 v16, v0, v1\n\t"
    "v_cvt_pk_bf16_f32 v17, v2, v3\n\t"
    "v_mfma_f32_16x16x32_bf16 a[124:127], v[252:255], v[20:23], a[124:127]\n\t"
    "s_waitcnt vmcnt(3)\n\t"
    "v_cvt_pk_bf16_f32 v18, v4, v5\n\t"
    "v_cvt_pk_bf16_f32 v19, v6, v7\n\t"
    "v_mfma_f32_16x16x32_bf16 v[8:11], a[192:195], v[160:163], v[44:47]\n\t"
    "s_barrier\n\t"
    "ds_read_b128 a[192:195], v48 offset:40960\n\t"
    "v_mfma_f32_16x16x32_bf16 v[8:11], a[196:199], v[164:167], v[8:11]\n\t"
    "ds_read_b128 a[196:199], v48 offset:41216\n\t"
    "v_mfma_f32_16x16x32_bf16 v[8:11], a[200:203], v[168:171], v[8:11]\n\t"
    "ds_read_b128 a[200:203], v48 offset:41472\n\t"
    "v_mfma_f32_16x16x32_bf16 v[8:11], a[204:207], v[172:175], v[8:11]\n\t"
    "ds_read_b128 a[204:207], v48 offset:41728\n\t"
    "v_mfma_f32_16x16x32_bf16 v[12:15], a[208:211], v[160:163], v[44:47]\n\t"
    "ds_read_b128 a[208:211], v48 offset:45056\n\t"
    "v_mfma_f32_16x16x32_bf16 v[12:15], a[212:215], v[164:167], v[12:15]\n\t"
    "ds_read_b128 a[212:215], v48 offset:45312\n\t"
    "v_mfma_f32_16x16x32_bf16 v[12:15], a[216:219], v[168:171], v[12:15]\n\t"
    "ds_read_b128 a[216:219], v48 offset:45568\n\t"
    "v_mfma_f32_16x16x32_bf16 v[12:15], a[220:223], v[172:175], v[12:15]\n\t"
    "v_exp_f32 v8, v8\n\t"
    "v_mfma_f32_16x16x32_bf16 a[240:243], a[248:251], v[16:19], a[240:243]\n\t"
    "v_exp_f32 v9, v9\n\t"
    "v_mfma_f32_16x16x32_bf16 a[128:131], v[224:227], v[16:19], a[128:131]\n\t"
    "ds_read_b128 a[220:223], v48 offset:45824\n\t"
    "v_cvt_pk_bf16_f32 v20, v8, v9\n\t"
    "v_mfma_f32_16x16x32_bf16 a[132:135], v[228:231], v[16:19], a[132:135]\n\t"
    "v_exp_f32 v10, v10\n\t"
    "v_mfma_f32_16x16x32_bf16 a[136:139], v[232:235], v[16:19], a[136:139]\n\t"
    "v_exp_f32 v11, v11\n\t"
    "v_mfma_f32_16x16x32_bf16 a[140:143], v[236:239], v[16:19], a[140:143]\n\t"
    "v_exp_f32 v12, v12\n\t"
    "v_mfma_f32_16x16x32_bf16 a[144:147], v[240:243], v[16:19], a[144:147]\n\t"
    "v_exp_f32 v13, v13\n\t"
    "v_mfma_f32_16x16x32_bf16 a[148:151], v[244:247], v[16:19], a[148:151]\n\t"
    "v_exp_f32 v14, v14\n\t"
    "v_mfma_f32_16x16x32_bf16 a[152:155], v[248:251], v[16:19], a[152:155]\n\t"
    "v_exp_f32 v15, v15\n\t"
    "v_mfma_f32_16x16x32_bf16 a[156:159], v[252:255], v[16:19], a[156:159]\n\t"
    "s_waitcnt lgkmcnt(1)\n\t"
    "v_cvt_pk_bf16_f32 v21, v10, v11\n\t"
    "v_cvt_pk_bf16_f32 v22, v12, v13\n\t"
    "v_mfma_f32_16x16x32_bf16 v[0:3], a[192:195], v[80:83], v[24:27]\n\t"
    "v_cvt_pk_bf16_f32 v23, v14, v15\n\t"
    "v_mfma_f32_16x16x32_bf16 v[0:3], a[196:199], v[84:87], v[0:3]\n\t"
    "v_mfma_f32_16x16x32_bf16 v[0:3], a[200:203], v[88:91], v[0:3]\n\t"
    "v_mfma_f32_16x16x32_bf16 v[0:3], a[204:207], v[92:95], v[0:3]\n\t"
    "v_mfma_f32_16x16x32_bf16 v[4:7], a[208:211], v[80:83], v[24:27]\n\t"
    "s_add_u32 s16, s16, 2048\n\ts_addc_u32 s17, s17, 0\n\t"
    "v_mfma_f32_16x16x32_bf16 v[4:7], a[212:215], v[84:87], v[4:7]\n\t"
    "v_mfma_f32_16x16x32_bf16 v[4:7], a[216:219], v[88:91], v[4:7]\n\t"
    "s_waitcnt lgkmcnt(0)\n\t"
    "global_load_dword a255, v55, s[16:17]\n\t"
    "v_mfma_f32_16x16x32_bf16 v[4:7], a[220:223], v[92:95], v[4:7]\n\t"
    "v_exp_f32 v0, v0\n\t"
    "v_mfma_f32_16x16x32_bf16 a[244:247], a[248:251], v[20:23], a[244:247]\n\t"
    "v_exp_f32 v1, v1\n\t"
    "v_mfma_f32_16x16x32_bf16 a[160:163], v[224:227], v[20:23], a[160:163]\n\t"
    "ds_read_b64_tr_b16 v[224:225], v50 offset:40960\n\t"
    "ds_read_b64_tr_b16 v[226:227], v50 offset:45056\n\t"
    "v_mfma_f32_16x16x32_bf16 a[164:167], v[228:231], v[20:23], a[164:167]\n\t"
    "ds_read_b64_tr_b16 v[228:229], v50 offset:40968\n\t"
    "ds_read_b64_tr_b16 v[230:231], v50 offset:45064\n\t"
    "v_mfma_f32_16x16x32_bf16 a[168:171], v[232:235], v[20:23], a[168:171]\n\t"
    "ds_read_b64_tr_b16 v[232:233], v50 offset:41216\n\t"
    "ds_read_b64_tr_b16 v[234:235], v50 offset:45312\n\t"
    "v_mfma_f32_16x16x32_bf16 a[172:175], v[236:239], v[20:23], a[172:175]\n\t"
    "ds_read_b64_tr_b16 v[236:237], v50 offset:41224\n\t"
    "ds_read_b64_tr_b16 v[238:239], v50 offset:45320\n\t"
    "v_mfma_f32_16x16x32_bf16 a[176:179], v[240:243], v[20:23], a[176:179]\n\t"
    "v_exp_f32 v2, v2\n\t"
    "v_mfma_f32_16x16x32_bf16 a[180:183], v[244:247], v[20:23], a[180:183]\n\t"
    "v_exp_f32 v3, v3\n\t"
    "v_mfma_f32_16x16x32_bf16 a[184:187], v[248:251], v[20:23], a[184:187]\n\t"
    "v_exp_f32 v4, v4\n\t"
    "v_mfma_f32_16x16x32_bf16 a[188:191], v[252:255], v[20:23], a[188:191]\n\t"
    "v_exp_f32 v5, v5\n\t"
    "v_mfma_f32_16x16x32_bf16 v[8:11], a[192:195], v[96:99], v[28:31]\n\t"
    "v_exp_f32 v6, v6\n\t"
    "v_mfma_f32_16x16x32_bf16 v[8:11], a[196:199], v[100:103], v[8:11]\n\t"
    "v_exp_f32 v7, v7\n\t"
    "v_mfma_f32_16x16x32_bf16 v[8:11], a[200:203], v[104:107], v[8:11]\n\t"
    "ds_read_b64_tr_b16 v[240:241], v50 offset:41472\n\t"
    "ds_read_b64_tr_b16 v[242:243], v50 offset:45568\n\t"
    "v_mfma_f32_16x16x32_bf16 v[8:11], a[204:207], v[108:111], v[8:11]\n\t"
    "v_cvt_pk_bf16_f32 v16, v0, v1\n\t"
    "v_cvt_pk_bf16_f32 v17, v2, v3\n\t"
    "v_mfma_f32_16x16x32_bf16 v[12:15], a[208:211], v[96:99], v[28:31]\n\t"
    "v_cvt_pk_bf16_f32 v18, v4, v5\n\t"
    "v_cvt_pk_bf16_f32 v19, v6, v7\n\t"
    "v_mfma_f32_16x16x32_bf16 v[12:15], a[212:215], v[100:103], v[12:15]\n\t"
    "ds_read_b64_tr_b16 v[244:245], v50 offset:41480\n\t"
    "ds_read_b64_tr_b16 v[246:247], v50 offset:45576\n\t"
    "v_mfma_f32_16x16x32_bf16 v[12:15], a[216:219], v[104:107], v[12:15]\n\t"
    "ds_read_b64_tr_b16 v[248:249], v50 offset:41728\n\t"
    "ds_read_b64_tr_b16 v[250:251], v50 offset:45824\n\t"
    "v_mfma_f32_16x16x32_bf16 v[12:15], a[220:223], v[108:111], v[12:15]\n\t"
    "ds_read_b64_tr_b16 v[252:253], v50 offset:41736\n\t"
    "ds_read_b64_tr_b16 v[254:255], v50 offset:45832\n\t"
    "v_mfma_f32_16x16x32_bf16 a[224:227], a[248:251], v[16:19], a[224:227]\n\t"
    "s_waitcnt lgkmcnt(8)\n\t"
    "v_exp_f32 v8, v8\n\t"
    "v_mfma_f32_16x16x32_bf16 a[0:3], v[224:227], v[16:19], a[0:3]\n\t"
    "v_exp_f32 v9, v9\n\t"
    "v_mfma_f32_16x16x32_bf16 a[4:7], v[228:231], v[16:19], a[4:7]\n\t"
    "v_exp_f32 v10, v10\n\t"
    "v_mfma_f32_16x16x32_bf16 a[8:11], v[232:235], v[16:19], a[8:11]\n\t"
    "v_exp_f32 v11, v11\n\t"
    "v_mfma_f32_16x16x32_bf16 a[12:15], v[236:239], v[16:19], a[12:15]\n\t"
    "s_waitcnt lgkmcnt(6)\n\t"
    "v_exp_f32 v12, v12\n\t"
    "v_mfma_f32_16x16x32_bf16 a[16:19], v[240:243], v[16:19], a[16:19]\n\t"
    "s_waitcnt lgkmcnt(4)\n\t"
    "v_exp_f32 v13, v13\n\t"
    "v_mfma_f32_16x16x32_bf16 a[20:23], v[244:247], v[16:19], a[20:23]\n\t"
    "s_waitcnt lgkmcnt(2)\n\t"
    "v_exp_f32 v14, v14\n\t"
    "v_mfma_f32_16x16x32_bf16 a[24:27], v[248:251], v[16:19], a[24:27]\n\t"
    "s_waitcnt lgkmcnt(0)\n\t"
    "v_exp_f32 v15, v15\n\t"
    "v_mfma_f32_16x16x32_bf16 a[28:31], v[252:255], v[16:19], a[28:31]\n\t"
    "v_cvt_pk_bf16_f32 v20, v8, v9\n\t"
    "v_cvt_pk_bf16_f32 v21, v10, v11\n\t"
    "v_mfma_f32_16x16x32_bf16 v[0:3], a[192:195], v[112:115], v[32:35]\n\t"
    "s_barrier\n\t"
    "v_cvt_pk_bf16_f32 v22, v12, v13\n\t"
    "v_cvt_pk_bf16_f32 v23, v14, v15\n\t"
    "v_mfma_f32_16x16x32_bf16 v[0:3], a[196:199], v[116:119], v[0:3]\n\t"
    "s_add_u32 m0, s51, 40960\n\t"
    "v_accvgpr_read_b32 v62, a224\n\t"
    "v_cmp_nle_f32_e64 s[80:81], v62, s50\n\t"
    "v_mfma_f32_16x16x32_bf16 v[0:3], a[200:203], v[120:123], v[0:3]\n\t"
    "buffer_load_dwordx4 v52, s[36:39], s74 offen offset:0 lds\n\t"
    "v_mfma_f32_16x16x32_bf16 v[0:3], a[204:207], v[124:127], v[0:3]\n\t"
    "v_rcp_f32 v62, v62\n\t"
    "v_mfma_f32_16x16x32_bf16 v[4:7], a[208:211], v[112:115], v[32:35]\n\t"
    "v_accvgpr_read_b32 v72, a0\n\t"
    "v_accvgpr_read_b32 v73, a1\n\t"
    "v_mfma_f32_16x16x32_bf16 v[4:7], a[212:215], v[116:119], v[4:7]\n\t"
    "v_accvgpr_read_b32 v74, a2\n\t"
    "v_accvgpr_read_b32 v75, a3\n\t"
    "v_mfma_f32_16x16x32_bf16 v[4:7], a[216:219], v[120:123], v[4:7]\n\t"
    "v_accvgpr_read_b32 v76, a4\n\t"
    "v_accvgpr_read_b32 v77, a5\n\t"
    "v_mfma_f32_16x16x32_bf16 v[4:7], a[220:223], v[124:127], v[4:7]\n\t"
    "PAD6_%=:\n\ts_cbranch_vccnz PAD10_%=\n\t"
    "v_exp_f32 v0, v0\n\t"
    "v_mfma_f32_16x16x32_bf16 a[228:231], a[248:251], v[20:23], a[228:231]\n\t"
    "v_exp_f32 v1, v1\n\t"
    "v_mfma_f32_16x16x32_bf16 a[32:35], v[224:227], v[20:23], a[32:35]\n\t"
    "v_exp_f32 v2, v2\n\t"
    "v_mfma_f32_16x16x32_bf16 a[36:39], v[228:231], v[20:23], a[36:39]\n\t"
    "v_exp_f32 v3, v3\n\t"
    "v_mfma_f32_16x16x32_bf16 a[40:43], v[232:235], v[20:23], a[40:43]\n\t"
    "v_exp_f32 v4, v4\n\t"
    "v_mfma_f32_16x16x32_bf16 a[44:47], v[236:239], v[20:23], a[44:47]\n\t"
    "v_exp_f32 v5, v5\n\t"
    "v_mfma_f32_16x16x32_bf16 a[48:51], v[240:243], v[20:23], a[48:51]\n\t"
    "v_exp_f32 v6, v6\n\t"
    "v_mfma_f32_16x16x32_bf16 a[52:55], v[244:247], v[20:23], a[52:55]\n\t"
    "v_exp_f32 v7, v7\n\t"
    "v_mfma_f32_16x16x32_bf16 a[56:59], v[248:251], v[20:23], a[56:59]\n\t"
    "v_cvt_pk_bf16_f32 v16, v0, v1\n\t"
    "v_cvt_pk_bf16_f32 v17, v2, v3\n\t"
    "v_mfma_f32_16x16x32_bf16 a[60:63], v[252:255], v[20:23], a[60:63]\n\t"
    "v_cvt_pk_bf16_f32 v18, v4, v5\n\t"
    "v_cvt_pk_bf16_f32 v19, v6, v7\n\t"
    "v_mfma_f32_16x16x32_bf16 v[8:11], a[192:195], v[128:131], v[36:39]\n\t"
    "buffer_load_dwordx4 v53, s[36:39], s74 offen offset:1024 lds\n\t"
    "v_mfma_f32_16x16x32_bf16 v[8:11], a[196:199], v[132:135], v[8:11]\n\t"
    "v_accvgpr_read_b32 v78, a6\n\t"
    "v_accvgpr_read_b32 v79, a7\n\t"
    "v_mfma_f32_16x16x32_bf16 v[8:11], a[200:203], v[136:139], v[8:11]\n\t"
    "v_mul_f32 v72, v62, v72\n\t"
    "v_mul_f32 v73, v62, v73\n\t"
    "v_mfma_f32_16x16x32_bf16 v[8:11], a[204:207], v[140:143], v[8:11]\n\t"
    "v_mul_f32 v74, v62, v74\n\t"
    "v_mul_f32 v75, v62, v75\n\t"
    "v_mfma_f32_16x16x32_bf16 v[12:15], a[208:211], v[128:131], v[36:39]\n\t"
    "v_mul_f32 v76, v62, v76\n\t"
    "v_mul_f32 v77, v62, v77\n\t"
    "v_mfma_f32_16x16x32_bf16 v[12:15], a[212:215], v[132:135], v[12:15]\n\t"
    "v_mul_f32 v78, v62, v78\n\t"
    "v_mul_f32 v79, v62, v79\n\t"
    "v_mfma_f32_16x16x32_bf16 v[12:15], a[216:219], v[136:139], v[12:15]\n\t"
    "v_cvt_pk_bf16_f32 v72, v72, v73\n\t"
    "v_cvt_pk_bf16_f32 v73, v74, v75\n\t"
    "v_mfma_f32_16x16x32_bf16 v[12:15], a[220:223], v[140:143], v[12:15]\n\t"
    "s_add_u32 m0, s51, 122880\n\t"
    "v_cvt_pk_bf16_f32 v74, v76, v77\n\t"
    "v_cvt_pk_bf16_f32 v75, v78, v79\n\t"
    "v_mfma_f32_16x16x32_bf16 a[232:235], a[248:251], v[16:19], a[232:235]\n\t"
    "buffer_store_dwordx4 v[72:75], v54, s[44:47], s52 offen offset:0 sc1\n\t"
    "v_mfma_f32_16x16x32_bf16 a[64:67], v[224:227], v[16:19], a[64:67]\n\t"
    "buffer_load_dwordx4 v52, s[40:43], s74 offen offset:0 lds\n\t"
    "v_mfma_f32_16x16x32_bf16 a[68:71], v[228:231], v[16:19], a[68:71]\n\t"
    "v_exp_f32 v8, v8\n\t"
    "v_mfma_f32_16x16x32_bf16 a[72:75], v[232:235], v[16:19], a[72:75]\n\t"
    "v_exp_f32 v9, v9\n\t"
    "v_mfma_f32_16x16x32_bf16 a[76:79], v[236:239], v[16:19], a[76:79]\n\t"
    "v_exp_f32 v10, v10\n\t"
    "v_mfma_f32_16x16x32_bf16 a[80:83], v[240:243], v[16:19], a[80:83]\n\t"
    "v_exp_f32 v11, v11\n\t"
    "v_mfma_f32_16x16x32_bf16 a[84:87], v[244:247], v[16:19], a[84:87]\n\t"
    "v_exp_f32 v12, v12\n\t"
    "v_mfma_f32_16x16x32_bf16 a[88:91], v[248:251], v[16:19], a[88:91]\n\t"
    "v_exp_f32 v13, v13\n\t"
    "v_mfma_f32_16x16x32_bf16 a[92:95], v[252:255], v[16:19], a[92:95]\n\t"
    "v_exp_f32 v14, v14\n\t"
    "v_mfma_f32_16x16x32_bf16 v[0:3], a[192:195], v[144:147], v[40:43]\n\t"
    "v_exp_f32 v15, v15\n\t"
    "v_mfma_f32_16x16x32_bf16 v[0:3], a[196:199], v[148:151], v[0:3]\n\t"
    "v_cvt_pk_bf16_f32 v20, v8, v9\n\t"
    "v_cvt_pk_bf16_f32 v21, v10, v11\n\t"
    "v_mfma_f32_16x16x32_bf16 v[0:3], a[200:203], v[152:155], v[0:3]\n\t"
    "v_cvt_pk_bf16_f32 v22, v12, v13\n\t"
    "v_cvt_pk_bf16_f32 v23, v14, v15\n\t"
    "v_mfma_f32_16x16x32_bf16 v[0:3], a[204:207], v[156:159], v[0:3]\n\t"
    "buffer_load_dwordx4 v53, s[40:43], s74 offen offset:1024 lds\n\t"
    "v_mfma_f32_16x16x32_bf16 v[4:7], a[208:211], v[144:147], v[40:43]\n\t"
    "v_accvgpr_read_b32 v72, a8\n\t"
    "v_accvgpr_read_b32 v73, a9\n\t"
    "v_mfma_f32_16x16x32_bf16 v[4:7], a[212:215], v[148:151], v[4:7]\n\t"
    "v_accvgpr_read_b32 v74, a10\n\t"
    "v_accvgpr_read_b32 v75, a11\n\t"
    "v_mfma_f32_16x16x32_bf16 v[4:7], a[216:219], v[152:155], v[4:7]\n\t"
    "v_accvgpr_read_b32 v76, a12\n\t"
    "v_accvgpr_read_b32 v77, a13\n\t"
    "v_mfma_f32_16x16x32_bf16 v[4:7], a[220:223], v[156:159], v[4:7]\n\t"
    "v_exp_f32 v0, v0\n\t"
    "v_mfma_f32_16x16x32_bf16 a[236:239], a[248:251], v[20:23], a[236:239]\n\t"
    "v_exp_f32 v1, v1\n\t"
    "v_mfma_f32_16x16x32_bf16 a[96:99], v[224:227], v[20:23], a[96:99]\n\t"
    "v_exp_f32 v2, v2\n\t"
    "v_mfma_f32_16x16x32_bf16 a[100:103], v[228:231], v[20:23], a[100:103]\n\t"
    "v_exp_f32 v3, v3\n\t"
    "v_mfma_f32_16x16x32_bf16 a[104:107], v[232:235], v[20:23], a[104:107]\n\t"
    "v_exp_f32 v4, v4\n\t"
    "v_mfma_f32_16x16x32_bf16 a[108:111], v[236:239], v[20:23], a[108:111]\n\t"
    "v_exp_f32 v5, v5\n\t"
    "v_mfma_f32_16x16x32_bf16 a[112:115], v[240:243], v[20:23], a[112:115]\n\t"
    "v_exp_f32 v6, v6\n\t"
    "v_mfma_f32_16x16x32_bf16 a[116:119], v[244:247], v[20:23], a[116:119]\n\t"
    "v_exp_f32 v7, v7\n\t"
    "v_mfma_f32_16x16x32_bf16 a[120:123], v[248:251], v[20:23], a[120:123]\n\t"
    "v_cvt_pk_bf16_f32 v16, v0, v1\n\t"
    "v_cvt_pk_bf16_f32 v17, v2, v3\n\t"
    "v_mfma_f32_16x16x32_bf16 a[124:127], v[252:255], v[20:23], a[124:127]\n\t"
    "v_cvt_pk_bf16_f32 v18, v4, v5\n\t"
    "v_cvt_pk_bf16_f32 v19, v6, v7\n\t"
    "v_mfma_f32_16x16x32_bf16 v[56:59], a[192:195], v[208:211], 0\n\t"
    "v_accvgpr_read_b32 v78, a14\n\t"
    "v_accvgpr_read_b32 v79, a15\n\t"
    "v_mfma_f32_16x16x32_bf16 v[56:59], a[196:199], v[212:215], v[56:59]\n\t"
    "v_mul_f32 v72, v62, v72\n\t"
    "v_mul_f32 v73, v62, v73\n\t"
    "v_mfma_f32_16x16x32_bf16 v[56:59], a[200:203], v[216:219], v[56:59]\n\t"
    "v_mul_f32 v74, v62, v74\n\t"
    "v_mul_f32 v75, v62, v75\n\t"
    "v_mfma_f32_16x16x32_bf16 v[56:59], a[204:207], v[220:223], v[56:59]\n\t"
    "v_mul_f32 v76, v62, v76\n\t"
    "v_mul_f32 v77, v62, v77\n\t"
    "v_mfma_f32_16x16x32_bf16 v[8:11], a[192:195], v[160:163], v[44:47]\n\t"
    "v_mul_f32 v78, v62, v78\n\t"
    "v_mul_f32 v79, v62, v79\n\t"
    "v_mfma_f32_16x16x32_bf16 v[8:11], a[196:199], v[164:167], v[8:11]\n\t"
    "v_cvt_pk_bf16_f32 v72, v72, v73\n\t"
    "v_cvt_pk_bf16_f32 v73, v74, v75\n\t"
    "v_mfma_f32_16x16x32_bf16 v[8:11], a[200:203], v[168:171], v[8:11]\n\t"
    "v_cvt_pk_bf16_f32 v74, v76, v77\n\t"
    "v_cvt_pk_bf16_f32 v75, v78, v79\n\t"
    "v_mfma_f32_16x16x32_bf16 v[8:11], a[204:207], v[172:175], v[8:11]\n\t"
    "buffer_store_dwordx4 v[72:75], v54, s[44:47], s52 offen offset:64 sc1\n\t"
    "v_mfma_f32_16x16x32_bf16 v[12:15], a[208:211], v[160:163], v[44:47]\n\t"
    "v_max3_f32 v60, v56, v57, v58\n\t"
    "v_max_f32 v60, v60, v59\n\t"
    "v_mfma_f32_16x16x32_bf16 v[12:15], a[212:215], v[164:167], v[12:15]\n\t"
    "v_accvgpr_read_b32 v72, a16\n\t"
    "v_mov_b32 v61, v60\n\t"
    "v_mfma_f32_16x16x32_bf16 v[12:15], a[216:219], v[168:171], v[12:15]\n\t"
    "s_nop 0\n\t"
    "v_permlane16_swap_b32 v60, v61\n\t"
    "v_mfma_f32_16x16x32_bf16 v[12:15], a[220:223], v[172:175], v[12:15]\n\t"
    "v_max_f32 v60, v60, v61\n\t"
    "v_mov_b32 v61, v60\n\t"
    "v_mfma_f32_16x16x32_bf16 a[240:243], a[248:251], v[16:19], a[240:243]\n\t"
    "s_nop 0\n\t"
    "v_permlane32_swap_b32 v60, v61\n\t"
    "v_mfma_f32_16x16x32_bf16 a[128:131], v[224:227], v[16:19], a[128:131]\n\t"
    "v_max_f32 v60, v60, v61\n\t"
    "v_sub_f32 v32, 0, v60\n\t"
    "v_mfma_f32_16x16x32_bf16 a[132:135], v[228:231], v[16:19], a[132:135]\n\t"
    "v_sub_f32 v33, 0, v60\n\t"
    "v_sub_f32 v34, 0, v60\n\t"
    "v_mfma_f32_16x16x32_bf16 a[136:139], v[232:235], v[16:19], a[136:139]\n\t"
    "v_accvgpr_read_b32 v73, a17\n\t"
    "v_sub_f32 v35, 0, v60\n\t"
    "v_mfma_f32_16x16x32_bf16 a[140:143], v[236:239], v[16:19], a[140:143]\n\t"
    "v_exp_f32 v8, v8\n\t"
    "v_mfma_f32_16x16x32_bf16 a[144:147], v[240:243], v[16:19], a[144:147]\n\t"
    "v_exp_f32 v9, v9\n\t"
    "v_mfma_f32_16x16x32_bf16 a[148:151], v[244:247], v[16:19], a[148:151]\n\t"
    "v_exp_f32 v10, v10\n\t"
    "v_mfma_f32_16x16x32_bf16 a[152:155], v[248:251], v[16:19], a[152:155]\n\t"
    "v_exp_f32 v11, v11\n\t"
    "v_mfma_f32_16x16x32_bf16 a[156:159], v[252:255], v[16:19], a[156:159]\n\t"
    "v_exp_f32 v12, v12\n\t"
    "v_mfma_f32_16x16x32_bf16 v[56:59], a[192:195], v[176:179], 0\n\t"
    "v_exp_f32 v13, v13\n\t"
    "v_mfma_f32_16x16x32_bf16 v[56:59], a[196:199], v[180:183], v[56:59]\n\t"
    "v_exp_f32 v14, v14\n\t"
    "v_mfma_f32_16x16x32_bf16 v[56:59], a[200:203], v[184:187], v[56:59]\n\t"
    "v_exp_f32 v15, v15\n\t"
    "v_mfma_f32_16x16x32_bf16 v[56:59], a[204:207], v[188:191], v[56:59]\n\t"
    "v_cvt_pk_bf16_f32 v20, v8, v9\n\t"
    "v_cvt_pk_bf16_f32 v21, v10, v11\n\t"
    "v_mfma_f32_16x16x32_bf16 v[0:3], a[192:195], v[208:211], v[32:35]\n\t"
    "v_cvt_pk_bf16_f32 v22, v12, v13\n\t"
    "v_cvt_pk_bf16_f32 v23, v14, v15\n\t"
    "v_mfma_f32_16x16x32_bf16 v[0:3], a[196:199], v[212:215], v[0:3]\n\t"
    "v_accvgpr_read_b32 v74, a18\n\t"
    "v_accvgpr_read_b32 v75, a19\n\t"
    "v_mfma_f32_16x16x32_bf16 v[0:3], a[200:203], v[216:219], v[0:3]\n\t"
    "v_accvgpr_read_b32 v76, a20\n\t"
    "v_accvgpr_read_b32 v77, a21\n\t"
    "v_mfma_f32_16x16x32_bf16 v[0:3], a[204:207], v[220:223], v[0:3]\n\t"
    "v_accvgpr_read_b32 v78, a22\n\t"
    "v_accvgpr_read_b32 v79, a23\n\t"
    "v_mfma_f32_16x16x32_bf16 v[4:7], a[208:211], v[208:211], v[32:35]\n\t"
    "v_mul_f32 v72, v62, v72\n\t"
    "v_mul_f32 v73, v62, v73\n\t"
    "v_mfma_f32_16x16x32_bf16 v[4:7], a[212:215], v[212:215], v[4:7]\n\t"
    "s_add_u32 s16, s16, 2048\n\ts_addc_u32 s17, s17, 0\n\t"
    "v_mul_f32 v74, v62, v74\n\t"
    "v_mul_f32 v75, v62, v75\n\t"
    "v_mfma_f32_16x16x32_bf16 v[4:7], a[216:219], v[216:219], v[4:7]\n\t"
    "v_mul_f32 v76, v62, v76\n\t"
    "v_mul_f32 v77, v62, v77\n\t"
    "v_mfma_f32_16x16x32_bf16 v[4:7], a[220:223], v[220:223], v[4:7]\n\t"
    "v_mul_f32 v78, v62, v78\n\t"
    "v_mul_f32 v79, v62, v79\n\t"
    "v_mfma_f32_16x16x32_bf16 a[244:247], a[248:251], v[20:23], a[244:247]\n\t"
    "v_cvt_pk_bf16_f32 v72, v72, v73\n\t"
    "v_cvt_pk_bf16_f32 v73, v74, v75\n\t"
    "v_mfma_f32_16x16x32_bf16 a[160:163], v[224:227], v[20:23], a[160:163]\n\t"
    "v_cvt_pk_bf16_f32 v74, v76, v77\n\t"
    "v_cvt_pk_bf16_f32 v75, v78, v79\n\t"
    "v_mfma_f32_16x16x32_bf16 a[164:167], v[228:231], v[20:23], a[164:167]\n\t"
    "buffer_store_dwordx4 v[72:75], v54, s[44:47], s52 offen offset:128 sc1\n\t"
    "v_mfma_f32_16x16x32_bf16 a[168:171], v[232:235], v[20:23], a[168:171]\n\t"
    "global_load_dword a255, v55, s[16:17]\n\t"
    "v_max3_f32 v60, v56, v57, v58\n\t"
    "v_mfma_f32_16x16x32_bf16 a[172:175], v[236:239], v[20:23], a[172:175]\n\t"
    "s_waitcnt vmcnt(11)\n\t"
    "v_lshlrev_b32 v64, 16, v200\n\t"
    "v_max_f32 v60, v60, v59\n\t"
    "v_mfma_f32_16x16x32_bf16 a[176:179], v[240:243], v[20:23], a[176:179]\n\t"
    "v_and_b32 v65, s49, v200\n\t"
    "v_mul_f32 v64, s48, v64\n\t"
    "v_mfma_f32_16x16x32_bf16 a[180:183], v[244:247], v[20:23], a[180:183]\n\t"
    "v_mul_f32 v65, s48, v65\n\t"
    "v_cvt_pk_bf16_f32 v200, v64, v65\n\t"
    "v_mfma_f32_16x16x32_bf16 a[184:187], v[248:251], v[20:23], a[184:187]\n\t"
    "v_lshlrev_b32 v66, 16, v201\n\t"
    "v_and_b32 v67, s49, v201\n\t"
    "v_mfma_f32_16x16x32_bf16 a[188:191], v[252:255], v[20:23], a[188:191]\n\t"
    "v_mul_f32 v66, s48, v66\n\t"
    "v_mul_f32 v67, s48, v67\n\t"
    "v_mfma_f32_16x16x32_bf16 v[56:59], a[192:195], v[192:195], 0\n\t"
    "v_cvt_pk_bf16_f32 v201, v66, v67\n\t"
    "v_lshlrev_b32 v68, 16, v202\n\t"
    "v_and_b32 v69, s49, v202\n\t"
    "v_mul_f32 v68, s48, v68\n\t"
    "v_mul_f32 v69, s48, v69\n\t"
    "v_cvt_pk_bf16_f32 v202, v68, v69\n\t"
    "v_lshlrev_b32 v70, 16, v203\n\t"
    "v_and_b32 v71, s49, v203\n\t"
    "v_mul_f32 v70, s48, v70\n\t"
    "v_mul_f32 v71, s48, v71\n\t"
    "v_cvt_pk_bf16_f32 v203, v70, v71\n\t"
    "v_mov_b32 v61, v60\n\t"
    "v_mfma_f32_16x16x32_bf16 v[56:59], a[196:199], v[196:199], v[56:59]\n\t"
    "s_waitcnt vmcnt(10)\n\t"
    "v_lshlrev_b32 v64, 16, v204\n\t"
    "v_and_b32 v65, s49, v204\n\t"
    "v_mul_f32 v64, s48, v64\n\t"
    "v_mul_f32 v65, s48, v65\n\t"
    "v_cvt_pk_bf16_f32 v204, v64, v65\n\t"
    "v_lshlrev_b32 v66, 16, v205\n\t"
    "v_and_b32 v67, s49, v205\n\t"
    "v_mul_f32 v66, s48, v66\n\t"
    "v_mul_f32 v67, s48, v67\n\t"
    "v_cvt_pk_bf16_f32 v205, v66, v67\n\t"
    "v_lshlrev_b32 v68, 16, v206\n\t"
    "v_and_b32 v69, s49, v206\n\t"
    "v_mul_f32 v68, s48, v68\n\t"
    "v_mul_f32 v69, s48, v69\n\t"
    "v_cvt_pk_bf16_f32 v206, v68, v69\n\t"
    "v_lshlrev_b32 v70, 16, v207\n\t"
    "v_and_b32 v71, s49, v207\n\t"
    "v_mul_f32 v70, s48, v70\n\t"
    "v_mul_f32 v71, s48, v71\n\t"
    "v_cvt_pk_bf16_f32 v207, v70, v71\n\t"
    "v_permlane16_swap_b32 v60, v61\n\t"
    "v_max_f32 v60, v60, v61\n\t"
    "v_mov_b32 v61, v60\n\t"
    "v_mfma_f32_16x16x32_bf16 v[56:59], a[200:203], v[200:203], v[56:59]\n\t"
    "s_nop 0\n\t"
    "v_permlane32_swap_b32 v60, v61\n\t"
    "v_max_f32 v60, v60, v61\n\t"
    "v_sub_f32 v24, 0, v60\n\t"
    "v_sub_f32 v25, 0, v60\n\t"
    "v_sub_f32 v26, 0, v60\n\t"
    "v_sub_f32 v27, 0, v60\n\t"
    "v_mfma_f32_16x16x32_bf16 v[56:59], a[204:207], v[204:207], v[56:59]\n\t"
    "v_exp_f32 v0, v0\n\t"
    "v_mfma_f32_16x16x32_bf16 v[8:11], a[192:195], v[176:179], v[24:27]\n\t"
    "v_exp_f32 v1, v1\n\t"
    "v_mfma_f32_16x16x32_bf16 v[8:11], a[196:199], v[180:183], v[8:11]\n\t"
    "v_exp_f32 v2, v2\n\t"
    "v_mfma_f32_16x16x32_bf16 v[8:11], a[200:203], v[184:187], v[8:11]\n\t"
    "v_exp_f32 v3, v3\n\t"
    "v_mfma_f32_16x16x32_bf16 v[8:11], a[204:207], v[188:191], v[8:11]\n\t"
    "v_exp_f32 v4, v4\n\t"
    "v_mfma_f32_16x16x32_bf16 v[12:15], a[208:211], v[176:179], v[24:27]\n\t"
    "v_exp_f32 v5, v5\n\t"
    "v_mfma_f32_16x16x32_bf16 v[12:15], a[212:215], v[180:183], v[12:15]\n\t"
    "v_exp_f32 v6, v6\n\t"
    "v_exp_f32 v7, v7\n\t"
    "v_mfma_f32_16x16x32_bf16 v[12:15], a[216:219], v[184:187], v[12:15]\n\t"
    "v_cvt_pk_bf16_f32 v16, v0, v1\n\t"
    "v_cvt_pk_bf16_f32 v17, v2, v3\n\t"
    "v_cvt_pk_bf16_f32 v18, v4, v5\n\t"
    "v_cvt_pk_bf16_f32 v19, v6, v7\n\t"
    "v_mfma_f32_16x16x32_bf16 v[12:15], a[220:223], v[188:191], v[12:15]\n\t"
    "v_max3_f32 v60, v56, v57, v58\n\t"
    "v_max_f32 v60, v60, v59\n\t"
    "v_mfma_f32_16x16x32_bf16 v[44:47], a[248:251], v[16:19], 0\n\t"
    "v_accvgpr_read_b32 v72, a24\n\t"
    "v_mov_b32 v61, v60\n\t"
    "v_mfma_f32_16x16x32_bf16 v[144:147], v[224:227], v[16:19], 0\n\t"
    "s_nop 0\n\t"
    "v_permlane16_swap_b32 v60, v61\n\t"
    "v_mfma_f32_16x16x32_bf16 v[148:151], v[228:231], v[16:19], 0\n\t"
    "v_max_f32 v60, v60, v61\n\t"
    "v_mov_b32 v61, v60\n\t"
    "v_mfma_f32_16x16x32_bf16 v[152:155], v[232:235], v[16:19], 0\n\t"
    "s_nop 0\n\t"
    "v_permlane32_swap_b32 v60, v61\n\t"
    "v_mfma_f32_16x16x32_bf16 v[156:159], v[236:239], v[16:19], 0\n\t"
    "v_max_f32 v60, v60, v61\n\t"
    "v_sub_f32 v28, 0, v60\n\t"
    "v_mfma_f32_16x16x32_bf16 v[160:163], v[240:243], v[16:19], 0\n\t"
    "v_sub_f32 v29, 0, v60\n\t"
    "v_sub_f32 v30, 0, v60\n\t"
    "v_mfma_f32_16x16x32_bf16 v[164:167], v[244:247], v[16:19], 0\n\t"
    "v_accvgpr_read_b32 v73, a25\n\t"
    "v_sub_f32 v31, 0, v60\n\t"
    "v_mfma_f32_16x16x32_bf16 v[168:171], v[248:251], v[16:19], 0\n\t"
    "v_accvgpr_read_b32 v74, a26\n\t"
    "v_accvgpr_read_b32 v75, a27\n\t"
    "v_mfma_f32_16x16x32_bf16 v[172:175], v[252:255], v[16:19], 0\n\t"
    "v_accvgpr_read_b32 v76, a28\n\t"
    "v_accvgpr_read_b32 v77, a29\n\t"
    "v_mfma_f32_16x16x32_bf16 v[0:3], a[192:195], v[192:195], v[28:31]\n\t"
    "v_accvgpr_read_b32 v78, a30\n\t"
    "v_accvgpr_read_b32 v79, a31\n\t"
    "v_mfma_f32_16x16x32_bf16 v[0:3], a[196:199], v[196:199], v[0:3]\n\t"
    "v_mul_f32 v72, v62, v72\n\t"
    "v_mul_f32 v73, v62, v73\n\t"
    "v_mfma_f32_16x16x32_bf16 v[0:3], a[200:203], v[200:203], v[0:3]\n\t"
    "v_mul_f32 v74, v62, v74\n\t"
    "v_mul_f32 v75, v62, v75\n\t"
    "v_mfma_f32_16x16x32_bf16 v[0:3], a[204:207], v[204:207], v[0:3]\n\t"
    "v_mul_f32 v76, v62, v76\n\t"
    "v_mul_f32 v77, v62, v77\n\t"
    "v_mfma_f32_16x16x32_bf16 v[4:7], a[208:211], v[192:195], v[28:31]\n\t"
    "v_mul_f32 v78, v62, v78\n\t"
    "v_mul_f32 v79, v62, v79\n\t"
    "v_cvt_pk_bf16_f32 v72, v72, v73\n\t"
    "v_cvt_pk_bf16_f32 v73, v74, v75\n\t"
    "v_cvt_pk_bf16_f32 v74, v76, v77\n\t"
    "v_cvt_pk_bf16_f32 v75, v78, v79\n\t"
    "v_mfma_f32_16x16x32_bf16 v[4:7], a[212:215], v[196:199], v[4:7]\n\t"
    "buffer_store_dwordx4 v[72:75], v54, s[44:47], s52 offen offset:192 sc1\n\t"
    "v_exp_f32 v8, v8\n\t"
    "v_exp_f32 v9, v9\n\t"
    "v_exp_f32 v10, v10\n\t"
    "v_exp_f32 v11, v11\n\t"
    "v_exp_f32 v12, v12\n\t"
    "v_exp_f32 v13, v13\n\t"
    "v_exp_f32 v14, v14\n\t"
    "v_exp_f32 v15, v15\n\t"
    "v_mfma_f32_16x16x32_bf16 v[4:7], a[216:219], v[200:203], v[4:7]\n\t"
    "v_cvt_pk_bf16_f32 v20, v8, v9\n\t"
    "v_cvt_pk_bf16_f32 v21, v10, v11\n\t"
    "v_cvt_pk_bf16_f32 v22, v12, v13\n\t"
    "v_cvt_pk_bf16_f32 v23, v14, v15\n\t"
    "v_mfma_f32_16x16x32_bf16 v[4:7], a[220:223], v[204:207], v[4:7]\n\t"
    "ds_read_b128 a[192:195], v49 offset:8192\n\t"
    "v_accvgpr_read_b32 v63, a228\n\t"
    "v_mfma_f32_16x16x32_bf16 v[36:39], a[248:251], v[20:23], 0\n\t"
    "ds_read_b128 a[196:199], v49 offset:8448\n\t"
    "v_cmp_nle_f32_e64 s[82:83], v63, s50\n\t"
    "v_mfma_f32_16x16x32_bf16 v[80:83], v[224:227], v[20:23], 0\n\t"
    "ds_read_b128 a[200:203], v49 offset:8704\n\t"
    "v_accvgpr_read_b32 v64, a32\n\t"
    "v_mfma_f32_16x16x32_bf16 v[84:87], v[228:231], v[20:23], 0\n\t"
    "ds_read_b128 a[204:207], v49 offset:8960\n\t"
    "v_accvgpr_read_b32 v65, a33\n\t"
    "v_mfma_f32_16x16x32_bf16 v[88:91], v[232:235], v[20:23], 0\n\t"
    "ds_read_b128 a[208:211], v49 offset:12288\n\t"
    "v_accvgpr_read_b32 v66, a34\n\t"
    "v_mfma_f32_16x16x32_bf16 v[92:95], v[236:239], v[20:23], 0\n\t"
    "ds_read_b128 a[212:215], v49 offset:12544\n\t"
    "v_accvgpr_read_b32 v67, a35\n\t"
    "v_mfma_f32_16x16x32_bf16 v[96:99], v[240:243], v[20:23], 0\n\t"
    "ds_read_b128 a[216:219], v49 offset:12800\n\t"
    "v_accvgpr_read_b32 v68, a36\n\t"
    "v_mfma_f32_16x16x32_bf16 v[100:103], v[244:247], v[20:23], 0\n\t"
    "ds_read_b128 a[220:223], v49 offset:13056\n\t"
    "v_accvgpr_read_b32 v69, a37\n\t"
    "v_mfma_f32_16x16x32_bf16 v[104:107], v[248:251], v[20:23], 0\n\t"
    "v_exp_f32 v0, v0\n\t"
    "v_mfma_f32_16x16x32_bf16 v[108:111], v[252:255], v[20:23], 0\n\t"
    "s_waitcnt lgkmcnt(6)\n\t"
    "v_exp_f32 v1, v1\n\t"
    "v_mfma_f32_16x16x32_bf16 v[8:11], a[192:195], v[208:211], v[32:35]\n\t"
    "v_exp_f32 v2, v2\n\t"
    "v_mfma_f32_16x16x32_bf16 v[8:11], a[196:199], v[212:215], v[8:11]\n\t"
    "s_waitcnt lgkmcnt(4)\n\t"
    "v_exp_f32 v3, v3\n\t"
    "v_mfma_f32_16x16x32_bf16 v[8:11], a[200:203], v[216:219], v[8:11]\n\t"
    "v_exp_f32 v4, v4\n\t"
    "v_mfma_f32_16x16x32_bf16 v[8:11], a[204:207], v[220:223], v[8:11]\n\t"
    "s_waitcnt lgkmcnt(2)\n\t"
    "v_exp_f32 v5, v5\n\t"
    "v_mfma_f32_16x16x32_bf16 v[12:15], a[208:211], v[208:211], v[32:35]\n\t"
    "v_exp_f32 v6, v6\n\t"
    "v_mfma_f32_16x16x32_bf16 v[12:15], a[212:215], v[212:215], v[12:15]\n\t"
    "s_waitcnt lgkmcnt(0)\n\t"
    "v_exp_f32 v7, v7\n\t"
    "v_mfma_f32_16x16x32_bf16 v[12:15], a[216:219], v[216:219], v[12:15]\n\t"
    "v_cvt_pk_bf16_f32 v16, v0, v1\n\t"
    "v_cvt_pk_bf16_f32 v17, v2, v3\n\t"
    "v_cvt_pk_bf16_f32 v18, v4, v5\n\t"
    "v_cvt_pk_bf16_f32 v19, v6, v7\n\t"
    "v_mfma_f32_16x16x32_bf16 v[12:15], a[220:223], v[220:223], v[12:15]\n\t"
    "v_rcp_f32 v63, v63\n\t"
    "v_mfma_f32_16x16x32_bf16 v[40:43], a[248:251], v[16:19], 0\n\t"
    "v_accvgpr_read_b32 v70, a38\n\t"
    "v_accvgpr_read_b32 v71, a39\n\t"
    "v_mfma_f32_16x16x32_bf16 v[112:115], v[224:227], v[16:19], 0\n\t"
    "v_mul_f32 v64, v63, v64\n\t"
    "v_mul_f32 v65, v63, v65\n\t"
    "v_mfma_f32_16x16x32_bf16 v[116:119], v[228:231], v[16:19], 0\n\t"
    "v_mul_f32 v66, v63, v66\n\t"
    "v_mul_f32 v67, v63, v67\n\t"
    "v_mfma_f32_16x16x32_bf16 v[120:123], v[232:235], v[16:19], 0\n\t"
    "v_mul_f32 v68, v63, v68\n\t"
    "v_mul_f32 v69, v63, v69\n\t"
    "v_mfma_f32_16x16x32_bf16 v[124:127], v[236:239], v[16:19], 0\n\t"
    "v_mul_f32 v70, v63, v70\n\t"
    "v_mul_f32 v71, v63, v71\n\t"
    "v_mfma_f32_16x16x32_bf16 v[128:131], v[240:243], v[16:19], 0\n\t"
    "v_cvt_pk_bf16_f32 v64, v64, v65\n\t"
    "v_cvt_pk_bf16_f32 v65, v66, v67\n\t"
    "v_mfma_f32_16x16x32_bf16 v[132:135], v[244:247], v[16:19], 0\n\t"
    "v_cvt_pk_bf16_f32 v66, v68, v69\n\t"
    "v_cvt_pk_bf16_f32 v67, v70, v71\n\t"
    "v_mfma_f32_16x16x32_bf16 v[136:139], v[248:251], v[16:19], 0\n\t"
    "buffer_store_dwordx4 v[64:67], v54, s[44:47], s53 offen offset:0 sc1\n\t"
    "v_mfma_f32_16x16x32_bf16 v[140:143], v[252:255], v[16:19], 0\n\t"
    "ds_read_b64_tr_b16 v[224:225], v51 offset:8192\n\t"
    "ds_read_b64_tr_b16 v[226:227], v51 offset:12288\n\t"
    "v_mfma_f32_16x16x32_bf16 v[0:3], a[192:195], v[176:179], v[24:27]\n\t"
    "ds_read_b64_tr_b16 v[228:229], v51 offset:8200\n\t"
    "ds_read_b64_tr_b16 v[230:231], v51 offset:12296\n\t"
    "v_mfma_f32_16x16x32_bf16 v[0:3], a[196:199], v[180:183], v[0:3]\n\t"
    "ds_read_b64_tr_b16 v[232:233], v51 offset:8448\n\t"
    "ds_read_b64_tr_b16 v[234:235], v51 offset:12544\n\t"
    "v_mfma_f32_16x16x32_bf16 v[0:3], a[200:203], v[184:187], v[0:3]\n\t"
    "ds_read_b64_tr_b16 v[236:237], v51 offset:8456\n\t"
    "ds_read_b64_tr_b16 v[238:239], v51 offset:12552\n\t"
    "v_mfma_f32_16x16x32_bf16 v[0:3], a[204:207], v[188:191], v[0:3]\n\t"
    "v_exp_f32 v8, v8\n\t"
    "v_mfma_f32_16x16x32_bf16 v[4:7], a[208:211], v[176:179], v[24:27]\n\t"
    "v_exp_f32 v9, v9\n\t"
    "v_mfma_f32_16x16x32_bf16 v[4:7], a[212:215], v[180:183], v[4:7]\n\t"
    "v_exp_f32 v10, v10\n\t"
    "v_exp_f32 v11, v11\n\t"
    "v_exp_f32 v12, v12\n\t"
    "v_exp_f32 v13, v13\n\t"
    "v_exp_f32 v14, v14\n\t"
    "v_exp_f32 v15, v15\n\t"
    "v_mfma_f32_16x16x32_bf16 v[4:7], a[216:219], v[184:187], v[4:7]\n\t"
    "ds_read_b64_tr_b16 v[240:241], v51 offset:8704\n\t"
    "ds_read_b64_tr_b16 v[242:243], v51 offset:12800\n\t"
    "v_cvt_pk_bf16_f32 v20, v8, v9\n\t"
    "v_cvt_pk_bf16_f32 v21, v10, v11\n\t"
    "v_cvt_pk_bf16_f32 v22, v12, v13\n\t"
    "v_cvt_pk_bf16_f32 v23, v14, v15\n\t"
    "v_mfma_f32_16x16x32_bf16 v[4:7], a[220:223], v[188:191], v[4:7]\n\t"
    "ds_read_b64_tr_b16 v[244:245], v51 offset:8712\n\t"
    "ds_read_b64_tr_b16 v[246:247], v51 offset:12808\n\t"
    "v_mfma_f32_16x16x32_bf16 v[44:47], a[248:251], v[20:23], v[44:47]\n\t"
    "s_waitcnt lgkmcnt(8)\n\t"
    "ds_read_b64_tr_b16 v[248:249], v51 offset:8960\n\t"
    "ds_read_b64_tr_b16 v[250:251], v51 offset:13056\n\t"
    "v_mfma_f32_16x16x32_bf16 v[144:147], v[224:227], v[20:23], v[144:147]\n\t"
    "ds_read_b64_tr_b16 v[252:253], v51 offset:8968\n\t"
    "ds_read_b64_tr_b16 v[254:255], v51 offset:13064\n\t"
    "v_mfma_f32_16x16x32_bf16 v[148:151], v[228:231], v[20:23], v[148:151]\n\t"
    "s_waitcnt lgkmcnt(8)\n\t"
    "v_exp_f32 v0, v0\n\t"
    "v_mfma_f32_16x16x32_bf16 v[152:155], v[232:235], v[20:23], v[152:155]\n\t"
    "v_exp_f32 v1, v1\n\t"
    "v_mfma_f32_16x16x32_bf16 v[156:159], v[236:239], v[20:23], v[156:159]\n\t"
    "s_waitcnt lgkmcnt(6)\n\t"
    "v_exp_f32 v2, v2\n\t"
    "v_mfma_f32_16x16x32_bf16 v[160:163], v[240:243], v[20:23], v[160:163]\n\t"
    "s_waitcnt lgkmcnt(4)\n\t"
    "v_exp_f32 v3, v3\n\t"
    "v_mfma_f32_16x16x32_bf16 v[164:167], v[244:247], v[20:23], v[164:167]\n\t"
    "s_waitcnt lgkmcnt(2)\n\t"
    "v_exp_f32 v4, v4\n\t"
    "v_mfma_f32_16x16x32_bf16 v[168:171], v[248:251], v[20:23], v[168:171]\n\t"
    "s_waitcnt lgkmcnt(0)\n\t"
    "v_exp_f32 v5, v5\n\t"
    "v_mfma_f32_16x16x32_bf16 v[172:175], v[252:255], v[20:23], v[172:175]\n\t"
    "v_exp_f32 v6, v6\n\t"
    "v_mfma_f32_16x16x32_bf16 v[8:11], a[192:195], v[192:195], v[28:31]\n\t"
    "s_barrier\n\t"
    "v_exp_f32 v7, v7\n\t"
    "v_mfma_f32_16x16x32_bf16 v[8:11], a[196:199], v[196:199], v[8:11]\n\t"
    "s_add_u32 m0, s51, 73728\n\t"
    "v_cvt_pk_bf16_f32 v16, v0, v1\n\t"
    "v_cvt_pk_bf16_f32 v17, v2, v3\n\t"
    "v_mfma_f32_16x16x32_bf16 v[8:11], a[200:203], v[200:203], v[8:11]\n\t"
    "v_cvt_pk_bf16_f32 v18, v4, v5\n\t"
    "v_cvt_pk_bf16_f32 v19, v6, v7\n\t"
    "v_mfma_f32_16x16x32_bf16 v[8:11], a[204:207], v[204:207], v[8:11]\n\t"
    "v_accvgpr_read_b32 v72, a40\n\t"
    "v_accvgpr_read_b32 v73, a41\n\t"
    "v_mfma_f32_16x16x32_bf16 v[12:15], a[208:211], v[192:195], v[28:31]\n\t"
    "v_accvgpr_read_b32 v74, a42\n\t"
    "v_accvgpr_read_b32 v75, a43\n\t"
    "v_mfma_f32_16x16x32_bf16 v[12:15], a[212:215], v[196:199], v[12:15]\n\t"
    "v_accvgpr_read_b32 v76, a44\n\t"
    "v_accvgpr_read_b32 v77, a45\n\t"
    "v_mfma_f32_16x16x32_bf16 v[12:15], a[216:219], v[200:203], v[12:15]\n\t"
    "v_accvgpr_read_b32 v78, a46\n\t"
    "v_accvgpr_read_b32 v79, a47\n\t"
    "v_mfma_f32_16x16x32_bf16 v[12:15], a[220:223], v[204:207], v[12:15]\n\t"
    "v_mul_f32 v72, v63, v72\n\t"
    "v_mul_f32 v73, v63, v73\n\t"
    "v_mfma_f32_16x16x32_bf16 v[36:39], a[248:251], v[16:19], v[36:39]\n\t"
    "v_mul_f32 v74, v63, v74\n\t"
    "v_mul_f32 v75, v63, v75\n\t"
    "v_mfma_f32_16x16x32_bf16 v[80:83], v[224:227], v[16:19], v[80:83]\n\t"
    "v_mul_f32 v76, v63, v76\n\t"
    "v_mul_f32 v77, v63, v77\n\t"
    "v_mul_f32 v78, v63, v78\n\t"
    "v_mul_f32 v79, v63, v79\n\t"
    "v_cvt_pk_bf16_f32 v72, v72, v73\n\t"
    "v_cvt_pk_bf16_f32 v73, v74, v75\n\t"
    "v_cvt_pk_bf16_f32 v74, v76, v77\n\t"
    "v_cvt_pk_bf16_f32 v75, v78, v79\n\t"
    "v_mfma_f32_16x16x32_bf16 v[84:87], v[228:231], v[16:19], v[84:87]\n\t"
    "buffer_store_dwordx4 v[72:75], v54, s[44:47], s53 offen offset:64 sc1\n\t"
    "ds_read_b128 a[192:195], v49 offset:0\n\t"
    "v_mfma_f32_16x16x32_bf16 v[88:91], v[232:235], v[16:19], v[88:91]\n\t"
    "ds_read_b128 a[196:199], v49 offset:256\n\t"
    "v_accvgpr_read_b32 v64, a48\n\t"
    "v_mfma_f32_16x16x32_bf16 v[92:95], v[236:239], v[16:19], v[92:95]\n\t"
    "ds_read_b128 a[200:203], v49 offset:512\n\t"
    "v_accvgpr_read_b32 v65, a49\n\t"
    "v_mfma_f32_16x16x32_bf16 v[96:99], v[240:243], v[16:19], v[96:99]\n\t"
    "ds_read_b128 a[204:207], v49 offset:768\n\t"
    "v_accvgpr_read_b32 v66, a50\n\t"
    "v_mfma_f32_16x16x32_bf16 v[100:103], v[244:247], v[16:19], v[100:103]\n\t"
    "buffer_load_dwordx4 v52, s[36:39], s73 offen offset:0 lds\n\t"
    "ds_read_b128 a[208:211], v49 offset:4096\n\t"
    "v_mfma_f32_16x16x32_bf16 v[104:107], v[248:251], v[16:19], v[104:107]\n\t"
    "ds_read_b128 a[212:215], v49 offset:4352\n\t"
    "v_accvgpr_read_b32 v67, a51\n\t"
    "v_mfma_f32_16x16x32_bf16 v[108:111], v[252:255], v[16:19], v[108:111]\n\t"
    "s_waitcnt lgkmcnt(5)\n\t"
    "ds_read_b128 a[216:219], v49 offset:4608\n\t"
    "v_accvgpr_read_b32 v68, a52\n\t"
    "v_mfma_f32_16x16x32_bf16 v[0:3], a[192:195], v[208:211], v[32:35]\n\t"
    "s_waitcnt lgkmcnt(5)\n\t"
    "ds_read_b128 a[220:223], v49 offset:4864\n\t"
    "v_accvgpr_read_b32 v69, a53\n\t"
    "v_mfma_f32_16x16x32_bf16 v[0:3], a[196:199], v[212:215], v[0:3]\n\t"
    "s_waitcnt lgkmcnt(5)\n\t"
    "v_exp_f32 v8, v8\n\t"
    "v_mfma_f32_16x16x32_bf16 v[0:3], a[200:203], v[216:219], v[0:3]\n\t"
    "s_waitcnt lgkmcnt(4)\n\t"
    "v_exp_f32 v9, v9\n\t"
    "v_mfma_f32_16x16x32_bf16 v[0:3], a[204:207], v[220:223], v[0:3]\n\t"
    "s_waitcnt lgkmcnt(3)\n\t"
    "v_exp_f32 v10, v10\n\t"
    "v_mfma_f32_16x16x32_bf16 v[4:7], a[208:211], v[208:211], v[32:35]\n\t"
    "s_waitcnt lgkmcnt(2)\n\t"
    "v_exp_f32 v11, v11\n\t"
    "v_mfma_f32_16x16x32_bf16 v[4:7], a[212:215], v[212:215], v[4:7]\n\t"
    "s_waitcnt lgkmcnt(1)\n\t"
    "v_exp_f32 v12, v12\n\t"
    "v_exp_f32 v13, v13\n\t"
    "v_exp_f32 v14, v14\n\t"
    "v_exp_f32 v15, v15\n\t"
    "v_mfma_f32_16x16x32_bf16 v[4:7], a[216:219], v[216:219], v[4:7]\n\t"
    "s_waitcnt lgkmcnt(0)\n\t"
    "v_cvt_pk_bf16_f32 v20, v8, v9\n\t"
    "v_cvt_pk_bf16_f32 v21, v10, v11\n\t"
    "v_cvt_pk_bf16_f32 v22, v12, v13\n\t"
    "v_cvt_pk_bf16_f32 v23, v14, v15\n\t"
    "v_mfma_f32_16x16x32_bf16 v[4:7], a[220:223], v[220:223], v[4:7]\n\t"
    "buffer_load_dwordx4 v53, s[36:39], s73 offen offset:1024 lds\n\t"
    "v_mfma_f32_16x16x32_bf16 v[40:43], a[248:251], v[20:23], v[40:43]\n\t"
    "s_add_u32 m0, s51, 155648\n\t"
    "v_accvgpr_read_b32 v70, a54\n\t"
    "v_accvgpr_read_b32 v71, a55\n\t"
    "v_mfma_f32_16x16x32_bf16 v[112:115], v[224:227], v[20:23], v[112:115]\n\t"
    "ds_read_b64_tr_b16 v[224:225], v51 offset:0\n\t"
    "ds_read_b64_tr_b16 v[226:227], v51 offset:4096\n\t"
    "v_mfma_f32_16x16x32_bf16 v[116:119], v[228:231], v[20:23], v[116:119]\n\t"
    "v_mul_f32 v64, v63, v64\n\t"
    "v_mul_f32 v65, v63, v65\n\t"
    "v_mfma_f32_16x16x32_bf16 v[120:123], v[232:235], v[20:23], v[120:123]\n\t"
    "s_add_u32 s16, s16, 2048\n\ts_addc_u32 s17, s17, 0\n\t"
    "v_mul_f32 v66, v63, v66\n\t"
    "v_mul_f32 v67, v63, v67\n\t"
    "v_mfma_f32_16x16x32_bf16 v[124:127], v[236:239], v[20:23], v[124:127]\n\t"
    "v_mul_f32 v68, v63, v68\n\t"
    "v_mul_f32 v69, v63, v69\n\t"
    "v_mfma_f32_16x16x32_bf16 v[128:131], v[240:243], v[20:23], v[128:131]\n\t"
    "v_mul_f32 v70, v63, v70\n\t"
    "v_mul_f32 v71, v63, v71\n\t"
    "v_mfma_f32_16x16x32_bf16 v[132:135], v[244:247], v[20:23], v[132:135]\n\t"
    "v_cvt_pk_bf16_f32 v64, v64, v65\n\t"
    "v_cvt_pk_bf16_f32 v65, v66, v67\n\t"
    "v_mfma_f32_16x16x32_bf16 v[136:139], v[248:251], v[20:23], v[136:139]\n\t"
    "v_cvt_pk_bf16_f32 v66, v68, v69\n\t"
    "v_cvt_pk_bf16_f32 v67, v70, v71\n\t"
    "v_mfma_f32_16x16x32_bf16 v[140:143], v[252:255], v[20:23], v[140:143]\n\t"
    "buffer_load_dwordx4 v52, s[40:43], s73 offen offset:0 lds\n\t"
    "v_mfma_f32_16x16x32_bf16 v[8:11], a[192:195], v[176:179], v[24:27]\n\t"
    "ds_read_b64_tr_b16 v[228:229], v51 offset:8\n\t"
    "ds_read_b64_tr_b16 v[230:231], v51 offset:4104\n\t"
    "v_mfma_f32_16x16x32_bf16 v[8:11], a[196:199], v[180:183], v[8:11]\n\t"
    "buffer_store_dwordx4 v[64:67], v54, s[44:47], s53 offen offset:128 sc1\n\t"
    "v_mfma_f32_16x16x32_bf16 v[8:11], a[200:203], v[184:187], v[8:11]\n\t"
    "ds_read_b64_tr_b16 v[232:233], v51 offset:256\n\t"
    "ds_read_b64_tr_b16 v[234:235], v51 offset:4352\n\t"
    "v_mfma_f32_16x16x32_bf16 v[8:11], a[204:207], v[188:191], v[8:11]\n\t"
    "ds_read_b64_tr_b16 v[236:237], v51 offset:264\n\t"
    "global_load_dword a255, v55, s[16:17]\n\t"
    "v_mfma_f32_16x16x32_bf16 v[12:15], a[208:211], v[176:179], v[24:27]\n\t"
    "ds_read_b64_tr_b16 v[238:239], v51 offset:4360\n\t"
    "ds_read_b64_tr_b16 v[240:241], v51 offset:512\n\t"
    "v_mfma_f32_16x16x32_bf16 v[12:15], a[212:215], v[180:183], v[12:15]\n\t"
    "v_exp_f32 v0, v0\n\t"
    "v_exp_f32 v1, v1\n\t"
    "v_exp_f32 v2, v2\n\t"
    "v_exp_f32 v3, v3\n\t"
    "v_exp_f32 v4, v4\n\t"
    "v_exp_f32 v5, v5\n\t"
    "v_exp_f32 v6, v6\n\t"
    "v_exp_f32 v7, v7\n\t"
    "v_mfma_f32_16x16x32_bf16 v[12:15], a[216:219], v[184:187], v[12:15]\n\t"
    "ds_read_b64_tr_b16 v[242:243], v51 offset:4608\n\t"
    "v_cvt_pk_bf16_f32 v16, v0, v1\n\t"
    "v_cvt_pk_bf16_f32 v17, v2, v3\n\t"
    "v_cvt_pk_bf16_f32 v18, v4, v5\n\t"
    "v_cvt_pk_bf16_f32 v19, v6, v7\n\t"
    "v_mfma_f32_16x16x32_bf16 v[12:15], a[220:223], v[188:191], v[12:15]\n\t"
    "PAD7_%=:\n\ts_cbranch_vccnz PADRET_%=\n\t"
    "ds_read_b64_tr_b16 v[244:245], v51 offset:520\n\t"
    "ds_read_b64_tr_b16 v[246:247], v51 offset:4616\n\t"
    "v_mfma_f32_16x16x32_bf16 v[44:47], a[248:251], v[16:19], v[44:47]\n\t"
    "s_waitcnt lgkmcnt(8)\n\t"
    "ds_read_b64_tr_b16 v[248:249], v51 offset:768\n\t"
    "ds_read_b64_tr_b16 v[250:251], v51 offset:4864\n\t"
    "v_mfma_f32_16x16x32_bf16 v[144:147], v[224:227], v[16:19], v[144:147]\n\t"
    "ds_read_b64_tr_b16 v[252:253], v51 offset:776\n\t"
    "ds_read_b64_tr_b16 v[254:255], v51 offset:4872\n\t"
    "v_mfma_f32_16x16x32_bf16 v[148:151], v[228:231], v[16:19], v[148:151]\n\t"
    "s_waitcnt lgkmcnt(10)\n\t"
    "buffer_load_dwordx4 v53, s[40:43], s73 offen offset:1024 lds\n\t"
    "v_mfma_f32_16x16x32_bf16 v[152:155], v[232:235], v[16:19], v[152:155]\n\t"
    "s_waitcnt lgkmcnt(8)\n\t"
    "v_exp_f32 v8, v8\n\t"
    "v_mfma_f32_16x16x32_bf16 v[156:159], v[236:239], v[16:19], v[156:159]\n\t"
    "s_waitcnt lgkmcnt(6)\n\t"
    "v_exp_f32 v9, v9\n\t"
    "v_mfma_f32_16x16x32_bf16 v[160:163], v[240:243], v[16:19], v[160:163]\n\t"
    "s_waitcnt lgkmcnt(4)\n\t"
    "v_exp_f32 v10, v10\n\t"
    "v_mfma_f32_16x16x32_bf16 v[164:167], v[244:247], v[16:19], v[164:167]\n\t"
    "s_waitcnt lgkmcnt(2)\n\t"
    "v_exp_f32 v11, v11\n\t"
    "v_mfma_f32_16x16x32_bf16 v[168:171], v[248:251], v[16:19], v[168:171]\n\t"
    "s_waitcnt lgkmcnt(0)\n\t"
    "v_exp_f32 v12, v12\n\t"
    "v_mfma_f32_16x16x32_bf16 v[172:175], v[252:255], v[16:19], v[172:175]\n\t"
    "v_exp_f32 v13, v13\n\t"
    "v_mfma_f32_16x16x32_bf16 v[0:3], a[192:195], v[192:195], v[28:31]\n\t"
    "s_barrier\n\t"
    "v_exp_f32 v14, v14\n\t"
    "v_mfma_f32_16x16x32_bf16 v[0:3], a[196:199], v[196:199], v[0:3]\n\t"
    "s_add_u32 m0, s51, 65536\n\t"
    "v_exp_f32 v15, v15\n\t"
    "v_mfma_f32_16x16x32_bf16 v[0:3], a[200:203], v[200:203], v[0:3]\n\t"
    "v_cvt_pk_bf16_f32 v20, v8, v9\n\t"
    "v_cvt_pk_bf16_f32 v21, v10, v11\n\t"
    "v_mfma_f32_16x16x32_bf16 v[0:3], a[204:207], v[204:207], v[0:3]\n\t"
    "v_cvt_pk_bf16_f32 v22, v12, v13\n\t"
    "v_cvt_pk_bf16_f32 v23, v14, v15\n\t"
    "v_mfma_f32_16x16x32_bf16 v[4:7], a[208:211], v[192:195], v[28:31]\n\t"
    "ds_read_b128 a[192:195], v48 offset:57344\n\t"
    "v_accvgpr_read_b32 v72, a56\n\t"
    "v_mfma_f32_16x16x32_bf16 v[4:7], a[212:215], v[196:199], v[4:7]\n\t"
    "ds_read_b128 a[196:199], v48 offset:57600\n\t"
    "v_accvgpr_read_b32 v73, a57\n\t"
    "v_mfma_f32_16x16x32_bf16 v[4:7], a[216:219], v[200:203], v[4:7]\n\t"
    "ds_read_b128 a[200:203], v48 offset:57856\n\t"
    "v_accvgpr_read_b32 v74, a58\n\t"
    "v_mfma_f32_16x16x32_bf16 v[4:7], a[220:223], v[204:207], v[4:7]\n\t"
    "ds_read_b64_tr_b16 a[0:1], v50 offset:57344\n\t"
    "v_accvgpr_read_b32 v75, a59\n\t"
    "v_mfma_f32_16x16x32_bf16 v[36:39], a[248:251], v[20:23], v[36:39]\n\t"
    "ds_read_b128 a[204:207], v48 offset:58112\n\t"
    "v_accvgpr_read_b32 v76, a60\n\t"
    "v_mfma_f32_16x16x32_bf16 v[80:83], v[224:227], v[20:23], v[80:83]\n\t"
    "v_accvgpr_read_b32 v77, a61\n\t"
    "v_accvgpr_read_b32 v78, a62\n\t"
    "v_mfma_f32_16x16x32_bf16 v[84:87], v[228:231], v[20:23], v[84:87]\n\t"
    "v_accvgpr_read_b32 v79, a63\n\t"
    "v_mul_f32 v72, v63, v72\n\t"
    "v_mfma_f32_16x16x32_bf16 v[88:91], v[232:235], v[20:23], v[88:91]\n\t"
    "v_mul_f32 v73, v63, v73\n\t"
    "v_mul_f32 v74, v63, v74\n\t"
    "v_mfma_f32_16x16x32_bf16 v[92:95], v[236:239], v[20:23], v[92:95]\n\t"
    "v_mul_f32 v75, v63, v75\n\t"
    "v_mul_f32 v76, v63, v76\n\t"
    "v_mfma_f32_16x16x32_bf16 v[96:99], v[240:243], v[20:23], v[96:99]\n\t"
    "v_mul_f32 v77, v63, v77\n\t"
    "v_mul_f32 v78, v63, v78\n\t"
    "v_mfma_f32_16x16x32_bf16 v[100:103], v[244:247], v[20:23], v[100:103]\n\t"
    "buffer_load_dwordx4 v52, s[36:39], s72 offen offset:0 lds\n\t"
    "ds_read_b128 a[208:211], v48 offset:61440\n\t"
    "v_mul_f32 v79, v63, v79\n\t"
    "v_cvt_pk_bf16_f32 v72, v72, v73\n\t"
    "v_cvt_pk_bf16_f32 v73, v74, v75\n\t"
    "v_cvt_pk_bf16_f32 v74, v76, v77\n\t"
    "v_cvt_pk_bf16_f32 v75, v78, v79\n\t"
    "v_mfma_f32_16x16x32_bf16 v[104:107], v[248:251], v[20:23], v[104:107]\n\t"
    "buffer_store_dwordx4 v[72:75], v54, s[44:47], s53 offen offset:192 sc1\n\t"
    "ds_read_b64_tr_b16 a[2:3], v50 offset:61440\n\t"
    "ds_read_b128 a[212:215], v48 offset:61696\n\t"
    "v_mfma_f32_16x16x32_bf16 v[108:111], v[252:255], v[20:23], v[108:111]\n\t"
    "s_waitcnt lgkmcnt(3)\n\t"
    "ds_read_b128 a[216:219], v48 offset:61952\n\t"
    "v_cmp_nle_f32_e64 s[18:19], v62, 4.0\n\t"
    "v_mfma_f32_16x16x32_bf16 v[8:11], a[192:195], v[208:211], v[32:35]\n\t"
    "s_nop 0\n\t"
    "s_or_b64 s[80:81], s[80:81], s[18:19]\n\t"
    "ds_read_b64_tr_b16 a[4:5], v50 offset:57352\n\t"
    "ds_read_b128 a[220:223], v48 offset:62208\n\t"
    "v_mfma_f32_16x16x32_bf16 v[8:11], a[196:199], v[212:215], v[8:11]\n\t"
    "v_exp_f32 v0, v0\n\t"
    "v_mfma_f32_16x16x32_bf16 v[8:11], a[200:203], v[216:219], v[8:11]\n\t"
    "ds_read_b64_tr_b16 a[6:7], v50 offset:61448\n\t"
    "v_accvgpr_read_b32 v62, a232\n\t"
    "v_mfma_f32_16x16x32_bf16 v[8:11], a[204:207], v[220:223], v[8:11]\n\t"
    "s_waitcnt lgkmcnt(6)\n\t"
    "v_exp_f32 v1, v1\n\t"
    "v_mfma_f32_16x16x32_bf16 v[12:15], a[208:211], v[208:211], v[32:35]\n\t"
    "s_waitcnt lgkmcnt(4)\n\t"
    "ds_read_b64_tr_b16 a[8:9], v50 offset:57600\n\t"
    "v_cvt_pk_bf16_f32 v16, v0, v1\n\t"
    "v_mfma_f32_16x16x32_bf16 v[12:15], a[212:215], v[212:215], v[12:15]\n\t"
    "s_waitcnt lgkmcnt(4)\n\t"
    "v_exp_f32 v2, v2\n\t"
    "v_exp_f32 v3, v3\n\t"
    "v_exp_f32 v4, v4\n\t"
    "v_exp_f32 v5, v5\n\t"
    "v_exp_f32 v6, v6\n\t"
    "v_exp_f32 v7, v7\n\t"
    "v_mfma_f32_16x16x32_bf16 v[12:15], a[216:219], v[216:219], v[12:15]\n\t"
    "s_waitcnt lgkmcnt(2)\n\t"
    "ds_read_b64_tr_b16 a[10:11], v50 offset:61696\n\t"
    "v_cvt_pk_bf16_f32 v17, v2, v3\n\t"
    "v_cvt_pk_bf16_f32 v18, v4, v5\n\t"
    "v_cvt_pk_bf16_f32 v19, v6, v7\n\t"
    "v_mfma_f32_16x16x32_bf16 v[12:15], a[220:223], v[220:223], v[12:15]\n\t"
    "buffer_load_dwordx4 v53, s[36:39], s72 offen offset:1024 lds\n\t"
    "v_mfma_f32_16x16x32_bf16 v[40:43], a[248:251], v[16:19], v[40:43]\n\t"
    "s_add_u32 m0, s51, 147456\n\t"
    "ds_read_b64_tr_b16 a[12:13], v50 offset:57608\n\t"
    "v_cmp_nle_f32_e64 s[84:85], v62, s50\n\t"
    "v_mfma_f32_16x16x32_bf16 v[112:115], v[224:227], v[16:19], v[112:115]\n\t"
    "buffer_load_dwordx4 v52, s[40:43], s72 offen offset:0 lds\n\t"
    "v_mfma_f32_16x16x32_bf16 v[116:119], v[228:231], v[16:19], v[116:119]\n\t"
    "ds_read_b64_tr_b16 a[14:15], v50 offset:61704\n\t"
    "v_accvgpr_read_b32 v64, a64\n\t"
    "v_mfma_f32_16x16x32_bf16 v[120:123], v[232:235], v[16:19], v[120:123]\n\t"
    "v_exp_f32 v8, v8\n\t"
    "v_mfma_f32_16x16x32_bf16 v[124:127], v[236:239], v[16:19], v[124:127]\n\t"
    "ds_read_b64_tr_b16 a[16:17], v50 offset:57856\n\t"
    "v_accvgpr_read_b32 v65, a65\n\t"
    "v_mfma_f32_16x16x32_bf16 v[128:131], v[240:243], v[16:19], v[128:131]\n\t"
    "v_exp_f32 v9, v9\n\t"
    "v_mfma_f32_16x16x32_bf16 v[132:135], v[244:247], v[16:19], v[132:135]\n\t"
    "ds_read_b64_tr_b16 a[18:19], v50 offset:61952\n\t"
    "v_cvt_pk_bf16_f32 v20, v8, v9\n\t"
    "v_mfma_f32_16x16x32_bf16 v[136:139], v[248:251], v[16:19], v[136:139]\n\t"
    "v_exp_f32 v10, v10\n\t"
    "v_mfma_f32_16x16x32_bf16 v[140:143], v[252:255], v[16:19], v[140:143]\n\t"
    "ds_read_b64_tr_b16 a[20:21], v50 offset:57864\n\t"
    "v_accvgpr_read_b32 v66, a66\n\t"
    "v_mfma_f32_16x16x32_bf16 v[0:3], a[192:195], v[176:179], v[24:27]\n\t"
    "v_exp_f32 v11, v11\n\t"
    "v_mfma_f32_16x16x32_bf16 v[0:3], a[196:199], v[180:183], v[0:3]\n\t"
    "ds_read_b64_tr_b16 a[22:23], v50 offset:61960\n\t"
    "v_cvt_pk_bf16_f32 v21, v10, v11\n\t"
    "v_mfma_f32_16x16x32_bf16 v[0:3], a[200:203], v[184:187], v[0:3]\n\t"
    "v_exp_f32 v12, v12\n\t"
    "v_mfma_f32_16x16x32_bf16 v[0:3], a[204:207], v[188:191], v[0:3]\n\t"
    "ds_read_b64_tr_b16 a[24:25], v50 offset:58112\n\t"
    "v_accvgpr_read_b32 v67, a67\n\t"
    "v_mfma_f32_16x16x32_bf16 v[4:7], a[208:211], v[176:179], v[24:27]\n\t"
    "v_exp_f32 v13, v13\n\t"
    "v_mfma_f32_16x16x32_bf16 v[4:7], a[212:215], v[180:183], v[4:7]\n\t"
    "ds_read_b64_tr_b16 a[26:27], v50 offset:62208\n\t"
    "v_exp_f32 v14, v14\n\t"
    "v_exp_f32 v15, v15\n\t"
    "v_mfma_f32_16x16x32_bf16 v[4:7], a[216:219], v[184:187], v[4:7]\n\t"
    "v_cvt_pk_bf16_f32 v22, v12, v13\n\t"
    "v_cvt_pk_bf16_f32 v23, v14, v15\n\t"
    "v_mfma_f32_16x16x32_bf16 v[4:7], a[220:223], v[188:191], v[4:7]\n\t"
    "ds_read_b64_tr_b16 a[28:29], v50 offset:58120\n\t"
    "v_rcp_f32 v62, v62\n\t"
    "v_mfma_f32_16x16x32_bf16 v[44:47], a[248:251], v[20:23], v[44:47]\n\t"
    "v_accvgpr_read_b32 v68, a68\n\t"
    "v_accvgpr_read_b32 v69, a69\n\t"
    "v_accvgpr_read_b32 v70, a70\n\t"
    "v_accvgpr_read_b32 v71, a71\n\t"
    "v_mul_f32 v64, v62, v64\n\t"
    "v_mul_f32 v65, v62, v65\n\t"
    "v_mul_f32 v66, v62, v66\n\t"
    "v_mul_f32 v67, v62, v67\n\t"
    "v_mul_f32 v68, v62, v68\n\t"
    "v_mul_f32 v69, v62, v69\n\t"
    "v_mul_f32 v70, v62, v70\n\t"
    "v_mul_f32 v71, v62, v71\n\t"
    "v_cvt_pk_bf16_f32 v64, v64, v65\n\t"
    "v_cvt_pk_bf16_f32 v65, v66, v67\n\t"
    "v_cvt_pk_bf16_f32 v66, v68, v69\n\t"
    "v_cvt_pk_bf16_f32 v67, v70, v71\n\t"
    "v_mfma_f32_16x16x32_bf16 v[144:147], a[0:3], v[20:23], v[144:147]\n\t"
    "s_waitcnt lgkmcnt(3)\n\t"
    "buffer_store_dwordx4 v[64:67], v54, s[44:47], s54 offen offset:0 sc1\n\t"
    "ds_read_b64_tr_b16 a[30:31], v50 offset:62216\n\t"
    "v_mfma_f32_16x16x32_bf16 v[148:151], a[4:7], v[20:23], v[148:151]\n\t"
    "buffer_load_dwordx4 v53, s[40:43], s72 offen offset:1024 lds\n\t"
    "v_mfma_f32_16x16x32_bf16 v[152:155], a[8:11], v[20:23], v[152:155]\n\t"
    "v_exp_f32 v0, v0\n\t"
    "v_mfma_f32_16x16x32_bf16 v[156:159], a[12:15], v[20:23], v[156:159]\n\t"
    "v_exp_f32 v1, v1\n\t"
    "v_mfma_f32_16x16x32_bf16 v[160:163], a[16:19], v[20:23], v[160:163]\n\t"
    "v_exp_f32 v2, v2\n\t"
    "v_mfma_f32_16x16x32_bf16 v[164:167], a[20:23], v[20:23], v[164:167]\n\t"
    "s_waitcnt lgkmcnt(2)\n\t"
    "v_exp_f32 v3, v3\n\t"
    "v_mfma_f32_16x16x32_bf16 v[168:171], a[24:27], v[20:23], v[168:171]\n\t"
    "s_waitcnt lgkmcnt(0)\n\t"
    "v_exp_f32 v4, v4\n\t"
    "v_mfma_f32_16x16x32_bf16 v[172:175], a[28:31], v[20:23], v[172:175]\n\t"
    "v_exp_f32 v5, v5\n\t"
    "v_mfma_f32_16x16x32_bf16 v[8:11], a[192:195], v[192:195], v[28:31]\n\t"
    "s_barrier\n\t"
    "v_exp_f32 v6, v6\n\t"
    "v_mfma_f32_16x16x32_bf16 v[8:11], a[196:199], v[196:199], v[8:11]\n\t"
    "s_add_u32 m0, s51, 57344\n\t"
    "v_exp_f32 v7, v7\n\t"
    "v_mfma_f32_16x16x32_bf16 v[8:11], a[200:203], v[200:203], v[8:11]\n\t"
    "v_cvt_pk_bf16_f32 v16, v0, v1\n\t"
    "v_cvt_pk_bf16_f32 v17, v2, v3\n\t"
    "v_mfma_f32_16x16x32_bf16 v[8:11], a[204:207], v[204:207], v[8:11]\n\t"
    "v_cvt_pk_bf16_f32 v18, v4, v5\n\t"
    "v_cvt_pk_bf16_f32 v19, v6, v7\n\t"
    "v_mfma_f32_16x16x32_bf16 v[12:15], a[208:211], v[192:195], v[28:31]\n\t"
    "ds_read_b128 a[192:195], v48 offset:49152\n\t"
    "v_accvgpr_read_b32 v72, a72\n\t"
    "v_mfma_f32_16x16x32_bf16 v[12:15], a[212:215], v[196:199], v[12:15]\n\t"
    "ds_read_b128 a[196:199], v48 offset:49408\n\t"
    "v_accvgpr_read_b32 v73, a73\n\t"
    "v_mfma_f32_16x16x32_bf16 v[12:15], a[216:219], v[200:203], v[12:15]\n\t"
    "ds_read_b128 a[200:203], v48 offset:49664\n\t"
    "v_accvgpr_read_b32 v74, a74\n\t"
    "v_mfma_f32_16x16x32_bf16 v[12:15], a[220:223], v[204:207], v[12:15]\n\t"
    "ds_read_b64_tr_b16 v[224:225], v50 offset:49152\n\t"
    "v_accvgpr_read_b32 v75, a75\n\t"
    "v_mfma_f32_16x16x32_bf16 v[36:39], a[248:251], v[16:19], v[36:39]\n\t"
    "ds_read_b128 a[204:207], v48 offset:49920\n\t"
    "v_accvgpr_read_b32 v76, a76\n\t"
    "v_mfma_f32_16x16x32_bf16 v[80:83], a[0:3], v[16:19], v[80:83]\n\t"
    "buffer_load_dwordx4 v52, s[36:39], s71 offen offset:0 lds\n\t"
    "v_mfma_f32_16x16x32_bf16 v[84:87], a[4:7], v[16:19], v[84:87]\n\t"
    "ds_read_b128 a[208:211], v48 offset:53248\n\t"
    "v_accvgpr_read_b32 v77, a77\n\t"
    "v_mfma_f32_16x16x32_bf16 v[88:91], a[8:11], v[16:19], v[88:91]\n\t"
    "ds_read_b64_tr_b16 v[226:227], v50 offset:53248\n\t"
    "v_accvgpr_read_b32 v78, a78\n\t"
    "v_mfma_f32_16x16x32_bf16 v[92:95], a[12:15], v[16:19], v[92:95]\n\t"
    "ds_read_b128 a[212:215], v48 offset:53504\n\t"
    "v_accvgpr_read_b32 v79, a79\n\t"
    "v_mfma_f32_16x16x32_bf16 v[96:99], a[16:19], v[16:19], v[96:99]\n\t"
    "ds_read_b128 a[216:219], v48 offset:53760\n\t"
    "v_mul_f32 v72, v62, v72\n\t"
    "v_mfma_f32_16x16x32_bf16 v[100:103], a[20:23], v[16:19], v[100:103]\n\t"
    "ds_read_b64_tr_b16 v[228:229], v50 offset:49160\n\t"
    "v_mul_f32 v73, v62, v73\n\t"
    "v_mfma_f32_16x16x32_bf16 v[104:107], a[24:27], v[16:19], v[104:107]\n\t"
    "ds_read_b128 a[220:223], v48 offset:54016\n\t"
    "v_mul_f32 v74, v62, v74\n\t"
    "v_mfma_f32_16x16x32_bf16 v[108:111], a[28:31], v[16:19], v[108:111]\n\t"
    "s_waitcnt lgkmcnt(6)\n\t"
    "v_mul_f32 v75, v62, v75\n\t"
    "v_mul_f32 v76, v62, v76\n\t"
    "v_mfma_f32_16x16x32_bf16 v[0:3], a[192:195], v[208:211], v[32:35]\n\t"
    "v_mul_f32 v77, v62, v77\n\t"
    "v_mul_f32 v78, v62, v78\n\t"
    "v_mfma_f32_16x16x32_bf16 v[0:3], a[196:199], v[212:215], v[0:3]\n\t"
    "v_mul_f32 v79, v62, v79\n\t"
    "v_cvt_pk_bf16_f32 v72, v72, v73\n\t"
    "v_mfma_f32_16x16x32_bf16 v[0:3], a[200:203], v[216:219], v[0:3]\n\t"
    "ds_read_b64_tr_b16 v[230:231], v50 offset:53256\n\t"
    "v_cvt_pk_bf16_f32 v73, v74, v75\n\t"
    "v_cvt_pk_bf16_f32 v74, v76, v77\n\t"
    "v_cvt_pk_bf16_f32 v75, v78, v79\n\t"
    "v_mfma_f32_16x16x32_bf16 v[0:3], a[204:207], v[220:223], v[0:3]\n\t"
    "s_waitcnt lgkmcnt(4)\n\t"
    "buffer_store_dwordx4 v[72:75], v54, s[44:47], s54 offen offset:64 sc1\n\t"
    "v_mfma_f32_16x16x32_bf16 v[4:7], a[208:211], v[208:211], v[32:35]\n\t"
    "ds_read_b64_tr_b16 v[232:233], v50 offset:49408\n\t"
    "v_accvgpr_read_b32 v64, a80\n\t"
    "v_mfma_f32_16x16x32_bf16 v[4:7], a[212:215], v[212:215], v[4:7]\n\t"
    "s_waitcnt lgkmcnt(3)\n\t"
    "v_exp_f32 v8, v8\n\t"
    "v_exp_f32 v9, v9\n\t"
    "v_exp_f32 v10, v10\n\t"
    "v_exp_f32 v11, v11\n\t"
    "v_exp_f32 v12, v12\n\t"
    "v_exp_f32 v13, v13\n\t"
    "v_exp_f32 v14, v14\n\t"
    "v_exp_f32 v15, v15\n\t"
    "v_mfma_f32_16x16x32_bf16 v[4:7], a[216:219], v[216:219], v[4:7]\n\t"
    "s_waitcnt lgkmcnt(2)\n\t"
    "ds_read_b64_tr_b16 v[234:235], v50 offset:53504\n\t"
    "v_cvt_pk_bf16_f32 v20, v8, v9\n\t"
    "v_cvt_pk_bf16_f32 v21, v10, v11\n\t"
    "v_cvt_pk_bf16_f32 v22, v12, v13\n\t"
    "v_cvt_pk_bf16_f32 v23, v14, v15\n\t"
    "v_mfma_f32_16x16x32_bf16 v[4:7], a[220:223], v[220:223], v[4:7]\n\t"
    "buffer_load_dwordx4 v53, s[36:39], s71 offen offset:1024 lds\n\t"
    "v_mfma_f32_16x16x32_bf16 v[40:43], a[248:251], v[20:23], v[40:43]\n\t"
    "s_add_u32 m0, s51, 139264\n\t"
    "ds_read_b64_tr_b16 v[236:237], v50 offset:49416\n\t"
    "v_accvgpr_read_b32 v65, a81\n\t"
    "v_mfma_f32_16x16x32_bf16 v[112:115], a[0:3], v[20:23], v[112:115]\n\t"
    "buffer_load_dwordx4 v52, s[40:43], s71 offen offset:0 lds\n\t"
    "v_mfma_f32_16x16x32_bf16 v[116:119], a[4:7], v[20:23], v[116:119]\n\t"
    "ds_read_b64_tr_b16 v[238:239], v50 offset:53512\n\t"
    "v_accvgpr_read_b32 v66, a82\n\t"
    "v_mfma_f32_16x16x32_bf16 v[120:123], a[8:11], v[20:23], v[120:123]\n\t"
    "v_exp_f32 v0, v0\n\t"
    "v_mfma_f32_16x16x32_bf16 v[124:127], a[12:15], v[20:23], v[124:127]\n\t"
    "ds_read_b64_tr_b16 v[240:241], v50 offset:49664\n\t"
    "v_accvgpr_read_b32 v67, a83\n\t"
    "v_mfma_f32_16x16x32_bf16 v[128:131], a[16:19], v[20:23], v[128:131]\n\t"
    "v_exp_f32 v1, v1\n\t"
    "v_mfma_f32_16x16x32_bf16 v[132:135], a[20:23], v[20:23], v[132:135]\n\t"
    "ds_read_b64_tr_b16 v[242:243], v50 offset:53760\n\t"
    "v_cvt_pk_bf16_f32 v16, v0, v1\n\t"
    "v_mfma_f32_16x16x32_bf16 v[136:139], a[24:27], v[20:23], v[136:139]\n\t"
    "v_exp_f32 v2, v2\n\t"
    "v_mfma_f32_16x16x32_bf16 v[140:143], a[28:31], v[20:23], v[140:143]\n\t"
    "ds_read_b64_tr_b16 v[244:245], v50 offset:49672\n\t"
    "v_accvgpr_read_b32 v68, a84\n\t"
    "v_mfma_f32_16x16x32_bf16 v[8:11], a[192:195], v[176:179], v[24:27]\n\t"
    "v_exp_f32 v3, v3\n\t"
    "v_mfma_f32_16x16x32_bf16 v[8:11], a[196:199], v[180:183], v[8:11]\n\t"
    "ds_read_b64_tr_b16 v[246:247], v50 offset:53768\n\t"
    "v_cvt_pk_bf16_f32 v17, v2, v3\n\t"
    "v_mfma_f32_16x16x32_bf16 v[8:11], a[200:203], v[184:187], v[8:11]\n\t"
    "v_exp_f32 v4, v4\n\t"
    "v_mfma_f32_16x16x32_bf16 v[8:11], a[204:207], v[188:191], v[8:11]\n\t"
    "ds_read_b64_tr_b16 v[248:249], v50 offset:49920\n\t"
    "v_accvgpr_read_b32 v69, a85\n\t"
    "v_mfma_f32_16x16x32_bf16 v[12:15], a[208:211], v[176:179], v[24:27]\n\t"
    "s_add_u32 s16, s16, 2048\n\ts_addc_u32 s17, s17, 0\n\t"
    "v_exp_f32 v5, v5\n\t"
    "v_mfma_f32_16x16x32_bf16 v[12:15], a[212:215], v[180:183], v[12:15]\n\t"
    "ds_read_b64_tr_b16 v[250:251], v50 offset:54016\n\t"
    "v_exp_f32 v6, v6\n\t"
    "v_exp_f32 v7, v7\n\t"
    "v_mfma_f32_16x16x32_bf16 v[12:15], a[216:219], v[184:187], v[12:15]\n\t"
    "v_cvt_pk_bf16_f32 v18, v4, v5\n\t"
    "v_cvt_pk_bf16_f32 v19, v6, v7\n\t"
    "v_mfma_f32_16x16x32_bf16 v[12:15], a[220:223], v[188:191], v[12:15]\n\t"
    "ds_read_b64_tr_b16 v[252:253], v50 offset:49928\n\t"
    "v_accvgpr_read_b32 v70, a86\n\t"
    "v_mfma_f32_16x16x32_bf16 v[44:47], a[248:251], v[16:19], v[44:47]\n\t"
    "v_accvgpr_read_b32 v71, a87\n\t"
    "v_mul_f32 v64, v62, v64\n\t"
    "v_mfma_f32_16x16x32_bf16 v[144:147], v[224:227], v[16:19], v[144:147]\n\t"
    "s_waitcnt lgkmcnt(3)\n\t"
    "ds_read_b64_tr_b16 v[254:255], v50 offset:54024\n\t"
    "v_mul_f32 v65, v62, v65\n\t"
    "v_mfma_f32_16x16x32_bf16 v[148:151], v[228:231], v[16:19], v[148:151]\n\t"
    "v_mul_f32 v66, v62, v66\n\t"
    "v_mul_f32 v67, v62, v67\n\t"
    "v_mfma_f32_16x16x32_bf16 v[152:155], v[232:235], v[16:19], v[152:155]\n\t"
    "v_mul_f32 v68, v62, v68\n\t"
    "v_mul_f32 v69, v62, v69\n\t"
    "v_mfma_f32_16x16x32_bf16 v[156:159], v[236:239], v[16:19], v[156:159]\n\t"
    "v_mul_f32 v70, v62, v70\n\t"
    "v_mul_f32 v71, v62, v71\n\t"
    "v_mfma_f32_16x16x32_bf16 v[160:163], v[240:243], v[16:19], v[160:163]\n\t"
    "v_cvt_pk_bf16_f32 v64, v64, v65\n\t"
    "v_cvt_pk_bf16_f32 v65, v66, v67\n\t"
    "v_cvt_pk_bf16_f32 v66, v68, v69\n\t"
    "v_cvt_pk_bf16_f32 v67, v70, v71\n\t"
    "v_mfma_f32_16x16x32_bf16 v[164:167], v[244:247], v[16:19], v[164:167]\n\t"
    "s_waitcnt lgkmcnt(2)\n\t"
    "buffer_store_dwordx4 v[64:67], v54, s[44:47], s54 offen offset:128 sc1\n\t"
    "global_load_dword a255, v55, s[16:17]\n\t"
    "v_mfma_f32_16x16x32_bf16 v[168:171], v[248:251], v[16:19], v[168:171]\n\t"
    "s_waitcnt lgkmcnt(0)\n\t"
    "buffer_load_dwordx4 v53, s[40:43], s71 offen offset:1024 lds\n\t"
    "v_mfma_f32_16x16x32_bf16 v[172:175], v[252:255], v[16:19], v[172:175]\n\t"
    "ds_read_b128 a[32:35], v48 offset:0\n\t"
    "v_accvgpr_read_b32 v72, a88\n\t"
    "v_mfma_f32_16x16x32_bf16 v[0:3], a[192:195], v[192:195], v[28:31]\n\t"
    "s_barrier\n\t"
    "v_exp_f32 v8, v8\n\t"
    "v_mfma_f32_16x16x32_bf16 v[0:3], a[196:199], v[196:199], v[0:3]\n\t"
    "s_add_u32 m0, s51, 49152\n\t"
    "v_exp_f32 v9, v9\n\t"
    "v_mfma_f32_16x16x32_bf16 v[0:3], a[200:203], v[200:203], v[0:3]\n\t"
    "v_exp_f32 v10, v10\n\t"
    "v_mfma_f32_16x16x32_bf16 v[0:3], a[204:207], v[204:207], v[0:3]\n\t"
    "v_exp_f32 v11, v11\n\t"
    "v_mfma_f32_16x16x32_bf16 v[4:7], a[208:211], v[192:195], v[28:31]\n\t"
    "v_exp_f32 v12, v12\n\t"
    "v_mfma_f32_16x16x32_bf16 v[4:7], a[212:215], v[196:199], v[4:7]\n\t"
    "ds_read_b128 a[36:39], v48 offset:256\n\t"
    "v_exp_f32 v13, v13\n\t"
    "v_exp_f32 v14, v14\n\t"
    "v_exp_f32 v15, v15\n\t"
    "v_mfma_f32_16x16x32_bf16 v[4:7], a[216:219], v[200:203], v[4:7]\n\t"
    "v_cvt_pk_bf16_f32 v20, v8, v9\n\t"
    "v_cvt_pk_bf16_f32 v21, v10, v11\n\t"
    "v_cvt_pk_bf16_f32 v22, v12, v13\n\t"
    "v_cvt_pk_bf16_f32 v23, v14, v15\n\t"
    "v_mfma_f32_16x16x32_bf16 v[4:7], a[220:223], v[204:207], v[4:7]\n\t"
    "ds_read_b128 a[40:43], v48 offset:512\n\t"
    "v_accvgpr_read_b32 v73, a89\n\t"
    "v_mfma_f32_16x16x32_bf16 v[36:39], a[248:251], v[20:23], v[36:39]\n\t"
    "ds_read_b64_tr_b16 a[0:1], v50 offset:0\n\t"
    "v_accvgpr_read_b32 v74, a90\n\t"
    "v_mfma_f32_16x16x32_bf16 v[80:83], v[224:227], v[20:23], v[80:83]\n\t"
    "ds_read_b128 a[44:47], v48 offset:768\n\t"
    "v_accvgpr_read_b32 v75, a91\n\t"
    "v_mfma_f32_16x16x32_bf16 v[84:87], v[228:231], v[20:23], v[84:87]\n\t"
    "buffer_load_dwordx4 v52, s[36:39], s70 offen offset:0 lds\n\t"
    "v_mfma_f32_16x16x32_bf16 v[88:91], v[232:235], v[20:23], v[88:91]\n\t"
    "ds_read_b128 a[48:51], v48 offset:4096\n\t"
    "v_accvgpr_read_b32 v76, a92\n\t"
    "v_mfma_f32_16x16x32_bf16 v[92:95], v[236:239], v[20:23], v[92:95]\n\t"
    "ds_read_b64_tr_b16 a[2:3], v50 offset:4096\n\t"
    "v_accvgpr_read_b32 v77, a93\n\t"
    "v_mfma_f32_16x16x32_bf16 v[96:99], v[240:243], v[20:23], v[96:99]\n\t"
    "ds_read_b128 a[52:55], v48 offset:4352\n\t"
    "v_accvgpr_read_b32 v78, a94\n\t"
    "v_mfma_f32_16x16x32_bf16 v[100:103], v[244:247], v[20:23], v[100:103]\n\t"
    "ds_read_b64_tr_b16 a[4:5], v50 offset:8\n\t"
    "v_accvgpr_read_b32 v79, a95\n\t"
    "v_mfma_f32_16x16x32_bf16 v[104:107], v[248:251], v[20:23], v[104:107]\n\t"
    "ds_read_b128 a[56:59], v48 offset:4608\n\t"
    "v_mul_f32 v72, v62, v72\n\t"
    "v_mfma_f32_16x16x32_bf16 v[108:111], v[252:255], v[20:23], v[108:111]\n\t"
    "s_waitcnt lgkmcnt(6)\n\t"
    "ds_read_b64_tr_b16 a[6:7], v50 offset:4104\n\t"
    "v_mul_f32 v73, v62, v73\n\t"
    "v_mfma_f32_16x16x32_bf16 v[8:11], a[32:35], v[208:211], v[32:35]\n\t"
    "ds_read_b128 a[60:63], v48 offset:4864\n\t"
    "v_mul_f32 v74, v62, v74\n\t"
    "v_mfma_f32_16x16x32_bf16 v[8:11], a[36:39], v[212:215], v[8:11]\n\t"
    "ds_read_b64_tr_b16 a[8:9], v50 offset:256\n\t"
    "v_mul_f32 v75, v62, v75\n\t"
    "v_mfma_f32_16x16x32_bf16 v[8:11], a[40:43], v[216:219], v[8:11]\n\t"
    "s_waitcnt lgkmcnt(7)\n\t"
    "v_exp_f32 v0, v0\n\t"
    "v_mfma_f32_16x16x32_bf16 v[8:11], a[44:47], v[220:223], v[8:11]\n\t"
    "v_exp_f32 v1, v1\n\t"
    "v_mfma_f32_16x16x32_bf16 v[12:15], a[48:51], v[208:211], v[32:35]\n\t"
    "s_waitcnt lgkmcnt(5)\n\t"
    "v_exp_f32 v2, v2\n\t"
    "v_mfma_f32_16x16x32_bf16 v[12:15], a[52:55], v[212:215], v[12:15]\n\t"
    "s_waitcnt lgkmcnt(3)\n\t"
    "v_exp_f32 v3, v3\n\t"
    "v_exp_f32 v4, v4\n\t"
    "v_exp_f32 v5, v5\n\t"
    "v_exp_f32 v6, v6\n\t"
    "v_exp_f32 v7, v7\n\t"
    "v_mfma_f32_16x16x32_bf16 v[12:15], a[56:59], v[216:219], v[12:15]\n\t"
    "s_waitcnt lgkmcnt(1)\n\t"
    "ds_read_b64_tr_b16 a[10:11], v50 offset:4352\n\t"
    "v_cvt_pk_bf16_f32 v16, v0, v1\n\t"
    "v_cvt_pk_bf16_f32 v17, v2, v3\n\t"
    "v_cvt_pk_bf16_f32 v18, v4, v5\n\t"
    "v_cvt_pk_bf16_f32 v19, v6, v7\n\t"
    "v_mfma_f32_16x16x32_bf16 v[12:15], a[60:63], v[220:223], v[12:15]\n\t"
    "v_mul_f32 v76, v62, v76\n\t"
    "v_mul_f32 v77, v62, v77\n\t"
    "v_mul_f32 v78, v62, v78\n\t"
    "v_mul_f32 v79, v62, v79\n\t"
    "v_cvt_pk_bf16_f32 v72, v72, v73\n\t"
    "v_cvt_pk_bf16_f32 v73, v74, v75\n\t"
    "v_cvt_pk_bf16_f32 v74, v76, v77\n\t"
    "v_cvt_pk_bf16_f32 v75, v78, v79\n\t"
    "v_mfma_f32_16x16x32_bf16 v[40:43], a[248:251], v[16:19], v[40:43]\n\t"
    "buffer_store_dwordx4 v[72:75], v54, s[44:47], s54 offen offset:192 sc1\n\t"
    "buffer_load_dwordx4 v53, s[36:39], s70 offen offset:1024 lds\n\t"
    "ds_read_b64_tr_b16 a[12:13], v50 offset:264\n\t"
    "v_mfma_f32_16x16x32_bf16 v[112:115], v[224:227], v[16:19], v[112:115]\n\t"
    "s_add_u32 m0, s51, 131072\n\t"
    "v_exp_f32 v8, v8\n\t"
    "v_mfma_f32_16x16x32_bf16 v[116:119], v[228:231], v[16:19], v[116:119]\n\t"
    "ds_read_b64_tr_b16 a[14:15], v50 offset:4360\n\t"
    "v_cmp_nle_f32_e64 s[20:21], v63, 4.0\n\t"
    "v_mfma_f32_16x16x32_bf16 v[120:123], v[232:235], v[16:19], v[120:123]\n\t"
    "s_nop 0\n\t"
    "s_or_b64 s[82:83], s[82:83], s[20:21]\n\t"
    "buffer_load_dwordx4 v52, s[40:43], s70 offen offset:0 lds\n\t"
    "v_mfma_f32_16x16x32_bf16 v[124:127], v[236:239], v[16:19], v[124:127]\n\t"
    "ds_read_b64_tr_b16 a[16:17], v50 offset:512\n\t"
    "v_accvgpr_read_b32 v63, a236\n\t"
    "v_mfma_f32_16x16x32_bf16 v[128:131], v[240:243], v[16:19], v[128:131]\n\t"
    "v_exp_f32 v9, v9\n\t"
    "v_mfma_f32_16x16x32_bf16 v[132:135], v[244:247], v[16:19], v[132:135]\n\t"
    "ds_read_b64_tr_b16 a[18:19], v50 offset:4608\n\t"
    "v_cvt_pk_bf16_f32 v20, v8, v9\n\t"
    "v_mfma_f32_16x16x32_bf16 v[136:139], v[248:251], v[16:19], v[136:139]\n\t"
    "v_exp_f32 v10, v10\n\t"
    "v_mfma_f32_16x16x32_bf16 v[140:143], v[252:255], v[16:19], v[140:143]\n\t"
    "ds_read_b64_tr_b16 a[20:21], v50 offset:520\n\t"
    "v_cmp_nle_f32_e64 s[86:87], v63, s50\n\t"
    "v_mfma_f32_16x16x32_bf16 v[0:3], a[32:35], v[176:179], v[24:27]\n\t"
    "v_exp_f32 v11, v11\n\t"
    "v_mfma_f32_16x16x32_bf16 v[0:3], a[36:39], v[180:183], v[0:3]\n\t"
    "ds_read_b64_tr_b16 a[22:23], v50 offset:4616\n\t"
    "v_cvt_pk_bf16_f32 v21, v10, v11\n\t"
    "v_mfma_f32_16x16x32_bf16 v[0:3], a[40:43], v[184:187], v[0:3]\n\t"
    "v_exp_f32 v12, v12\n\t"
    "v_mfma_f32_16x16x32_bf16 v[0:3], a[44:47], v[188:191], v[0:3]\n\t"
    "ds_read_b64_tr_b16 a[24:25], v50 offset:768\n\t"
    "v_accvgpr_read_b32 v64, a96\n\t"
    "v_mfma_f32_16x16x32_bf16 v[4:7], a[48:51], v[176:179], v[24:27]\n\t"
    "v_exp_f32 v13, v13\n\t"
    "v_mfma_f32_16x16x32_bf16 v[4:7], a[52:55], v[180:183], v[4:7]\n\t"
    "ds_read_b64_tr_b16 a[26:27], v50 offset:4864\n\t"
    "v_exp_f32 v14, v14\n\t"
    "v_exp_f32 v15, v15\n\t"
    "v_mfma_f32_16x16x32_bf16 v[4:7], a[56:59], v[184:187], v[4:7]\n\t"
    "v_cvt_pk_bf16_f32 v22, v12, v13\n\t"
    "v_cvt_pk_bf16_f32 v23, v14, v15\n\t"
    "v_mfma_f32_16x16x32_bf16 v[4:7], a[60:63], v[188:191], v[4:7]\n\t"
    "ds_read_b64_tr_b16 a[28:29], v50 offset:776\n\t"
    "v_accvgpr_read_b32 v65, a97\n\t"
    "v_mfma_f32_16x16x32_bf16 v[44:47], a[248:251], v[20:23], v[44:47]\n\t"
    "buffer_load_dwordx4 v53, s[40:43], s70 offen offset:1024 lds\n\t"
    "v_mfma_f32_16x16x32_bf16 v[144:147], a[0:3], v[20:23], v[144:147]\n\t"
    "ds_read_b64_tr_b16 a[30:31], v50 offset:4872\n\t"
    "v_accvgpr_read_b32 v66, a98\n\t"
    "v_mfma_f32_16x16x32_bf16 v[148:151], a[4:7], v[20:23], v[148:151]\n\t"
    "s_waitcnt lgkmcnt(4)\n\t"
    "v_rcp_f32 v63, v63\n\t"
    "v_mfma_f32_16x16x32_bf16 v[152:155], a[8:11], v[20:23], v[152:155]\n\t"
    "v_accvgpr_read_b32 v67, a99\n\t"
    "v_accvgpr_read_b32 v68, a100\n\t"
    "v_mfma_f32_16x16x32_bf16 v[156:159], a[12:15], v[20:23], v[156:159]\n\t"
    "v_accvgpr_read_b32 v69, a101\n\t"
    "v_accvgpr_read_b32 v70, a102\n\t"
    "v_mfma_f32_16x16x32_bf16 v[160:163], a[16:19], v[20:23], v[160:163]\n\t"
    "v_accvgpr_read_b32 v71, a103\n\t"
    "v_mul_f32 v64, v63, v64\n\t"
    "v_mfma_f32_16x16x32_bf16 v[164:167], a[20:23], v[20:23], v[164:167]\n\t"
    "s_waitcnt lgkmcnt(2)\n\t"
    "v_mul_f32 v65, v63, v65\n\t"
    "v_mul_f32 v66, v63, v66\n\t"
    "v_mfma_f32_16x16x32_bf16 v[168:171], a[24:27], v[20:23], v[168:171]\n\t"
    "s_waitcnt lgkmcnt(0)\n\t"
    "v_mul_f32 v67, v63, v67\n\t"
    "v_mul_f32 v68, v63, v68\n\t"
    "v_mfma_f32_16x16x32_bf16 v[172:175], a[28:31], v[20:23], v[172:175]\n\t"
    "v_mul_f32 v69, v63, v69\n\t"
    "v_mul_f32 v70, v63, v70\n\t"
    "v_mfma_f32_16x16x32_bf16 v[8:11], a[32:35], v[192:195], v[28:31]\n\t"
    "s_barrier\n\t"
    "v_mul_f32 v71, v63, v71\n\t"
    "v_cvt_pk_bf16_f32 v64, v64, v65\n\t"
    "v_mfma_f32_16x16x32_bf16 v[8:11], a[36:39], v[196:199], v[8:11]\n\t"
    "s_add_u32 m0, s51, 0\n\t"
    "v_cvt_pk_bf16_f32 v65, v66, v67\n\t"
    "v_cvt_pk_bf16_f32 v66, v68, v69\n\t"
    "v_cvt_pk_bf16_f32 v67, v70, v71\n\t"
    "v_mfma_f32_16x16x32_bf16 v[8:11], a[40:43], v[200:203], v[8:11]\n\t"
    "buffer_store_dwordx4 v[64:67], v54, s[44:47], s55 offen offset:0 sc1\n\t"
    "v_mfma_f32_16x16x32_bf16 v[8:11], a[44:47], v[204:207], v[8:11]\n\t"
    "ds_read_b128 a[192:195], v48 offset:8192\n\t"
    "v_accvgpr_read_b32 v72, a104\n\t"
    "v_mfma_f32_16x16x32_bf16 v[12:15], a[48:51], v[192:195], v[28:31]\n\t"
    "v_exp_f32 v0, v0\n\t"
    "v_mfma_f32_16x16x32_bf16 v[12:15], a[52:55], v[196:199], v[12:15]\n\t"
    "ds_read_b128 a[196:199], v48 offset:8448\n\t"
    "v_exp_f32 v1, v1\n\t"
    "v_exp_f32 v2, v2\n\t"
    "v_exp_f32 v3, v3\n\t"
    "v_exp_f32 v4, v4\n\t"
    "v_exp_f32 v5, v5\n\t"
    "v_exp_f32 v6, v6\n\t"
    "v_exp_f32 v7, v7\n\t"
    "v_mfma_f32_16x16x32_bf16 v[12:15], a[56:59], v[200:203], v[12:15]\n\t"
    "v_cvt_pk_bf16_f32 v16, v0, v1\n\t"
    "v_cvt_pk_bf16_f32 v17, v2, v3\n\t"
    "v_cvt_pk_bf16_f32 v18, v4, v5\n\t"
    "v_cvt_pk_bf16_f32 v19, v6, v7\n\t"
    "v_mfma_f32_16x16x32_bf16 v[12:15], a[60:63], v[204:207], v[12:15]\n\t"
    "ds_read_b128 a[200:203], v48 offset:8704\n\t"
    "v_accvgpr_read_b32 v73, a105\n\t"
    "v_mfma_f32_16x16x32_bf16 v[36:39], a[248:251], v[16:19], v[36:39]\n\t"
    "ds_read_b64_tr_b16 v[224:225], v50 offset:8192\n\t"
    "v_accvgpr_read_b32 v74, a106\n\t"
    "v_mfma_f32_16x16x32_bf16 v[80:83], a[0:3], v[16:19], v[80:83]\n\t"
    "ds_read_b128 a[204:207], v48 offset:8960\n\t"
    "v_accvgpr_read_b32 v75, a107\n\t"
    "v_mfma_f32_16x16x32_bf16 v[84:87], a[4:7], v[16:19], v[84:87]\n\t"
    "buffer_load_dwordx4 v52, s[36:39], s69 offen offset:0 lds\n\t"
    "v_mfma_f32_16x16x32_bf16 v[88:91], a[8:11], v[16:19], v[88:91]\n\t"
    "ds_read_b128 a[208:211], v48 offset:12288\n\t"
    "v_accvgpr_read_b32 v76, a108\n\t"
    "v_mfma_f32_16x16x32_bf16 v[92:95], a[12:15], v[16:19], v[92:95]\n\t"
    "ds_read_b64_tr_b16 v[226:227], v50 offset:12288\n\t"
    "v_accvgpr_read_b32 v77, a109\n\t"
    "v_mfma_f32_16x16x32_bf16 v[96:99], a[16:19], v[16:19], v[96:99]\n\t"
    "ds_read_b128 a[212:215], v48 offset:12544\n\t"
    "v_accvgpr_read_b32 v78, a110\n\t"
    "v_mfma_f32_16x16x32_bf16 v[100:103], a[20:23], v[16:19], v[100:103]\n\t"
    "ds_read_b64_tr_b16 v[228:229], v50 offset:8200\n\t"
    "v_accvgpr_read_b32 v79, a111\n\t"
    "v_mfma_f32_16x16x32_bf16 v[104:107], a[24:27], v[16:19], v[104:107]\n\t"
    "ds_read_b128 a[216:219], v48 offset:12800\n\t"
    "v_mul_f32 v72, v63, v72\n\t"
    "v_mfma_f32_16x16x32_bf16 v[108:111], a[28:31], v[16:19], v[108:111]\n\t"
    "s_waitcnt lgkmcnt(6)\n\t"
    "ds_read_b64_tr_b16 v[230:231], v50 offset:12296\n\t"
    "v_mul_f32 v73, v63, v73\n\t"
    "v_mfma_f32_16x16x32_bf16 v[0:3], a[192:195], v[208:211], v[32:35]\n\t"
    "PAD8_%=:\n\ts_cbranch_vccnz PADRET_%=\n\t"
    "ds_read_b128 a[220:223], v48 offset:13056\n\t"
    "v_mul_f32 v74, v63, v74\n\t"
    "v_mfma_f32_16x16x32_bf16 v[0:3], a[196:199], v[212:215], v[0:3]\n\t"
    "ds_read_b64_tr_b16 v[232:233], v50 offset:8448\n\t"
    "v_mul_f32 v75, v63, v75\n\t"
    "v_mfma_f32_16x16x32_bf16 v[0:3], a[200:203], v[216:219], v[0:3]\n\t"
    "s_waitcnt lgkmcnt(7)\n\t"
    "v_exp_f32 v8, v8\n\t"
    "v_mfma_f32_16x16x32_bf16 v[0:3], a[204:207], v[220:223], v[0:3]\n\t"
    "v_exp_f32 v9, v9\n\t"
    "v_mfma_f32_16x16x32_bf16 v[4:7], a[208:211], v[208:211], v[32:35]\n\t"
    "s_waitcnt lgkmcnt(5)\n\t"
    "v_exp_f32 v10, v10\n\t"
    "v_mfma_f32_16x16x32_bf16 v[4:7], a[212:215], v[212:215], v[4:7]\n\t"
    "s_waitcnt lgkmcnt(3)\n\t"
    "v_exp_f32 v11, v11\n\t"
    "v_exp_f32 v12, v12\n\t"
    "v_exp_f32 v13, v13\n\t"
    "v_exp_f32 v14, v14\n\t"
    "v_exp_f32 v15, v15\n\t"
    "v_mfma_f32_16x16x32_bf16 v[4:7], a[216:219], v[216:219], v[4:7]\n\t"
    "s_waitcnt lgkmcnt(1)\n\t"
    "ds_read_b64_tr_b16 v[234:235], v50 offset:12544\n\t"
    "v_cvt_pk_bf16_f32 v20, v8, v9\n\t"
    "v_cvt_pk_bf16_f32 v21, v10, v11\n\t"
    "v_cvt_pk_bf16_f32 v22, v12, v13\n\t"
    "v_cvt_pk_bf16_f32 v23, v14, v15\n\t"
    "v_mfma_f32_16x16x32_bf16 v[4:7], a[220:223], v[220:223], v[4:7]\n\t"
    "buffer_load_dwordx4 v53, s[36:39], s69 offen offset:1024 lds\n\t"
    "v_mfma_f32_16x16x32_bf16 v[40:43], a[248:251], v[20:23], v[40:43]\n\t"
    "s_add_u32 m0, s51, 81920\n\t"
    "ds_read_b64_tr_b16 v[236:237], v50 offset:8456\n\t"
    "v_mul_f32 v76, v63, v76\n\t"
    "v_mfma_f32_16x16x32_bf16 v[112:115], a[0:3], v[20:23], v[112:115]\n\t"
    "v_mul_f32 v77, v63, v77\n\t"
    "v_mul_f32 v78, v63, v78\n\t"
    "v_mfma_f32_16x16x32_bf16 v[116:119], a[4:7], v[20:23], v[116:119]\n\t"
    "ds_read_b64_tr_b16 v[238:239], v50 offset:12552\n\t"
    "v_mul_f32 v79, v63, v79\n\t"
    "v_mfma_f32_16x16x32_bf16 v[120:123], a[8:11], v[20:23], v[120:123]\n\t"
    "v_cvt_pk_bf16_f32 v72, v72, v73\n\t"
    "v_cvt_pk_bf16_f32 v73, v74, v75\n\t"
    "v_mfma_f32_16x16x32_bf16 v[124:127], a[12:15], v[20:23], v[124:127]\n\t"
    "ds_read_b64_tr_b16 v[240:241], v50 offset:8704\n\t"
    "v_cvt_pk_bf16_f32 v74, v76, v77\n\t"
    "v_cvt_pk_bf16_f32 v75, v78, v79\n\t"
    "v_mfma_f32_16x16x32_bf16 v[128:131], a[16:19], v[20:23], v[128:131]\n\t"
    "buffer_store_dwordx4 v[72:75], v54, s[44:47], s55 offen offset:64 sc1\n\t"
    "v_mfma_f32_16x16x32_bf16 v[132:135], a[20:23], v[20:23], v[132:135]\n\t"
    "ds_read_b64_tr_b16 v[242:243], v50 offset:12800\n\t"
    "v_accvgpr_read_b32 v64, a112\n\t"
    "v_mfma_f32_16x16x32_bf16 v[136:139], a[24:27], v[20:23], v[136:139]\n\t"
    "buffer_load_dwordx4 v52, s[40:43], s69 offen offset:0 lds\n\t"
    "v_mfma_f32_16x16x32_bf16 v[140:143], a[28:31], v[20:23], v[140:143]\n\t"
    "ds_read_b64_tr_b16 v[244:245], v50 offset:8712\n\t"
    "v_accvgpr_read_b32 v65, a113\n\t"
    "v_mfma_f32_16x16x32_bf16 v[8:11], a[192:195], v[176:179], v[24:27]\n\t"
    "v_exp_f32 v0, v0\n\t"
    "v_mfma_f32_16x16x32_bf16 v[8:11], a[196:199], v[180:183], v[8:11]\n\t"
    "ds_read_b64_tr_b16 v[246:247], v50 offset:12808\n\t"
    "v_accvgpr_read_b32 v66, a114\n\t"
    "v_mfma_f32_16x16x32_bf16 v[8:11], a[200:203], v[184:187], v[8:11]\n\t"
    "v_exp_f32 v1, v1\n\t"
    "v_mfma_f32_16x16x32_bf16 v[8:11], a[204:207], v[188:191], v[8:11]\n\t"
    "ds_read_b64_tr_b16 v[248:249], v50 offset:8960\n\t"
    "v_cvt_pk_bf16_f32 v16, v0, v1\n\t"
    "v_mfma_f32_16x16x32_bf16 v[12:15], a[208:211], v[176:179], v[24:27]\n\t"
    "v_exp_f32 v2, v2\n\t"
    "v_mfma_f32_16x16x32_bf16 v[12:15], a[212:215], v[180:183], v[12:15]\n\t"
    "ds_read_b64_tr_b16 v[250:251], v50 offset:13056\n\t"
    "v_exp_f32 v3, v3\n\t"
    "v_exp_f32 v4, v4\n\t"
    "v_exp_f32 v5, v5\n\t"
    "v_exp_f32 v6, v6\n\t"
    "v_exp_f32 v7, v7\n\t"
    "v_mfma_f32_16x16x32_bf16 v[12:15], a[216:219], v[184:187], v[12:15]\n\t"
    "v_cvt_pk_bf16_f32 v17, v2, v3\n\t"
    "v_cvt_pk_bf16_f32 v18, v4, v5\n\t"
    "v_cvt_pk_bf16_f32 v19, v6, v7\n\t"
    "v_mfma_f32_16x16x32_bf16 v[12:15], a[220:223], v[188:191], v[12:15]\n\t"
    "ds_read_b64_tr_b16 v[252:253], v50 offset:8968\n\t"
    "v_accvgpr_read_b32 v67, a115\n\t"
    "v_mfma_f32_16x16x32_bf16 v[44:47], a[248:251], v[16:19], v[44:47]\n\t"
    "buffer_load_dwordx4 v53, s[40:43], s69 offen offset:1024 lds\n\t"
    "v_mfma_f32_16x16x32_bf16 v[144:147], v[224:227], v[16:19], v[144:147]\n\t"
    "ds_read_b64_tr_b16 v[254:255], v50 offset:13064\n\t"
    "v_accvgpr_read_b32 v68, a116\n\t"
    "v_mfma_f32_16x16x32_bf16 v[148:151], v[228:231], v[16:19], v[148:151]\n\t"
    "s_waitcnt lgkmcnt(4)\n\t"
    "ds_read_b128 a[32:35], v48 offset:16384\n\t"
    "v_accvgpr_read_b32 v69, a117\n\t"
    "v_mfma_f32_16x16x32_bf16 v[152:155], v[232:235], v[16:19], v[152:155]\n\t"
    "v_exp_f32 v8, v8\n\t"
    "v_mfma_f32_16x16x32_bf16 v[156:159], v[236:239], v[16:19], v[156:159]\n\t"
    "v_exp_f32 v9, v9\n\t"
    "v_mfma_f32_16x16x32_bf16 v[160:163], v[240:243], v[16:19], v[160:163]\n\t"
    "v_exp_f32 v10, v10\n\t"
    "v_mfma_f32_16x16x32_bf16 v[164:167], v[244:247], v[16:19], v[164:167]\n\t"
    "s_waitcnt lgkmcnt(3)\n\t"
    "v_exp_f32 v11, v11\n\t"
    "v_mfma_f32_16x16x32_bf16 v[168:171], v[248:251], v[16:19], v[168:171]\n\t"
    "s_waitcnt lgkmcnt(1)\n\t"
    "v_exp_f32 v12, v12\n\t"
    "v_mfma_f32_16x16x32_bf16 v[172:175], v[252:255], v[16:19], v[172:175]\n\t"
    "v_exp_f32 v13, v13\n\t"
    "v_mfma_f32_16x16x32_bf16 v[0:3], a[192:195], v[192:195], v[28:31]\n\t"
    "v_exp_f32 v14, v14\n\t"
    "v_mfma_f32_16x16x32_bf16 v[0:3], a[196:199], v[196:199], v[0:3]\n\t"
    "v_exp_f32 v15, v15\n\t"
    "v_mfma_f32_16x16x32_bf16 v[0:3], a[200:203], v[200:203], v[0:3]\n\t"
    "ds_read_b128 a[36:39], v48 offset:16640\n\t"
    "v_accvgpr_read_b32 v70, a118\n\t"
    "v_mfma_f32_16x16x32_bf16 v[0:3], a[204:207], v[204:207], v[0:3]\n\t"
    "v_accvgpr_read_b32 v71, a119\n\t"
    "v_mul_f32 v64, v63, v64\n\t"
    "v_mfma_f32_16x16x32_bf16 v[4:7], a[208:211], v[192:195], v[28:31]\n\t"
    "v_mul_f32 v65, v63, v65\n\t"
    "v_mul_f32 v66, v63, v66\n\t"
    "v_mfma_f32_16x16x32_bf16 v[4:7], a[212:215], v[196:199], v[4:7]\n\t"
    "v_mul_f32 v67, v63, v67\n\t"
    "v_mul_f32 v68, v63, v68\n\t"
    "v_mfma_f32_16x16x32_bf16 v[4:7], a[216:219], v[200:203], v[4:7]\n\t"
    "v_mul_f32 v69, v63, v69\n\t"
    "v_mul_f32 v70, v63, v70\n\t"
    "v_mul_f32 v71, v63, v71\n\t"
    "v_cvt_pk_bf16_f32 v64, v64, v65\n\t"
    "v_cvt_pk_bf16_f32 v65, v66, v67\n\t"
    "v_cvt_pk_bf16_f32 v66, v68, v69\n\t"
    "v_cvt_pk_bf16_f32 v67, v70, v71\n\t"
    "v_cvt_pk_bf16_f32 v20, v8, v9\n\t"
    "v_cvt_pk_bf16_f32 v21, v10, v11\n\t"
    "v_cvt_pk_bf16_f32 v22, v12, v13\n\t"
    "v_cvt_pk_bf16_f32 v23, v14, v15\n\t"
    "v_mfma_f32_16x16x32_bf16 v[4:7], a[220:223], v[204:207], v[4:7]\n\t"
    "buffer_store_dwordx4 v[64:67], v54, s[44:47], s55 offen offset:128 sc1\n\t"
    "ds_read_b128 a[40:43], v48 offset:16896\n\t"
    "v_mfma_f32_16x16x32_bf16 v[36:39], a[248:251], v[20:23], v[36:39]\n\t"
    "ds_read_b64_tr_b16 a[0:1], v50 offset:16384\n\t"
    "v_accvgpr_read_b32 v72, a120\n\t"
    "v_mfma_f32_16x16x32_bf16 v[80:83], v[224:227], v[20:23], v[80:83]\n\t"
    "ds_read_b128 a[44:47], v48 offset:17152\n\t"
    "v_accvgpr_read_b32 v73, a121\n\t"
    "v_mfma_f32_16x16x32_bf16 v[84:87], v[228:231], v[20:23], v[84:87]\n\t"
    "ds_read_b64_tr_b16 a[2:3], v50 offset:20480\n\t"
    "v_accvgpr_read_b32 v74, a122\n\t"
    "v_mfma_f32_16x16x32_bf16 v[88:91], v[232:235], v[20:23], v[88:91]\n\t"
    "ds_read_b128 a[48:51], v48 offset:20480\n\t"
    "v_accvgpr_read_b32 v75, a123\n\t"
    "v_mfma_f32_16x16x32_bf16 v[92:95], v[236:239], v[20:23], v[92:95]\n\t"
    "ds_read_b64_tr_b16 a[4:5], v50 offset:16392\n\t"
    "v_accvgpr_read_b32 v76, a124\n\t"
    "v_mfma_f32_16x16x32_bf16 v[96:99], v[240:243], v[20:23], v[96:99]\n\t"
    "ds_read_b128 a[52:55], v48 offset:20736\n\t"
    "v_accvgpr_read_b32 v77, a125\n\t"
    "v_mfma_f32_16x16x32_bf16 v[100:103], v[244:247], v[20:23], v[100:103]\n\t"
    "ds_read_b64_tr_b16 a[6:7], v50 offset:20488\n\t"
    "v_accvgpr_read_b32 v78, a126\n\t"
    "v_mfma_f32_16x16x32_bf16 v[104:107], v[248:251], v[20:23], v[104:107]\n\t"
    "ds_read_b128 a[56:59], v48 offset:20992\n\t"
    "v_accvgpr_read_b32 v79, a127\n\t"
    "v_mfma_f32_16x16x32_bf16 v[108:111], v[252:255], v[20:23], v[108:111]\n\t"
    "s_waitcnt lgkmcnt(7)\n\t"
    "ds_read_b64_tr_b16 a[8:9], v50 offset:16640\n\t"
    "v_mul_f32 v72, v63, v72\n\t"
    "v_mfma_f32_16x16x32_bf16 v[8:11], a[32:35], v[208:211], v[32:35]\n\t"
    "ds_read_b128 a[60:63], v48 offset:21248\n\t"
    "v_mul_f32 v73, v63, v73\n\t"
    "v_mfma_f32_16x16x32_bf16 v[8:11], a[36:39], v[212:215], v[8:11]\n\t"
    "v_exp_f32 v0, v0\n\t"
    "v_mfma_f32_16x16x32_bf16 v[8:11], a[40:43], v[216:219], v[8:11]\n\t"
    "s_waitcnt lgkmcnt(6)\n\t"
    "v_exp_f32 v1, v1\n\t"
    "v_mfma_f32_16x16x32_bf16 v[8:11], a[44:47], v[220:223], v[8:11]\n\t"
    "v_exp_f32 v2, v2\n\t"
    "v_mfma_f32_16x16x32_bf16 v[12:15], a[48:51], v[208:211], v[32:35]\n\t"
    "s_waitcnt lgkmcnt(4)\n\t"
    "v_exp_f32 v3, v3\n\t"
    "v_mfma_f32_16x16x32_bf16 v[12:15], a[52:55], v[212:215], v[12:15]\n\t"
    "s_waitcnt lgkmcnt(2)\n\t"
    "v_exp_f32 v4, v4\n\t"
    "v_exp_f32 v5, v5\n\t"
    "v_exp_f32 v6, v6\n\t"
    "v_exp_f32 v7, v7\n\t"
    "v_mfma_f32_16x16x32_bf16 v[12:15], a[56:59], v[216:219], v[12:15]\n\t"
    "s_waitcnt lgkmcnt(0)\n\t"
    "ds_read_b64_tr_b16 a[10:11], v50 offset:20736\n\t"
    "v_cvt_pk_bf16_f32 v16, v0, v1\n\t"
    "v_cvt_pk_bf16_f32 v17, v2, v3\n\t"
    "v_cvt_pk_bf16_f32 v18, v4, v5\n\t"
    "v_cvt_pk_bf16_f32 v19, v6, v7\n\t"
    "v_mfma_f32_16x16x32_bf16 v[12:15], a[60:63], v[220:223], v[12:15]\n\t"
    "v_mul_f32 v74, v63, v74\n\t"
    "v_mul_f32 v75, v63, v75\n\t"
    "v_mfma_f32_16x16x32_bf16 v[40:43], a[248:251], v[16:19], v[40:43]\n\t"
    "ds_read_b64_tr_b16 a[12:13], v50 offset:16648\n\t"
    "v_mul_f32 v76, v63, v76\n\t"
    "v_mfma_f32_16x16x32_bf16 v[112:115], v[224:227], v[16:19], v[112:115]\n\t"
    "v_mul_f32 v77, v63, v77\n\t"
    "v_mul_f32 v78, v63, v78\n\t"
    "v_mfma_f32_16x16x32_bf16 v[116:119], v[228:231], v[16:19], v[116:119]\n\t"
    "ds_read_b64_tr_b16 a[14:15], v50 offset:20744\n\t"
    "v_mul_f32 v79, v63, v79\n\t"
    "v_mfma_f32_16x16x32_bf16 v[120:123], v[232:235], v[16:19], v[120:123]\n\t"
    "v_cvt_pk_bf16_f32 v72, v72, v73\n\t"
    "v_cvt_pk_bf16_f32 v73, v74, v75\n\t"
    "v_mfma_f32_16x16x32_bf16 v[124:127], v[236:239], v[16:19], v[124:127]\n\t"
    "ds_read_b64_tr_b16 a[16:17], v50 offset:16896\n\t"
    "v_cvt_pk_bf16_f32 v74, v76, v77\n\t"
    "v_mfma_f32_16x16x32_bf16 v[128:131], v[240:243], v[16:19], v[128:131]\n\t"
    "ds_read_b128 a[192:195], v48 offset:24576\n\t"
    "v_cvt_pk_bf16_f32 v75, v78, v79\n\t"
    "v_mfma_f32_16x16x32_bf16 v[132:135], v[244:247], v[16:19], v[132:135]\n\t"
    "ds_read_b64_tr_b16 a[18:19], v50 offset:20992\n\t"
    "v_cmp_nle_f32_e64 s[22:23], v62, 4.0\n\t"
    "v_mfma_f32_16x16x32_bf16 v[136:139], v[248:251], v[16:19], v[136:139]\n\t"
    "s_nop 0\n\t"
    "s_or_b64 s[84:85], s[84:85], s[22:23]\n\t"
    "buffer_store_dwordx4 v[72:75], v54, s[44:47], s55 offen offset:192 sc1\n\t"
    "v_mfma_f32_16x16x32_bf16 v[140:143], v[252:255], v[16:19], v[140:143]\n\t"
    "ds_read_b64_tr_b16 a[20:21], v50 offset:16904\n\t"
    "v_accvgpr_read_b32 v62, a240\n\t"
    "v_mfma_f32_16x16x32_bf16 v[0:3], a[32:35], v[176:179], v[24:27]\n\t"
    "v_exp_f32 v8, v8\n\t"
    "v_mfma_f32_16x16x32_bf16 v[0:3], a[36:39], v[180:183], v[0:3]\n\t"
    "ds_read_b64_tr_b16 a[22:23], v50 offset:21000\n\t"
    "v_cmp_nle_f32_e64 s[88:89], v62, s50\n\t"
    "v_mfma_f32_16x16x32_bf16 v[0:3], a[40:43], v[184:187], v[0:3]\n\t"
    "v_exp_f32 v9, v9\n\t"
    "v_mfma_f32_16x16x32_bf16 v[0:3], a[44:47], v[188:191], v[0:3]\n\t"
    "ds_read_b64_tr_b16 a[24:25], v50 offset:17152\n\t"
    "v_cvt_pk_bf16_f32 v20, v8, v9\n\t"
    "v_mfma_f32_16x16x32_bf16 v[4:7], a[48:51], v[176:179], v[24:27]\n\t"
    "v_exp_f32 v10, v10\n\t"
    "v_mfma_f32_16x16x32_bf16 v[4:7], a[52:55], v[180:183], v[4:7]\n\t"
    "ds_read_b64_tr_b16 a[26:27], v50 offset:21248\n\t"
    "v_exp_f32 v11, v11\n\t"
    "v_exp_f32 v12, v12\n\t"
    "v_exp_f32 v13, v13\n\t"
    "v_exp_f32 v14, v14\n\t"
    "v_exp_f32 v15, v15\n\t"
    "v_mfma_f32_16x16x32_bf16 v[4:7], a[56:59], v[184:187], v[4:7]\n\t"
    "v_cvt_pk_bf16_f32 v21, v10, v11\n\t"
    "v_cvt_pk_bf16_f32 v22, v12, v13\n\t"
    "v_cvt_pk_bf16_f32 v23, v14, v15\n\t"
    "v_mfma_f32_16x16x32_bf16 v[4:7], a[60:63], v[188:191], v[4:7]\n\t"
    "ds_read_b64_tr_b16 a[28:29], v50 offset:17160\n\t"
    "v_accvgpr_read_b32 v64, a128\n\t"
    "v_mfma_f32_16x16x32_bf16 v[44:47], a[248:251], v[20:23], v[44:47]\n\t"
    "v_exp_f32 v0, v0\n\t"
    "v_mfma_f32_16x16x32_bf16 v[144:147], a[0:3], v[20:23], v[144:147]\n\t"
    "ds_read_b64_tr_b16 a[30:31], v50 offset:21256\n\t"
    "v_accvgpr_read_b32 v65, a129\n\t"
    "v_mfma_f32_16x16x32_bf16 v[148:151], a[4:7], v[20:23], v[148:151]\n\t"
    "s_waitcnt lgkmcnt(4)\n\t"
    "v_exp_f32 v1, v1\n\t"
    "v_mfma_f32_16x16x32_bf16 v[152:155], a[8:11], v[20:23], v[152:155]\n\t"
    "v_exp_f32 v2, v2\n\t"
    "v_mfma_f32_16x16x32_bf16 v[156:159], a[12:15], v[20:23], v[156:159]\n\t"
    "v_exp_f32 v3, v3\n\t"
    "v_mfma_f32_16x16x32_bf16 v[160:163], a[16:19], v[20:23], v[160:163]\n\t"
    "v_exp_f32 v4, v4\n\t"
    "v_mfma_f32_16x16x32_bf16 v[164:167], a[20:23], v[20:23], v[164:167]\n\t"
    "s_waitcnt lgkmcnt(2)\n\t"
    "v_exp_f32 v5, v5\n\t"
    "v_mfma_f32_16x16x32_bf16 v[168:171], a[24:27], v[20:23], v[168:171]\n\t"
    "s_waitcnt lgkmcnt(0)\n\t"
    "v_exp_f32 v6, v6\n\t"
    "v_mfma_f32_16x16x32_bf16 v[172:175], a[28:31], v[20:23], v[172:175]\n\t"
    "v_exp_f32 v7, v7\n\t"
    "v_mfma_f32_16x16x32_bf16 v[8:11], a[32:35], v[192:195], v[28:31]\n\t"
    "ds_read_b128 a[196:199], v48 offset:24832\n\t"
    "v_cvt_pk_bf16_f32 v16, v0, v1\n\t"
    "v_mfma_f32_16x16x32_bf16 v[8:11], a[36:39], v[196:199], v[8:11]\n\t"
    "v_cvt_pk_bf16_f32 v17, v2, v3\n\t"
    "v_cvt_pk_bf16_f32 v18, v4, v5\n\t"
    "v_mfma_f32_16x16x32_bf16 v[8:11], a[40:43], v[200:203], v[8:11]\n\t"
    "ds_read_b128 a[200:203], v48 offset:25088\n\t"
    "v_cvt_pk_bf16_f32 v19, v6, v7\n\t"
    "v_mfma_f32_16x16x32_bf16 v[8:11], a[44:47], v[204:207], v[8:11]\n\t"
    "v_rcp_f32 v62, v62\n\t"
    "v_mfma_f32_16x16x32_bf16 v[12:15], a[48:51], v[192:195], v[28:31]\n\t"
    "ds_read_b128 a[204:207], v48 offset:25344\n\t"
    "v_accvgpr_read_b32 v66, a130\n\t"
    "v_mfma_f32_16x16x32_bf16 v[12:15], a[52:55], v[196:199], v[12:15]\n\t"
    "v_accvgpr_read_b32 v67, a131\n\t"
    "v_accvgpr_read_b32 v68, a132\n\t"
    "v_mfma_f32_16x16x32_bf16 v[12:15], a[56:59], v[200:203], v[12:15]\n\t"
    "v_accvgpr_read_b32 v69, a133\n\t"
    "v_accvgpr_read_b32 v70, a134\n\t"
    "v_mfma_f32_16x16x32_bf16 v[12:15], a[60:63], v[204:207], v[12:15]\n\t"
    "v_accvgpr_read_b32 v71, a135\n\t"
    "v_mul_f32 v64, v62, v64\n\t"
    "v_mfma_f32_16x16x32_bf16 v[36:39], a[248:251], v[16:19], v[36:39]\n\t"
    "v_mul_f32 v65, v62, v65\n\t"
    "v_mul_f32 v66, v62, v66\n\t"
    "v_mfma_f32_16x16x32_bf16 v[80:83], a[0:3], v[16:19], v[80:83]\n\t"
    "v_mul_f32 v67, v62, v67\n\t"
    "v_mul_f32 v68, v62, v68\n\t"
    "v_mfma_f32_16x16x32_bf16 v[84:87], a[4:7], v[16:19], v[84:87]\n\t"
    "v_mul_f32 v69, v62, v69\n\t"
    "v_mul_f32 v70, v62, v70\n\t"
    "v_mfma_f32_16x16x32_bf16 v[88:91], a[8:11], v[16:19], v[88:91]\n\t"
    "ds_read_b128 a[208:211], v48 offset:28672\n\t"
    "v_mul_f32 v71, v62, v71\n\t"
    "v_cvt_pk_bf16_f32 v64, v64, v65\n\t"
    "v_cvt_pk_bf16_f32 v65, v66, v67\n\t"
    "v_cvt_pk_bf16_f32 v66, v68, v69\n\t"
    "v_cvt_pk_bf16_f32 v67, v70, v71\n\t"
    "v_mfma_f32_16x16x32_bf16 v[92:95], a[12:15], v[16:19], v[92:95]\n\t"
    "buffer_store_dwordx4 v[64:67], v54, s[44:47], s56 offen offset:0 sc1\n\t"
    "v_mfma_f32_16x16x32_bf16 v[96:99], a[16:19], v[16:19], v[96:99]\n\t"
    "ds_read_b128 a[212:215], v48 offset:28928\n\t"
    "ds_read_b64_tr_b16 v[224:225], v50 offset:24576\n\t"
    "v_mfma_f32_16x16x32_bf16 v[100:103], a[20:23], v[16:19], v[100:103]\n\t"
    "v_exp_f32 v8, v8\n\t"
    "v_mfma_f32_16x16x32_bf16 v[104:107], a[24:27], v[16:19], v[104:107]\n\t"
    "ds_read_b128 a[216:219], v48 offset:29184\n\t"
    "ds_read_b64_tr_b16 v[226:227], v50 offset:28672\n\t"
    "v_mfma_f32_16x16x32_bf16 v[108:111], a[28:31], v[16:19], v[108:111]\n\t"
    "v_exp_f32 v9, v9\n\t"
    "v_mfma_f32_16x16x32_bf16 v[0:3], a[192:195], v[208:211], v[32:35]\n\t"
    "s_waitcnt lgkmcnt(5)\n\t"
    "ds_read_b128 a[220:223], v48 offset:29440\n\t"
    "ds_read_b64_tr_b16 v[228:229], v50 offset:24584\n\t"
    "v_mfma_f32_16x16x32_bf16 v[0:3], a[196:199], v[212:215], v[0:3]\n\t"
    "v_exp_f32 v10, v10\n\t"
    "v_mfma_f32_16x16x32_bf16 v[0:3], a[200:203], v[216:219], v[0:3]\n\t"
    "ds_read_b64_tr_b16 v[230:231], v50 offset:28680\n\t"
    "v_cvt_pk_bf16_f32 v20, v8, v9\n\t"
    "v_mfma_f32_16x16x32_bf16 v[0:3], a[204:207], v[220:223], v[0:3]\n\t"
    "s_waitcnt lgkmcnt(7)\n\t"
    "v_exp_f32 v11, v11\n\t"
    "v_mfma_f32_16x16x32_bf16 v[4:7], a[208:211], v[208:211], v[32:35]\n\t"
    "s_waitcnt lgkmcnt(5)\n\t"
    "ds_read_b64_tr_b16 v[232:233], v50 offset:24832\n\t"
    "v_cvt_pk_bf16_f32 v21, v10, v11\n\t"
    "v_mfma_f32_16x16x32_bf16 v[4:7], a[212:215], v[212:215], v[4:7]\n\t"
    "s_waitcnt lgkmcnt(5)\n\t"
    "v_exp_f32 v12, v12\n\t"
    "v_exp_f32 v13, v13\n\t"
    "v_exp_f32 v14, v14\n\t"
    "v_exp_f32 v15, v15\n\t"
    "v_mfma_f32_16x16x32_bf16 v[4:7], a[216:219], v[216:219], v[4:7]\n\t"
    "s_waitcnt lgkmcnt(3)\n\t"
    "ds_read_b64_tr_b16 v[234:235], v50 offset:28928\n\t"
    "v_cvt_pk_bf16_f32 v22, v12, v13\n\t"
    "v_cvt_pk_bf16_f32 v23, v14, v15\n\t"
    "v_mfma_f32_16x16x32_bf16 v[4:7], a[220:223], v[220:223], v[4:7]\n\t"
    "v_accvgpr_read_b32 v72, a136\n\t"
    "v_accvgpr_read_b32 v73, a137\n\t"
    "v_mfma_f32_16x16x32_bf16 v[40:43], a[248:251], v[20:23], v[40:43]\n\t"
    "ds_read_b64_tr_b16 v[236:237], v50 offset:24840\n\t"
    "v_accvgpr_read_b32 v74, a138\n\t"
    "v_mfma_f32_16x16x32_bf16 v[112:115], a[0:3], v[20:23], v[112:115]\n\t"
    "v_accvgpr_read_b32 v75, a139\n\t"
    "v_accvgpr_read_b32 v76, a140\n\t"
    "v_mfma_f32_16x16x32_bf16 v[116:119], a[4:7], v[20:23], v[116:119]\n\t"
    "ds_read_b64_tr_b16 v[238:239], v50 offset:28936\n\t"
    "v_accvgpr_read_b32 v77, a141\n\t"
    "v_mfma_f32_16x16x32_bf16 v[120:123], a[8:11], v[20:23], v[120:123]\n\t"
    "v_accvgpr_read_b32 v78, a142\n\t"
    "v_accvgpr_read_b32 v79, a143\n\t"
    "v_mfma_f32_16x16x32_bf16 v[124:127], a[12:15], v[20:23], v[124:127]\n\t"
    "ds_read_b64_tr_b16 v[240:241], v50 offset:25088\n\t"
    "v_mul_f32 v72, v62, v72\n\t"
    "v_mfma_f32_16x16x32_bf16 v[128:131], a[16:19], v[20:23], v[128:131]\n\t"
    "v_mul_f32 v73, v62, v73\n\t"
    "v_mul_f32 v74, v62, v74\n\t"
    "v_mfma_f32_16x16x32_bf16 v[132:135], a[20:23], v[20:23], v[132:135]\n\t"
    "ds_read_b64_tr_b16 v[242:243], v50 offset:29184\n\t"
    "v_mul_f32 v75, v62, v75\n\t"
    "v_mfma_f32_16x16x32_bf16 v[136:139], a[24:27], v[20:23], v[136:139]\n\t"
    "v_mul_f32 v76, v62, v76\n\t"
    "v_mul_f32 v77, v62, v77\n\t"
    "v_mfma_f32_16x16x32_bf16 v[140:143], a[28:31], v[20:23], v[140:143]\n\t"
    "ds_read_b64_tr_b16 v[244:245], v50 offset:25096\n\t"
    "v_mul_f32 v78, v62, v78\n\t"
    "v_mfma_f32_16x16x32_bf16 v[8:11], a[192:195], v[176:179], v[24:27]\n\t"
    "v_mul_f32 v79, v62, v79\n\t"
    "v_cvt_pk_bf16_f32 v72, v72, v73\n\t"
    "v_mfma_f32_16x16x32_bf16 v[8:11], a[196:199], v[180:183], v[8:11]\n\t"
    "ds_read_b64_tr_b16 v[246:247], v50 offset:29192\n\t"
    "v_cvt_pk_bf16_f32 v73, v74, v75\n\t"
    "v_mfma_f32_16x16x32_bf16 v[8:11], a[200:203], v[184:187], v[8:11]\n\t"
    "v_cvt_pk_bf16_f32 v74, v76, v77\n\t"
    "v_cvt_pk_bf16_f32 v75, v78, v79\n\t"
    "v_mfma_f32_16x16x32_bf16 v[8:11], a[204:207], v[188:191], v[8:11]\n\t"
    "ds_read_b64_tr_b16 v[248:249], v50 offset:25344\n\t"
    "v_accvgpr_read_b32 v64, a144\n\t"
    "v_mfma_f32_16x16x32_bf16 v[12:15], a[208:211], v[176:179], v[24:27]\n\t"
    "v_exp_f32 v0, v0\n\t"
    "v_mfma_f32_16x16x32_bf16 v[12:15], a[212:215], v[180:183], v[12:15]\n\t"
    "ds_read_b64_tr_b16 v[250:251], v50 offset:29440\n\t"
    "v_exp_f32 v1, v1\n\t"
    "v_exp_f32 v2, v2\n\t"
    "v_exp_f32 v3, v3\n\t"
    "v_exp_f32 v4, v4\n\t"
    "v_exp_f32 v5, v5\n\t"
    "v_exp_f32 v6, v6\n\t"
    "v_exp_f32 v7, v7\n\t"
    "v_mfma_f32_16x16x32_bf16 v[12:15], a[216:219], v[184:187], v[12:15]\n\t"
    "buffer_store_dwordx4 v[72:75], v54, s[44:47], s56 offen offset:64 sc1\n\t"
    "v_cvt_pk_bf16_f32 v16, v0, v1\n\t"
    "v_cvt_pk_bf16_f32 v17, v2, v3\n\t"
    "v_cvt_pk_bf16_f32 v18, v4, v5\n\t"
    "v_cvt_pk_bf16_f32 v19, v6, v7\n\t"
    "v_mfma_f32_16x16x32_bf16 v[12:15], a[220:223], v[188:191], v[12:15]\n\t"
    "ds_read_b64_tr_b16 v[252:253], v50 offset:25352\n\t"
    "v_accvgpr_read_b32 v65, a145\n\t"
    "v_mfma_f32_16x16x32_bf16 v[44:47], a[248:251], v[16:19], v[44:47]\n\t"
    "ds_read_b128 a[32:35], v48 offset:32768\n\t"
    "v_accvgpr_read_b32 v66, a146\n\t"
    "v_mfma_f32_16x16x32_bf16 v[144:147], v[224:227], v[16:19], v[144:147]\n\t"
    "s_waitcnt lgkmcnt(4)\n\t"
    "ds_read_b64_tr_b16 v[254:255], v50 offset:29448\n\t"
    "v_accvgpr_read_b32 v67, a147\n\t"
    "v_mfma_f32_16x16x32_bf16 v[148:151], v[228:231], v[16:19], v[148:151]\n\t"
    "v_exp_f32 v8, v8\n\t"
    "v_mfma_f32_16x16x32_bf16 v[152:155], v[232:235], v[16:19], v[152:155]\n\t"
    "v_exp_f32 v9, v9\n\t"
    "v_mfma_f32_16x16x32_bf16 v[156:159], v[236:239], v[16:19], v[156:159]\n\t"
    "v_exp_f32 v10, v10\n\t"
    "v_mfma_f32_16x16x32_bf16 v[160:163], v[240:243], v[16:19], v[160:163]\n\t"
    "v_exp_f32 v11, v11\n\t"
    "v_mfma_f32_16x16x32_bf16 v[164:167], v[244:247], v[16:19], v[164:167]\n\t"
    "s_waitcnt lgkmcnt(3)\n\t"
    "v_exp_f32 v12, v12\n\t"
    "v_mfma_f32_16x16x32_bf16 v[168:171], v[248:251], v[16:19], v[168:171]\n\t"
    "s_waitcnt lgkmcnt(0)\n\t"
    "v_exp_f32 v13, v13\n\t"
    "v_mfma_f32_16x16x32_bf16 v[172:175], v[252:255], v[16:19], v[172:175]\n\t"
    "s_waitcnt vmcnt(22)\n\t"
    "v_exp_f32 v14, v14\n\t"
    "v_mfma_f32_16x16x32_bf16 v[0:3], a[192:195], v[192:195], v[28:31]\n\t"
    "s_barrier\n\t"
    "v_exp_f32 v15, v15\n\t"
    "v_mfma_f32_16x16x32_bf16 v[0:3], a[196:199], v[196:199], v[0:3]\n\t"
    "ds_read_b128 a[36:39], v48 offset:33024\n\t"
    "v_cvt_pk_bf16_f32 v20, v8, v9\n\t"
    "v_mfma_f32_16x16x32_bf16 v[0:3], a[200:203], v[200:203], v[0:3]\n\t"
    "v_cvt_pk_bf16_f32 v21, v10, v11\n\t"
    "v_cvt_pk_bf16_f32 v22, v12, v13\n\t"
    "v_mfma_f32_16x16x32_bf16 v[0:3], a[204:207], v[204:207], v[0:3]\n\t"
    "ds_read_b128 a[40:43], v48 offset:33280\n\t"
    "v_cvt_pk_bf16_f32 v23, v14, v15\n\t"
    "v_mfma_f32_16x16x32_bf16 v[4:7], a[208:211], v[192:195], v[28:31]\n\t"
    "ds_read_b64_tr_b16 a[0:1], v50 offset:32768\n\t"
    "v_accvgpr_read_b32 v68, a148\n\t"
    "v_mfma_f32_16x16x32_bf16 v[4:7], a[212:215], v[196:199], v[4:7]\n\t"
    "ds_read_b128 a[44:47], v48 offset:33536\n\t"
    "v_accvgpr_read_b32 v69, a149\n\t"
    "v_mfma_f32_16x16x32_bf16 v[4:7], a[216:219], v[200:203], v[4:7]\n\t"
    "ds_read_b64_tr_b16 a[2:3], v50 offset:36864\n\t"
    "v_accvgpr_read_b32 v70, a150\n\t"
    "v_mfma_f32_16x16x32_bf16 v[4:7], a[220:223], v[204:207], v[4:7]\n\t"
    "ds_read_b128 a[48:51], v48 offset:36864\n\t"
    "v_accvgpr_read_b32 v71, a151\n\t"
    "v_mfma_f32_16x16x32_bf16 v[36:39], a[248:251], v[20:23], v[36:39]\n\t"
    "v_mul_f32 v64, v62, v64\n\t"
    "v_mul_f32 v65, v62, v65\n\t"
    "v_mfma_f32_16x16x32_bf16 v[80:83], v[224:227], v[20:23], v[80:83]\n\t"
    "ds_read_b128 a[52:55], v48 offset:37120\n\t"
    "v_mul_f32 v66, v62, v66\n\t"
    "v_mfma_f32_16x16x32_bf16 v[84:87], v[228:231], v[20:23], v[84:87]\n\t"
    "v_mul_f32 v67, v62, v67\n\t"
    "v_mul_f32 v68, v62, v68\n\t"
    "v_mfma_f32_16x16x32_bf16 v[88:91], v[232:235], v[20:23], v[88:91]\n\t"
    "ds_read_b128 a[56:59], v48 offset:37376\n\t"
    "v_mul_f32 v69, v62, v69\n\t"
    "v_mfma_f32_16x16x32_bf16 v[92:95], v[236:239], v[20:23], v[92:95]\n\t"
    "v_mul_f32 v70, v62, v70\n\t"
    "v_mul_f32 v71, v62, v71\n\t"
    "v_mfma_f32_16x16x32_bf16 v[96:99], v[240:243], v[20:23], v[96:99]\n\t"
    "v_cvt_pk_bf16_f32 v64, v64, v65\n\t"
    "v_cvt_pk_bf16_f32 v65, v66, v67\n\t"
    "v_mfma_f32_16x16x32_bf16 v[100:103], v[244:247], v[20:23], v[100:103]\n\t"
    "v_cvt_pk_bf16_f32 v66, v68, v69\n\t"
    "v_cvt_pk_bf16_f32 v67, v70, v71\n\t"
    "v_mfma_f32_16x16x32_bf16 v[104:107], v[248:251], v[20:23], v[104:107]\n\t"
    "buffer_store_dwordx4 v[64:67], v54, s[44:47], s56 offen offset:128 sc1\n\t"
    "v_mfma_f32_16x16x32_bf16 v[108:111], v[252:255], v[20:23], v[108:111]\n\t"
    "ds_read_b128 a[60:63], v48 offset:37632\n\t"
    "v_accvgpr_read_b32 v72, a152\n\t"
    "v_mfma_f32_16x16x32_bf16 v[8:11], a[32:35], v[208:211], v[32:35]\n\t"
    "s_waitcnt lgkmcnt(2)\n\t"
    "ds_read_b64_tr_b16 a[4:5], v50 offset:32776\n\t"
    "v_accvgpr_read_b32 v73, a153\n\t"
    "v_mfma_f32_16x16x32_bf16 v[8:11], a[36:39], v[212:215], v[8:11]\n\t"
    "v_exp_f32 v0, v0\n\t"
    "v_mfma_f32_16x16x32_bf16 v[8:11], a[40:43], v[216:219], v[8:11]\n\t"
    "ds_read_b64_tr_b16 a[6:7], v50 offset:36872\n\t"
    "v_accvgpr_read_b32 v74, a154\n\t"
    "v_mfma_f32_16x16x32_bf16 v[8:11], a[44:47], v[220:223], v[8:11]\n\t"
    "v_exp_f32 v1, v1\n\t"
    "v_mfma_f32_16x16x32_bf16 v[12:15], a[48:51], v[208:211], v[32:35]\n\t"
    "ds_read_b64_tr_b16 a[8:9], v50 offset:33024\n\t"
    "v_cvt_pk_bf16_f32 v16, v0, v1\n\t"
    "v_mfma_f32_16x16x32_bf16 v[12:15], a[52:55], v[212:215], v[12:15]\n\t"
    "s_waitcnt lgkmcnt(4)\n\t"
    "v_exp_f32 v2, v2\n\t"
    "v_exp_f32 v3, v3\n\t"
    "v_exp_f32 v4, v4\n\t"
    "v_exp_f32 v5, v5\n\t"
    "v_exp_f32 v6, v6\n\t"
    "v_exp_f32 v7, v7\n\t"
    "v_mfma_f32_16x16x32_bf16 v[12:15], a[56:59], v[216:219], v[12:15]\n\t"
    "s_waitcnt lgkmcnt(3)\n\t"
    "ds_read_b64_tr_b16 a[10:11], v50 offset:37120\n\t"
    "v_cvt_pk_bf16_f32 v17, v2, v3\n\t"
    "v_cvt_pk_bf16_f32 v18, v4, v5\n\t"
    "v_cvt_pk_bf16_f32 v19, v6, v7\n\t"
    "v_mfma_f32_16x16x32_bf16 v[12:15], a[60:63], v[220:223], v[12:15]\n\t"
    "v_exp_f32 v8, v8\n\t"
    "v_mfma_f32_16x16x32_bf16 v[40:43], a[248:251], v[16:19], v[40:43]\n\t"
    "ds_read_b64_tr_b16 a[12:13], v50 offset:33032\n\t"
    "v_accvgpr_read_b32 v75, a155\n\t"
    "v_mfma_f32_16x16x32_bf16 v[112:115], v[224:227], v[16:19], v[112:115]\n\t"
    "v_exp_f32 v9, v9\n\t"
    "v_mfma_f32_16x16x32_bf16 v[116:119], v[228:231], v[16:19], v[116:119]\n\t"
    "ds_read_b64_tr_b16 a[14:15], v50 offset:37128\n\t"
    "v_cvt_pk_bf16_f32 v20, v8, v9\n\t"
    "v_mfma_f32_16x16x32_bf16 v[120:123], v[232:235], v[16:19], v[120:123]\n\t"
    "v_exp_f32 v10, v10\n\t"
    "v_mfma_f32_16x16x32_bf16 v[124:127], v[236:239], v[16:19], v[124:127]\n\t"
    "ds_read_b64_tr_b16 a[16:17], v50 offset:33280\n\t"
    "v_accvgpr_read_b32 v76, a156\n\t"
    "v_mfma_f32_16x16x32_bf16 v[128:131], v[240:243], v[16:19], v[128:131]\n\t"
    "v_exp_f32 v11, v11\n\t"
    "v_mfma_f32_16x16x32_bf16 v[132:135], v[244:247], v[16:19], v[132:135]\n\t"
    "ds_read_b64_tr_b16 a[18:19], v50 offset:37376\n\t"
    "v_cvt_pk_bf16_f32 v21, v10, v11\n\t"
    "v_mfma_f32_16x16x32_bf16 v[136:139], v[248:251], v[16:19], v[136:139]\n\t"
    "v_exp_f32 v12, v12\n\t"
    "v_mfma_f32_16x16x32_bf16 v[140:143], v[252:255], v[16:19], v[140:143]\n\t"
    "ds_read_b64_tr_b16 a[20:21], v50 offset:33288\n\t"
    "v_accvgpr_read_b32 v77, a157\n\t"
    "v_mfma_f32_16x16x32_bf16 v[0:3], a[32:35], v[176:179], v[24:27]\n\t"
    "v_exp_f32 v13, v13\n\t"
    "v_mfma_f32_16x16x32_bf16 v[0:3], a[36:39], v[180:183], v[0:3]\n\t"
    "ds_read_b64_tr_b16 a[22:23], v50 offset:37384\n\t"
    "v_cvt_pk_bf16_f32 v22, v12, v13\n\t"
    "v_mfma_f32_16x16x32_bf16 v[0:3], a[40:43], v[184:187], v[0:3]\n\t"
    "v_exp_f32 v14, v14\n\t"
    "v_mfma_f32_16x16x32_bf16 v[0:3], a[44:47], v[188:191], v[0:3]\n\t"
    "ds_read_b64_tr_b16 a[24:25], v50 offset:33536\n\t"
    "v_accvgpr_read_b32 v78, a158\n\t"
    "v_mfma_f32_16x16x32_bf16 v[4:7], a[48:51], v[176:179], v[24:27]\n\t"
    "v_exp_f32 v15, v15\n\t"
    "v_mfma_f32_16x16x32_bf16 v[4:7], a[52:55], v[180:183], v[4:7]\n\t"
    "ds_read_b64_tr_b16 a[26:27], v50 offset:37632\n\t"
    "v_cvt_pk_bf16_f32 v23, v14, v15\n\t"
    "v_mfma_f32_16x16x32_bf16 v[4:7], a[56:59], v[184:187], v[4:7]\n\t"
    "v_accvgpr_read_b32 v79, a159\n\t"
    "v_mul_f32 v72, v62, v72\n\t"
    "v_mfma_f32_16x16x32_bf16 v[4:7], a[60:63], v[188:191], v[4:7]\n\t"
    "ds_read_b64_tr_b16 a[28:29], v50 offset:33544\n\t"
    "v_mul_f32 v73, v62, v73\n\t"
    "v_mfma_f32_16x16x32_bf16 v[44:47], a[248:251], v[20:23], v[44:47]\n\t"
    "v_mul_f32 v74, v62, v74\n\t"
    "v_mul_f32 v75, v62, v75\n\t"
    "v_mfma_f32_16x16x32_bf16 v[144:147], a[0:3], v[20:23], v[144:147]\n\t"
    "s_waitcnt lgkmcnt(3)\n\t"
    "ds_read_b64_tr_b16 a[30:31], v50 offset:37640\n\t"
    "v_mul_f32 v76, v62, v76\n\t"
    "v_mfma_f32_16x16x32_bf16 v[148:151], a[4:7], v[20:23], v[148:151]\n\t"
    "v_mul_f32 v77, v62, v77\n\t"
    "v_mul_f32 v78, v62, v78\n\t"
    "v_mul_f32 v79, v62, v79\n\t"
    "v_cvt_pk_bf16_f32 v72, v72, v73\n\t"
    "v_cvt_pk_bf16_f32 v73, v74, v75\n\t"
    "v_cvt_pk_bf16_f32 v74, v76, v77\n\t"
    "v_cvt_pk_bf16_f32 v75, v78, v79\n\t"
    "v_mfma_f32_16x16x32_bf16 v[152:155], a[8:11], v[20:23], v[152:155]\n\t"
    "buffer_store_dwordx4 v[72:75], v54, s[44:47], s56 offen offset:192 sc1\n\t"
    "v_mfma_f32_16x16x32_bf16 v[156:159], a[12:15], v[20:23], v[156:159]\n\t"
    "ds_read_b128 a[64:67], v48 offset:40960\n\t"
    "v_cmp_nle_f32_e64 s[18:19], v63, 4.0\n\t"
    "v_mfma_f32_16x16x32_bf16 v[160:163], a[16:19], v[20:23], v[160:163]\n\t"
    "s_nop 0\n\t"
    "s_or_b64 s[86:87], s[86:87], s[18:19]\n\t"
    "v_exp_f32 v0, v0\n\t"
    "v_mfma_f32_16x16x32_bf16 v[164:167], a[20:23], v[20:23], v[164:167]\n\t"
    "s_waitcnt lgkmcnt(3)\n\t"
    "ds_read_b128 a[68:71], v48 offset:41216\n\t"
    "v_accvgpr_read_b32 v63, a244\n\t"
    "v_mfma_f32_16x16x32_bf16 v[168:171], a[24:27], v[20:23], v[168:171]\n\t"
    "s_waitcnt lgkmcnt(2)\n\t"
    "v_exp_f32 v1, v1\n\t"
    "v_mfma_f32_16x16x32_bf16 v[172:175], a[28:31], v[20:23], v[172:175]\n\t"
    "v_exp_f32 v2, v2\n\t"
    "v_mfma_f32_16x16x32_bf16 v[8:11], a[32:35], v[192:195], v[28:31]\n\t"
    "v_exp_f32 v3, v3\n\t"
    "v_mfma_f32_16x16x32_bf16 v[8:11], a[36:39], v[196:199], v[8:11]\n\t"
    "v_exp_f32 v4, v4\n\t"
    "v_mfma_f32_16x16x32_bf16 v[8:11], a[40:43], v[200:203], v[8:11]\n\t"
    "v_exp_f32 v5, v5\n\t"
    "v_mfma_f32_16x16x32_bf16 v[8:11], a[44:47], v[204:207], v[8:11]\n\t"
    "v_exp_f32 v6, v6\n\t"
    "v_mfma_f32_16x16x32_bf16 v[12:15], a[48:51], v[192:195], v[28:31]\n\t"
    "v_exp_f32 v7, v7\n\t"
    "v_mfma_f32_16x16x32_bf16 v[12:15], a[52:55], v[196:199], v[12:15]\n\t"
    "v_cvt_pk_bf16_f32 v16, v0, v1\n\t"
    "v_cvt_pk_bf16_f32 v17, v2, v3\n\t"
    "v_mfma_f32_16x16x32_bf16 v[12:15], a[56:59], v[200:203], v[12:15]\n\t"
    "PAD9_%=:\n\ts_cbranch_vccnz PADRET_%=\n\t"
    "v_cvt_pk_bf16_f32 v18, v4, v5\n\t"
    "v_cvt_pk_bf16_f32 v19, v6, v7\n\t"
    "v_mfma_f32_16x16x32_bf16 v[12:15], a[60:63], v[204:207], v[12:15]\n\t"
    "ds_read_b128 a[72:75], v48 offset:41472\n\t"
    "v_cmp_nle_f32_e64 s[90:91], v63, s50\n\t"
    "v_mfma_f32_16x16x32_bf16 v[36:39], a[248:251], v[16:19], v[36:39]\n\t"
    "ds_read_b64_tr_b16 a[96:97], v50 offset:40960\n\t"
    "v_accvgpr_read_b32 v64, a160\n\t"
    "v_mfma_f32_16x16x32_bf16 v[80:83], a[0:3], v[16:19], v[80:83]\n\t"
    "ds_read_b128 a[76:79], v48 offset:41728\n\t"
    "v_accvgpr_read_b32 v65, a161\n\t"
    "v_mfma_f32_16x16x32_bf16 v[84:87], a[4:7], v[16:19], v[84:87]\n\t"
    "ds_read_b64_tr_b16 a[98:99], v50 offset:45056\n\t"
    "v_accvgpr_read_b32 v66, a162\n\t"
    "v_mfma_f32_16x16x32_bf16 v[88:91], a[8:11], v[16:19], v[88:91]\n\t"
    "ds_read_b128 a[80:83], v48 offset:45056\n\t"
    "v_accvgpr_read_b32 v67, a163\n\t"
    "v_mfma_f32_16x16x32_bf16 v[92:95], a[12:15], v[16:19], v[92:95]\n\t"
    "ds_read_b64_tr_b16 a[100:101], v50 offset:40968\n\t"
    "v_accvgpr_read_b32 v68, a164\n\t"
    "v_mfma_f32_16x16x32_bf16 v[96:99], a[16:19], v[16:19], v[96:99]\n\t"
    "ds_read_b128 a[84:87], v48 offset:45312\n\t"
    "v_accvgpr_read_b32 v69, a165\n\t"
    "v_mfma_f32_16x16x32_bf16 v[100:103], a[20:23], v[16:19], v[100:103]\n\t"
    "ds_read_b64_tr_b16 a[102:103], v50 offset:45064\n\t"
    "v_accvgpr_read_b32 v70, a166\n\t"
    "v_mfma_f32_16x16x32_bf16 v[104:107], a[24:27], v[16:19], v[104:107]\n\t"
    "ds_read_b128 a[88:91], v48 offset:45568\n\t"
    "v_accvgpr_read_b32 v71, a167\n\t"
    "v_mfma_f32_16x16x32_bf16 v[108:111], a[28:31], v[16:19], v[108:111]\n\t"
    "s_waitcnt lgkmcnt(7)\n\t"
    "v_rcp_f32 v63, v63\n\t"
    "v_mfma_f32_16x16x32_bf16 v[0:3], a[64:67], v[208:211], v[32:35]\n\t"
    "ds_read_b128 a[92:95], v48 offset:45824\n\t"
    "v_mul_f32 v64, v63, v64\n\t"
    "v_mfma_f32_16x16x32_bf16 v[0:3], a[68:71], v[212:215], v[0:3]\n\t"
    "v_mul_f32 v65, v63, v65\n\t"
    "v_mul_f32 v66, v63, v66\n\t"
    "v_mfma_f32_16x16x32_bf16 v[0:3], a[72:75], v[216:219], v[0:3]\n\t"
    "s_waitcnt lgkmcnt(5)\n\t"
    "v_mul_f32 v67, v63, v67\n\t"
    "v_mul_f32 v68, v63, v68\n\t"
    "v_mfma_f32_16x16x32_bf16 v[0:3], a[76:79], v[220:223], v[0:3]\n\t"
    "v_mul_f32 v69, v63, v69\n\t"
    "v_mul_f32 v70, v63, v70\n\t"
    "v_mfma_f32_16x16x32_bf16 v[4:7], a[80:83], v[208:211], v[32:35]\n\t"
    "s_waitcnt lgkmcnt(3)\n\t"
    "ds_read_b64_tr_b16 a[104:105], v50 offset:41216\n\t"
    "v_mul_f32 v71, v63, v71\n\t"
    "v_cvt_pk_bf16_f32 v64, v64, v65\n\t"
    "v_cvt_pk_bf16_f32 v65, v66, v67\n\t"
    "v_cvt_pk_bf16_f32 v66, v68, v69\n\t"
    "v_cvt_pk_bf16_f32 v67, v70, v71\n\t"
    "v_mfma_f32_16x16x32_bf16 v[4:7], a[84:87], v[212:215], v[4:7]\n\t"
    "s_waitcnt lgkmcnt(2)\n\t"
    "buffer_store_dwordx4 v[64:67], v54, s[44:47], s57 offen offset:0 sc1\n\t"
    "v_exp_f32 v8, v8\n\t"
    "v_exp_f32 v9, v9\n\t"
    "v_exp_f32 v10, v10\n\t"
    "v_exp_f32 v11, v11\n\t"
    "v_exp_f32 v12, v12\n\t"
    "v_exp_f32 v13, v13\n\t"
    "v_exp_f32 v14, v14\n\t"
    "v_exp_f32 v15, v15\n\t"
    "v_mfma_f32_16x16x32_bf16 v[4:7], a[88:91], v[216:219], v[4:7]\n\t"
    "s_waitcnt lgkmcnt(1)\n\t"
    "ds_read_b64_tr_b16 a[106:107], v50 offset:45312\n\t"
    "v_cvt_pk_bf16_f32 v20, v8, v9\n\t"
    "v_cvt_pk_bf16_f32 v21, v10, v11\n\t"
    "v_cvt_pk_bf16_f32 v22, v12, v13\n\t"
    "v_cvt_pk_bf16_f32 v23, v14, v15\n\t"
    "v_mfma_f32_16x16x32_bf16 v[4:7], a[92:95], v[220:223], v[4:7]\n\t"
    "v_exp_f32 v0, v0\n\t"
    "v_mfma_f32_16x16x32_bf16 v[40:43], a[248:251], v[20:23], v[40:43]\n\t"
    "ds_read_b64_tr_b16 a[108:109], v50 offset:41224\n\t"
    "v_accvgpr_read_b32 v72, a168\n\t"
    "v_mfma_f32_16x16x32_bf16 v[112:115], a[0:3], v[20:23], v[112:115]\n\t"
    "v_exp_f32 v1, v1\n\t"
    "v_mfma_f32_16x16x32_bf16 v[116:119], a[4:7], v[20:23], v[116:119]\n\t"
    "ds_read_b64_tr_b16 a[110:111], v50 offset:45320\n\t"
    "v_cvt_pk_bf16_f32 v16, v0, v1\n\t"
    "v_mfma_f32_16x16x32_bf16 v[120:123], a[8:11], v[20:23], v[120:123]\n\t"
    "v_exp_f32 v2, v2\n\t"
    "v_mfma_f32_16x16x32_bf16 v[124:127], a[12:15], v[20:23], v[124:127]\n\t"
    "ds_read_b64_tr_b16 a[112:113], v50 offset:41472\n\t"
    "v_accvgpr_read_b32 v73, a169\n\t"
    "v_mfma_f32_16x16x32_bf16 v[128:131], a[16:19], v[20:23], v[128:131]\n\t"
    "v_exp_f32 v3, v3\n\t"
    "v_mfma_f32_16x16x32_bf16 v[132:135], a[20:23], v[20:23], v[132:135]\n\t"
    "ds_read_b64_tr_b16 a[114:115], v50 offset:45568\n\t"
    "v_cvt_pk_bf16_f32 v17, v2, v3\n\t"
    "v_mfma_f32_16x16x32_bf16 v[136:139], a[24:27], v[20:23], v[136:139]\n\t"
    "v_exp_f32 v4, v4\n\t"
    "v_mfma_f32_16x16x32_bf16 v[140:143], a[28:31], v[20:23], v[140:143]\n\t"
    "ds_read_b64_tr_b16 a[116:117], v50 offset:41480\n\t"
    "v_accvgpr_read_b32 v74, a170\n\t"
    "v_mfma_f32_16x16x32_bf16 v[8:11], a[64:67], v[176:179], v[24:27]\n\t"
    "v_exp_f32 v5, v5\n\t"
    "v_mfma_f32_16x16x32_bf16 v[8:11], a[68:71], v[180:183], v[8:11]\n\t"
    "ds_read_b64_tr_b16 a[118:119], v50 offset:45576\n\t"
    "v_cvt_pk_bf16_f32 v18, v4, v5\n\t"
    "v_mfma_f32_16x16x32_bf16 v[8:11], a[72:75], v[184:187], v[8:11]\n\t"
    "v_exp_f32 v6, v6\n\t"
    "v_mfma_f32_16x16x32_bf16 v[8:11], a[76:79], v[188:191], v[8:11]\n\t"
    "ds_read_b64_tr_b16 a[120:121], v50 offset:41728\n\t"
    "v_accvgpr_read_b32 v75, a171\n\t"
    "v_mfma_f32_16x16x32_bf16 v[12:15], a[80:83], v[176:179], v[24:27]\n\t"
    "v_exp_f32 v7, v7\n\t"
    "v_mfma_f32_16x16x32_bf16 v[12:15], a[84:87], v[180:183], v[12:15]\n\t"
    "ds_read_b64_tr_b16 a[122:123], v50 offset:45824\n\t"
    "v_cvt_pk_bf16_f32 v19, v6, v7\n\t"
    "v_mfma_f32_16x16x32_bf16 v[12:15], a[88:91], v[184:187], v[12:15]\n\t"
    "v_accvgpr_read_b32 v76, a172\n\t"
    "v_accvgpr_read_b32 v77, a173\n\t"
    "v_mfma_f32_16x16x32_bf16 v[12:15], a[92:95], v[188:191], v[12:15]\n\t"
    "ds_read_b64_tr_b16 a[124:125], v50 offset:41736\n\t"
    "v_accvgpr_read_b32 v78, a174\n\t"
    "v_mfma_f32_16x16x32_bf16 v[44:47], a[248:251], v[16:19], v[44:47]\n\t"
    "v_accvgpr_read_b32 v79, a175\n\t"
    "v_mul_f32 v72, v63, v72\n\t"
    "v_mfma_f32_16x16x32_bf16 v[144:147], a[96:99], v[16:19], v[144:147]\n\t"
    "ds_read_b64_tr_b16 a[126:127], v50 offset:45832\n\t"
    "v_mul_f32 v73, v63, v73\n\t"
    "v_mfma_f32_16x16x32_bf16 v[148:151], a[100:103], v[16:19], v[148:151]\n\t"
    "s_waitcnt lgkmcnt(4)\n\t"
    "v_mul_f32 v74, v63, v74\n\t"
    "v_mul_f32 v75, v63, v75\n\t"
    "v_mfma_f32_16x16x32_bf16 v[152:155], a[104:107], v[16:19], v[152:155]\n\t"
    "v_mul_f32 v76, v63, v76\n\t"
    "v_mul_f32 v77, v63, v77\n\t"
    "v_mfma_f32_16x16x32_bf16 v[156:159], a[108:111], v[16:19], v[156:159]\n\t"
    "v_mul_f32 v78, v63, v78\n\t"
    "v_mul_f32 v79, v63, v79\n\t"
    "v_mfma_f32_16x16x32_bf16 v[160:163], a[112:115], v[16:19], v[160:163]\n\t"
    "v_cvt_pk_bf16_f32 v72, v72, v73\n\t"
    "v_cvt_pk_bf16_f32 v73, v74, v75\n\t"
    "v_mfma_f32_16x16x32_bf16 v[164:167], a[116:119], v[16:19], v[164:167]\n\t"
    "s_waitcnt lgkmcnt(2)\n\t"
    "v_cvt_pk_bf16_f32 v74, v76, v77\n\t"
    "v_cvt_pk_bf16_f32 v75, v78, v79\n\t"
    "v_mfma_f32_16x16x32_bf16 v[168:171], a[120:123], v[16:19], v[168:171]\n\t"
    "s_waitcnt lgkmcnt(0)\n\t"
    "buffer_store_dwordx4 v[72:75], v54, s[44:47], s57 offen offset:64 sc1\n\t"
    "v_mfma_f32_16x16x32_bf16 v[172:175], a[124:127], v[16:19], v[172:175]\n\t"
    "s_waitcnt vmcnt(8)\n\t"
    "ds_read_b128 a[192:195], v49 offset:8192\n\t"
    "v_accvgpr_read_b32 v64, a176\n\t"
    "v_mfma_f32_16x16x32_bf16 v[0:3], a[64:67], v[192:195], v[28:31]\n\t"
    "s_barrier\n\t"
    "v_exp_f32 v8, v8\n\t"
    "v_mfma_f32_16x16x32_bf16 v[0:3], a[68:71], v[196:199], v[0:3]\n\t"
    "ds_read_b128 a[196:199], v49 offset:8448\n\t"
    "v_accvgpr_read_b32 v65, a177\n\t"
    "v_mfma_f32_16x16x32_bf16 v[0:3], a[72:75], v[200:203], v[0:3]\n\t"
    "v_exp_f32 v9, v9\n\t"
    "v_mfma_f32_16x16x32_bf16 v[0:3], a[76:79], v[204:207], v[0:3]\n\t"
    "v_exp_f32 v10, v10\n\t"
    "v_mfma_f32_16x16x32_bf16 v[4:7], a[80:83], v[192:195], v[28:31]\n\t"
    "v_exp_f32 v11, v11\n\t"
    "v_mfma_f32_16x16x32_bf16 v[4:7], a[84:87], v[196:199], v[4:7]\n\t"
    "v_exp_f32 v12, v12\n\t"
    "v_exp_f32 v13, v13\n\t"
    "v_exp_f32 v14, v14\n\t"
    "v_exp_f32 v15, v15\n\t"
    "v_mfma_f32_16x16x32_bf16 v[4:7], a[88:91], v[200:203], v[4:7]\n\t"
    "v_cvt_pk_bf16_f32 v20, v8, v9\n\t"
    "v_cvt_pk_bf16_f32 v21, v10, v11\n\t"
    "v_cvt_pk_bf16_f32 v22, v12, v13\n\t"
    "v_cvt_pk_bf16_f32 v23, v14, v15\n\t"
    "v_mfma_f32_16x16x32_bf16 v[4:7], a[92:95], v[204:207], v[4:7]\n\t"
    "ds_read_b128 a[200:203], v49 offset:8704\n\t"
    "v_accvgpr_read_b32 v66, a178\n\t"
    "v_mfma_f32_16x16x32_bf16 v[36:39], a[248:251], v[20:23], v[36:39]\n\t"
    "ds_read_b64_tr_b16 v[224:225], v51 offset:8192\n\t"
    "v_accvgpr_read_b32 v67, a179\n\t"
    "v_mfma_f32_16x16x32_bf16 v[80:83], a[96:99], v[20:23], v[80:83]\n\t"
    "ds_read_b128 a[204:207], v49 offset:8960\n\t"
    "v_accvgpr_read_b32 v68, a180\n\t"
    "v_mfma_f32_16x16x32_bf16 v[84:87], a[100:103], v[20:23], v[84:87]\n\t"
    "ds_read_b64_tr_b16 v[226:227], v51 offset:12288\n\t"
    "v_accvgpr_read_b32 v69, a181\n\t"
    "v_mfma_f32_16x16x32_bf16 v[88:91], a[104:107], v[20:23], v[88:91]\n\t"
    "ds_read_b128 a[208:211], v49 offset:12288\n\t"
    "v_accvgpr_read_b32 v70, a182\n\t"
    "v_mfma_f32_16x16x32_bf16 v[92:95], a[108:111], v[20:23], v[92:95]\n\t"
    "ds_read_b64_tr_b16 v[228:229], v51 offset:8200\n\t"
    "v_accvgpr_read_b32 v71, a183\n\t"
    "v_mfma_f32_16x16x32_bf16 v[96:99], a[112:115], v[20:23], v[96:99]\n\t"
    "ds_read_b128 a[212:215], v49 offset:12544\n\t"
    "v_mul_f32 v64, v63, v64\n\t"
    "v_mfma_f32_16x16x32_bf16 v[100:103], a[116:119], v[20:23], v[100:103]\n\t"
    "ds_read_b64_tr_b16 v[230:231], v51 offset:12296\n\t"
    "v_mul_f32 v65, v63, v65\n\t"
    "v_mfma_f32_16x16x32_bf16 v[104:107], a[120:123], v[20:23], v[104:107]\n\t"
    "ds_read_b128 a[216:219], v49 offset:12800\n\t"
    "v_mul_f32 v66, v63, v66\n\t"
    "v_mfma_f32_16x16x32_bf16 v[108:111], a[124:127], v[20:23], v[108:111]\n\t"
    "s_waitcnt lgkmcnt(7)\n\t"
    "ds_read_b64_tr_b16 v[232:233], v51 offset:8448\n\t"
    "v_mul_f32 v67, v63, v67\n\t"
    "v_mfma_f32_16x16x32_bf16 v[8:11], a[192:195], v[208:211], v[32:35]\n\t"
    "ds_read_b128 a[220:223], v49 offset:13056\n\t"
    "v_mul_f32 v68, v63, v68\n\t"
    "v_mfma_f32_16x16x32_bf16 v[8:11], a[196:199], v[212:215], v[8:11]\n\t"
    "v_exp_f32 v0, v0\n\t"
    "v_mfma_f32_16x16x32_bf16 v[8:11], a[200:203], v[216:219], v[8:11]\n\t"
    "s_waitcnt lgkmcnt(6)\n\t"
    "v_exp_f32 v1, v1\n\t"
    "v_mfma_f32_16x16x32_bf16 v[8:11], a[204:207], v[220:223], v[8:11]\n\t"
    "v_exp_f32 v2, v2\n\t"
    "v_mfma_f32_16x16x32_bf16 v[12:15], a[208:211], v[208:211], v[32:35]\n\t"
    "s_waitcnt lgkmcnt(4)\n\t"
    "v_exp_f32 v3, v3\n\t"
    "v_mfma_f32_16x16x32_bf16 v[12:15], a[212:215], v[212:215], v[12:15]\n\t"
    "s_waitcnt lgkmcnt(2)\n\t"
    "v_exp_f32 v4, v4\n\t"
    "v_exp_f32 v5, v5\n\t"
    "v_exp_f32 v6, v6\n\t"
    "v_exp_f32 v7, v7\n\t"
    "v_mfma_f32_16x16x32_bf16 v[12:15], a[216:219], v[216:219], v[12:15]\n\t"
    "s_waitcnt lgkmcnt(0)\n\t"
    "ds_read_b64_tr_b16 v[234:235], v51 offset:12544\n\t"
    "v_cvt_pk_bf16_f32 v16, v0, v1\n\t"
    "v_cvt_pk_bf16_f32 v17, v2, v3\n\t"
    "v_cvt_pk_bf16_f32 v18, v4, v5\n\t"
    "v_cvt_pk_bf16_f32 v19, v6, v7\n\t"
    "v_mfma_f32_16x16x32_bf16 v[12:15], a[220:223], v[220:223], v[12:15]\n\t"
    "v_mul_f32 v69, v63, v69\n\t"
    "v_mul_f32 v70, v63, v70\n\t"
    "v_mfma_f32_16x16x32_bf16 v[40:43], a[248:251], v[16:19], v[40:43]\n\t"
    "ds_read_b64_tr_b16 v[236:237], v51 offset:8456\n\t"
    "v_mul_f32 v71, v63, v71\n\t"
    "v_mfma_f32_16x16x32_bf16 v[112:115], a[96:99], v[16:19], v[112:115]\n\t"
    "v_cvt_pk_bf16_f32 v64, v64, v65\n\t"
    "v_cvt_pk_bf16_f32 v65, v66, v67\n\t"
    "v_cvt_pk_bf16_f32 v66, v68, v69\n\t"
    "v_cvt_pk_bf16_f32 v67, v70, v71\n\t"
    "v_mfma_f32_16x16x32_bf16 v[116:119], a[100:103], v[16:19], v[116:119]\n\t"
    "buffer_store_dwordx4 v[64:67], v54, s[44:47], s57 offen offset:128 sc1\n\t"
    "ds_read_b64_tr_b16 v[238:239], v51 offset:12552\n\t"
    "v_mfma_f32_16x16x32_bf16 v[120:123], a[104:107], v[16:19], v[120:123]\n\t"
    "v_exp_f32 v8, v8\n\t"
    "v_mfma_f32_16x16x32_bf16 v[124:127], a[108:111], v[16:19], v[124:127]\n\t"
    "ds_read_b64_tr_b16 v[240:241], v51 offset:8704\n\t"
    "v_accvgpr_read_b32 v72, a184\n\t"
    "v_mfma_f32_16x16x32_bf16 v[128:131], a[112:115], v[16:19], v[128:131]\n\t"
    "v_exp_f32 v9, v9\n\t"
    "v_mfma_f32_16x16x32_bf16 v[132:135], a[116:119], v[16:19], v[132:135]\n\t"
    "ds_read_b64_tr_b16 v[242:243], v51 offset:12800\n\t"
    "v_cvt_pk_bf16_f32 v20, v8, v9\n\t"
    "v_mfma_f32_16x16x32_bf16 v[136:139], a[120:123], v[16:19], v[136:139]\n\t"
    "v_exp_f32 v10, v10\n\t"
    "v_mfma_f32_16x16x32_bf16 v[140:143], a[124:127], v[16:19], v[140:143]\n\t"
    "ds_read_b64_tr_b16 v[244:245], v51 offset:8712\n\t"
    "v_accvgpr_read_b32 v73, a185\n\t"
    "v_mfma_f32_16x16x32_bf16 v[0:3], a[192:195], v[176:179], v[24:27]\n\t"
    "v_exp_f32 v11, v11\n\t"
    "v_mfma_f32_16x16x32_bf16 v[0:3], a[196:199], v[180:183], v[0:3]\n\t"
    "ds_read_b64_tr_b16 v[246:247], v51 offset:12808\n\t"
    "v_cvt_pk_bf16_f32 v21, v10, v11\n\t"
    "v_mfma_f32_16x16x32_bf16 v[0:3], a[200:203], v[184:187], v[0:3]\n\t"
    "v_exp_f32 v12, v12\n\t"
    "v_mfma_f32_16x16x32_bf16 v[0:3], a[204:207], v[188:191], v[0:3]\n\t"
    "ds_read_b64_tr_b16 v[248:249], v51 offset:8960\n\t"
    "v_accvgpr_read_b32 v74, a186\n\t"
    "v_mfma_f32_16x16x32_bf16 v[4:7], a[208:211], v[176:179], v[24:27]\n\t"
    "v_exp_f32 v13, v13\n\t"
    "v_mfma_f32_16x16x32_bf16 v[4:7], a[212:215], v[180:183], v[4:7]\n\t"
    "ds_read_b64_tr_b16 v[250:251], v51 offset:13056\n\t"
    "v_exp_f32 v14, v14\n\t"
    "v_exp_f32 v15, v15\n\t"
    "v_mfma_f32_16x16x32_bf16 v[4:7], a[216:219], v[184:187], v[4:7]\n\t"
    "v_cvt_pk_bf16_f32 v22, v12, v13\n\t"
    "v_cvt_pk_bf16_f32 v23, v14, v15\n\t"
    "v_mfma_f32_16x16x32_bf16 v[4:7], a[220:223], v[188:191], v[4:7]\n\t"
    "ds_read_b64_tr_b16 v[252:253], v51 offset:8968\n\t"
    "v_accvgpr_read_b32 v75, a187\n\t"
    "v_mfma_f32_16x16x32_bf16 v[44:47], a[248:251], v[20:23], v[44:47]\n\t"
    "v_accvgpr_read_b32 v76, a188\n\t"
    "v_accvgpr_read_b32 v77, a189\n\t"
    "v_mfma_f32_16x16x32_bf16 v[144:147], v[224:227], v[20:23], v[144:147]\n\t"
    "ds_read_b64_tr_b16 v[254:255], v51 offset:13064\n\t"
    "v_accvgpr_read_b32 v78, a190\n\t"
    "v_mfma_f32_16x16x32_bf16 v[148:151], v[228:231], v[20:23], v[148:151]\n\t"
    "s_waitcnt lgkmcnt(4)\n\t"
    "v_accvgpr_read_b32 v79, a191\n\t"
    "v_mul_f32 v72, v63, v72\n\t"
    "v_mfma_f32_16x16x32_bf16 v[152:155], v[232:235], v[20:23], v[152:155]\n\t"
    "v_mul_f32 v73, v63, v73\n\t"
    "v_mul_f32 v74, v63, v74\n\t"
    "v_mfma_f32_16x16x32_bf16 v[156:159], v[236:239], v[20:23], v[156:159]\n\t"
    "v_mul_f32 v75, v63, v75\n\t"
    "v_mul_f32 v76, v63, v76\n\t"
    "v_mfma_f32_16x16x32_bf16 v[160:163], v[240:243], v[20:23], v[160:163]\n\t"
    "v_mul_f32 v77, v63, v77\n\t"
    "v_mul_f32 v78, v63, v78\n\t"
    "v_mfma_f32_16x16x32_bf16 v[164:167], v[244:247], v[20:23], v[164:167]\n\t"
    "s_waitcnt lgkmcnt(2)\n\t"
    "v_mul_f32 v79, v63, v79\n\t"
    "v_cvt_pk_bf16_f32 v72, v72, v73\n\t"
    "v_mfma_f32_16x16x32_bf16 v[168:171], v[248:251], v[20:23], v[168:171]\n\t"
    "s_waitcnt lgkmcnt(0)\n\t"
    "v_cvt_pk_bf16_f32 v73, v74, v75\n\t"
    "v_cvt_pk_bf16_f32 v74, v76, v77\n\t"
    "v_mfma_f32_16x16x32_bf16 v[172:175], v[252:255], v[20:23], v[172:175]\n\t"
    "ds_read_b128 a[32:35], v49 offset:0\n\t"
    "v_cvt_pk_bf16_f32 v75, v78, v79\n\t"
    "v_mfma_f32_16x16x32_bf16 v[8:11], a[192:195], v[192:195], v[28:31]\n\t"
    "buffer_store_dwordx4 v[72:75], v54, s[44:47], s57 offen offset:192 sc1\n\t"
    "v_mfma_f32_16x16x32_bf16 v[8:11], a[196:199], v[196:199], v[8:11]\n\t"
    "ds_read_b128 a[36:39], v49 offset:256\n\t"
    "v_cmp_nle_f32_e64 s[20:21], v62, 4.0\n\t"
    "v_mfma_f32_16x16x32_bf16 v[8:11], a[200:203], v[200:203], v[8:11]\n\t"
    "s_nop 0\n\t"
    "s_or_b64 s[88:89], s[88:89], s[20:21]\n\t"
    "v_exp_f32 v0, v0\n\t"
    "v_mfma_f32_16x16x32_bf16 v[8:11], a[204:207], v[204:207], v[8:11]\n\t"
    "v_exp_f32 v1, v1\n\t"
    "v_mfma_f32_16x16x32_bf16 v[12:15], a[208:211], v[192:195], v[28:31]\n\t"
    "v_exp_f32 v2, v2\n\t"
    "v_mfma_f32_16x16x32_bf16 v[12:15], a[212:215], v[196:199], v[12:15]\n\t"
    "v_exp_f32 v3, v3\n\t"
    "v_exp_f32 v4, v4\n\t"
    "v_exp_f32 v5, v5\n\t"
    "v_exp_f32 v6, v6\n\t"
    "v_exp_f32 v7, v7\n\t"
    "v_mfma_f32_16x16x32_bf16 v[12:15], a[216:219], v[200:203], v[12:15]\n\t"
    "v_cvt_pk_bf16_f32 v16, v0, v1\n\t"
    "v_cvt_pk_bf16_f32 v17, v2, v3\n\t"
    "v_cvt_pk_bf16_f32 v18, v4, v5\n\t"
    "v_cvt_pk_bf16_f32 v19, v6, v7\n\t"
    "v_mfma_f32_16x16x32_bf16 v[12:15], a[220:223], v[204:207], v[12:15]\n\t"
    "ds_read_b128 a[40:43], v49 offset:512\n\t"
    "v_cmp_nle_f32_e64 s[22:23], v63, 4.0\n\t"
    "v_mfma_f32_16x16x32_bf16 v[36:39], a[248:251], v[16:19], v[36:39]\n\t"
    "s_nop 0\n\t"
    "s_or_b64 s[90:91], s[90:91], s[22:23]\n\t"
    "v_exp_f32 v8, v8\n\t"
    "v_mfma_f32_16x16x32_bf16 v[80:83], v[224:227], v[16:19], v[80:83]\n\t"
    "ds_read_b128 a[44:47], v49 offset:768\n\t"
    "v_mfma_f32_16x16x32_bf16 v[84:87], v[228:231], v[16:19], v[84:87]\n\t"
    "v_exp_f32 v9, v9\n\t"
    "v_mfma_f32_16x16x32_bf16 v[88:91], v[232:235], v[16:19], v[88:91]\n\t"
    "ds_read_b128 a[48:51], v49 offset:4096\n\t"
    "v_cvt_pk_bf16_f32 v20, v8, v9\n\t"
    "v_mfma_f32_16x16x32_bf16 v[92:95], v[236:239], v[16:19], v[92:95]\n\t"
    "v_exp_f32 v10, v10\n\t"
    "v_mfma_f32_16x16x32_bf16 v[96:99], v[240:243], v[16:19], v[96:99]\n\t"
    "ds_read_b128 a[52:55], v49 offset:4352\n\t"
    "ds_read_b64_tr_b16 a[0:1], v51 offset:0\n\t"
    "v_mfma_f32_16x16x32_bf16 v[100:103], v[244:247], v[16:19], v[100:103]\n\t"
    "v_exp_f32 v11, v11\n\t"
    "v_mfma_f32_16x16x32_bf16 v[104:107], v[248:251], v[16:19], v[104:107]\n\t"
    "ds_read_b128 a[56:59], v49 offset:4608\n\t"
    "ds_read_b64_tr_b16 a[2:3], v51 offset:4096\n\t"
    "v_mfma_f32_16x16x32_bf16 v[108:111], v[252:255], v[16:19], v[108:111]\n\t"
    "s_waitcnt lgkmcnt(6)\n\t"
    "v_exp_f32 v12, v12\n\t"
    "v_mfma_f32_16x16x32_bf16 v[0:3], a[32:35], v[208:211], v[32:35]\n\t"
    "ds_read_b128 a[60:63], v49 offset:4864\n\t"
    "ds_read_b64_tr_b16 a[4:5], v51 offset:8\n\t"
    "v_mfma_f32_16x16x32_bf16 v[0:3], a[36:39], v[212:215], v[0:3]\n\t"
    "v_exp_f32 v13, v13\n\t"
    "v_mfma_f32_16x16x32_bf16 v[0:3], a[40:43], v[216:219], v[0:3]\n\t"
    "s_waitcnt lgkmcnt(6)\n\t"
    "ds_read_b64_tr_b16 a[6:7], v51 offset:4104\n\t"
    "v_cvt_pk_bf16_f32 v21, v10, v11\n\t"
    "v_mfma_f32_16x16x32_bf16 v[0:3], a[44:47], v[220:223], v[0:3]\n\t"
    "v_exp_f32 v14, v14\n\t"
    "v_mfma_f32_16x16x32_bf16 v[4:7], a[48:51], v[208:211], v[32:35]\n\t"
    "s_waitcnt lgkmcnt(5)\n\t"
    "ds_read_b64_tr_b16 a[8:9], v51 offset:256\n\t"
    "v_cvt_pk_bf16_f32 v22, v12, v13\n\t"
    "v_mfma_f32_16x16x32_bf16 v[4:7], a[52:55], v[212:215], v[4:7]\n\t"
    "s_waitcnt lgkmcnt(5)\n\t"
    "v_exp_f32 v15, v15\n\t"
    "v_mfma_f32_16x16x32_bf16 v[4:7], a[56:59], v[216:219], v[4:7]\n\t"
    "s_waitcnt lgkmcnt(3)\n\t"
    "ds_read_b64_tr_b16 a[10:11], v51 offset:4352\n\t"
    "v_cvt_pk_bf16_f32 v23, v14, v15\n\t"
    "v_mfma_f32_16x16x32_bf16 v[4:7], a[60:63], v[220:223], v[4:7]\n\t"
    "v_exp_f32 v0, v0\n\t"
    "v_mfma_f32_16x16x32_bf16 v[40:43], a[248:251], v[20:23], v[40:43]\n\t"
    "ds_read_b64_tr_b16 a[12:13], v51 offset:264\n\t"
    "v_mfma_f32_16x16x32_bf16 v[112:115], v[224:227], v[20:23], v[112:115]\n\t"
    "v_exp_f32 v1, v1\n\t"
    "v_mfma_f32_16x16x32_bf16 v[116:119], v[228:231], v[20:23], v[116:119]\n\t"
    "ds_read_b64_tr_b16 a[14:15], v51 offset:4360\n\t"
    "v_cvt_pk_bf16_f32 v16, v0, v1\n\t"
    "v_mfma_f32_16x16x32_bf16 v[120:123], v[232:235], v[20:23], v[120:123]\n\t"
    "v_exp_f32 v2, v2\n\t"
    "v_mfma_f32_16x16x32_bf16 v[124:127], v[236:239], v[20:23], v[124:127]\n\t"
    "ds_read_b64_tr_b16 a[16:17], v51 offset:512\n\t"
    "v_mfma_f32_16x16x32_bf16 v[128:131], v[240:243], v[20:23], v[128:131]\n\t"
    "v_exp_f32 v3, v3\n\t"
    "v_mfma_f32_16x16x32_bf16 v[132:135], v[244:247], v[20:23], v[132:135]\n\t"
    "ds_read_b64_tr_b16 a[18:19], v51 offset:4608\n\t"
    "v_cvt_pk_bf16_f32 v17, v2, v3\n\t"
    "v_mfma_f32_16x16x32_bf16 v[136:139], v[248:251], v[20:23], v[136:139]\n\t"
    "v_exp_f32 v4, v4\n\t"
    "v_mfma_f32_16x16x32_bf16 v[140:143], v[252:255], v[20:23], v[140:143]\n\t"
    "ds_read_b64_tr_b16 a[20:21], v51 offset:520\n\t"
    "v_mfma_f32_16x16x32_bf16 v[8:11], a[32:35], v[176:179], v[24:27]\n\t"
    "v_exp_f32 v5, v5\n\t"
    "v_mfma_f32_16x16x32_bf16 v[8:11], a[36:39], v[180:183], v[8:11]\n\t"
    "ds_read_b64_tr_b16 a[22:23], v51 offset:4616\n\t"
    "v_cvt_pk_bf16_f32 v18, v4, v5\n\t"
    "v_mfma_f32_16x16x32_bf16 v[8:11], a[40:43], v[184:187], v[8:11]\n\t"
    "v_exp_f32 v6, v6\n\t"
    "v_mfma_f32_16x16x32_bf16 v[8:11], a[44:47], v[188:191], v[8:11]\n\t"
    "ds_read_b64_tr_b16 a[24:25], v51 offset:768\n\t"
    "v_mfma_f32_16x16x32_bf16 v[12:15], a[48:51], v[176:179], v[24:27]\n\t"
    "v_exp_f32 v7, v7\n\t"
    "v_mfma_f32_16x16x32_bf16 v[12:15], a[52:55], v[180:183], v[12:15]\n\t"
    "ds_read_b64_tr_b16 a[26:27], v51 offset:4864\n\t"
    "v_cvt_pk_bf16_f32 v19, v6, v7\n\t"
    "v_mfma_f32_16x16x32_bf16 v[12:15], a[56:59], v[184:187], v[12:15]\n\t"
    "ds_read_b128 a[64:67], v48 offset:57344\n\t"
    "v_mfma_f32_16x16x32_bf16 v[12:15], a[60:63], v[188:191], v[12:15]\n\t"
    "ds_read_b64_tr_b16 a[28:29], v51 offset:776\n\t"
    "v_mfma_f32_16x16x32_bf16 v[44:47], a[248:251], v[16:19], v[44:47]\n\t"
    "v_exp_f32 v8, v8\n\t"
    "v_mfma_f32_16x16x32_bf16 v[144:147], a[0:3], v[16:19], v[144:147]\n\t"
    "s_waitcnt lgkmcnt(4)\n\t"
    "ds_read_b64_tr_b16 a[30:31], v51 offset:4872\n\t"
    "v_mfma_f32_16x16x32_bf16 v[148:151], a[4:7], v[16:19], v[148:151]\n\t"
    "v_exp_f32 v9, v9\n\t"
    "v_mfma_f32_16x16x32_bf16 v[152:155], a[8:11], v[16:19], v[152:155]\n\t"
    "ds_read_b64_tr_b16 a[96:97], v50 offset:57344\n\t"
    "v_cvt_pk_bf16_f32 v20, v8, v9\n\t"
    "v_mfma_f32_16x16x32_bf16 v[156:159], a[12:15], v[16:19], v[156:159]\n\t"
    "v_exp_f32 v10, v10\n\t"
    "v_mfma_f32_16x16x32_bf16 v[160:163], a[16:19], v[16:19], v[160:163]\n\t"
    "v_exp_f32 v11, v11\n\t"
    "v_mfma_f32_16x16x32_bf16 v[164:167], a[20:23], v[16:19], v[164:167]\n\t"
    "s_waitcnt lgkmcnt(3)\n\t"
    "ds_read_b64_tr_b16 a[98:99], v50 offset:61440\n\t"
    "v_cvt_pk_bf16_f32 v21, v10, v11\n\t"
    "v_mfma_f32_16x16x32_bf16 v[168:171], a[24:27], v[16:19], v[168:171]\n\t"
    "s_waitcnt lgkmcnt(2)\n\t"
    "v_exp_f32 v12, v12\n\t"
    "v_mfma_f32_16x16x32_bf16 v[172:175], a[28:31], v[16:19], v[172:175]\n\t"
    "v_exp_f32 v13, v13\n\t"
    "v_mfma_f32_16x16x32_bf16 v[0:3], a[32:35], v[192:195], v[28:31]\n\t"
    "ds_read_b64_tr_b16 a[100:101], v50 offset:57352\n\t"
    "v_cvt_pk_bf16_f32 v22, v12, v13\n\t"
    "v_mfma_f32_16x16x32_bf16 v[0:3], a[36:39], v[196:199], v[0:3]\n\t"
    "v_exp_f32 v14, v14\n\t"
    "v_mfma_f32_16x16x32_bf16 v[0:3], a[40:43], v[200:203], v[0:3]\n\t"
    "v_exp_f32 v15, v15\n\t"
    "v_mfma_f32_16x16x32_bf16 v[0:3], a[44:47], v[204:207], v[0:3]\n\t"
    "ds_read_b64_tr_b16 a[102:103], v50 offset:61448\n\t"
    "v_cvt_pk_bf16_f32 v23, v14, v15\n\t"
    "v_mfma_f32_16x16x32_bf16 v[4:7], a[48:51], v[192:195], v[28:31]\n\t"
    "ds_read_b128 a[68:71], v48 offset:57600\n\t"
    "v_mfma_f32_16x16x32_bf16 v[4:7], a[52:55], v[196:199], v[4:7]\n\t"
    "ds_read_b64_tr_b16 a[104:105], v50 offset:57600\n\t"
    "v_mfma_f32_16x16x32_bf16 v[4:7], a[56:59], v[200:203], v[4:7]\n\t"
    "ds_read_b128 a[72:75], v48 offset:57856\n\t"
    "v_mfma_f32_16x16x32_bf16 v[4:7], a[60:63], v[204:207], v[4:7]\n\t"
    "v_exp_f32 v0, v0\n\t"
    "v_mfma_f32_16x16x32_bf16 v[36:39], a[248:251], v[20:23], v[36:39]\n\t"
    "ds_read_b64_tr_b16 a[106:107], v50 offset:61696\n\t"
    "v_mfma_f32_16x16x32_bf16 v[80:83], a[0:3], v[20:23], v[80:83]\n\t"
    "ds_read_b128 a[76:79], v48 offset:58112\n\t"
    "v_mfma_f32_16x16x32_bf16 v[84:87], a[4:7], v[20:23], v[84:87]\n\t"
    "ds_read_b64_tr_b16 a[108:109], v50 offset:57608\n\t"
    "v_mfma_f32_16x16x32_bf16 v[88:91], a[8:11], v[20:23], v[88:91]\n\t"
    "ds_read_b128 a[80:83], v48 offset:61440\n\t"
    "v_mfma_f32_16x16x32_bf16 v[92:95], a[12:15], v[20:23], v[92:95]\n\t"
    "ds_read_b64_tr_b16 a[110:111], v50 offset:61704\n\t"
    "v_mfma_f32_16x16x32_bf16 v[96:99], a[16:19], v[20:23], v[96:99]\n\t"
    "ds_read_b128 a[84:87], v48 offset:61696\n\t"
    "v_mfma_f32_16x16x32_bf16 v[100:103], a[20:23], v[20:23], v[100:103]\n\t"
    "ds_read_b64_tr_b16 a[112:113], v50 offset:57856\n\t"
    "v_mfma_f32_16x16x32_bf16 v[104:107], a[24:27], v[20:23], v[104:107]\n\t"
    "ds_read_b128 a[88:91], v48 offset:61952\n\t"
    "v_mfma_f32_16x16x32_bf16 v[108:111], a[28:31], v[20:23], v[108:111]\n\t"
    "ds_read_b64_tr_b16 a[114:115], v50 offset:61952\n\t"
    "v_mfma_f32_16x16x32_bf16 v[8:11], a[64:67], v[176:179], v[24:27]\n\t"
    "s_waitcnt lgkmcnt(7)\n\t"
    "ds_read_b128 a[92:95], v48 offset:62208\n\t"
    "v_mfma_f32_16x16x32_bf16 v[8:11], a[68:71], v[180:183], v[8:11]\n\t"
    "ds_read_b64_tr_b16 a[116:117], v50 offset:57864\n\t"
    "v_mfma_f32_16x16x32_bf16 v[8:11], a[72:75], v[184:187], v[8:11]\n\t"
    "v_exp_f32 v1, v1\n\t"
    "v_mfma_f32_16x16x32_bf16 v[8:11], a[76:79], v[188:191], v[8:11]\n\t"
    "s_waitcnt lgkmcnt(6)\n\t"
    "ds_read_b64_tr_b16 a[118:119], v50 offset:61960\n\t"
    "ds_read_b128 a[192:195], v48 offset:49152\n\t"
    "v_mfma_f32_16x16x32_bf16 v[12:15], a[80:83], v[176:179], v[24:27]\n\t"
    "s_waitcnt lgkmcnt(7)\n\t"
    "v_exp_f32 v2, v2\n\t"
    "v_mfma_f32_16x16x32_bf16 v[12:15], a[84:87], v[180:183], v[12:15]\n\t"
    "s_waitcnt lgkmcnt(5)\n\t"
    "ds_read_b64_tr_b16 a[120:121], v50 offset:58112\n\t"
    "ds_read_b128 a[196:199], v48 offset:49408\n\t"
    "v_exp_f32 v3, v3\n\t"
    "v_exp_f32 v4, v4\n\t"
    "v_exp_f32 v5, v5\n\t"
    "v_exp_f32 v6, v6\n\t"
    "v_exp_f32 v7, v7\n\t"
    "v_mfma_f32_16x16x32_bf16 v[12:15], a[88:91], v[184:187], v[12:15]\n\t"
    "s_waitcnt lgkmcnt(5)\n\t"
    "v_cvt_pk_bf16_f32 v16, v0, v1\n\t"
    "v_cvt_pk_bf16_f32 v17, v2, v3\n\t"
    "v_cvt_pk_bf16_f32 v18, v4, v5\n\t"
    "v_cvt_pk_bf16_f32 v19, v6, v7\n\t"
    "v_mfma_f32_16x16x32_bf16 v[12:15], a[92:95], v[188:191], v[12:15]\n\t"
    "ds_read_b64_tr_b16 a[122:123], v50 offset:62208\n\t"
    "ds_read_b128 a[200:203], v48 offset:49664\n\t"
    "v_mfma_f32_16x16x32_bf16 v[40:43], a[248:251], v[16:19], v[40:43]\n\t"
    "v_exp_f32 v8, v8\n\t"
    "v_mfma_f32_16x16x32_bf16 v[112:115], a[0:3], v[16:19], v[112:115]\n\t"
    "ds_read_b64_tr_b16 a[124:125], v50 offset:58120\n\t"
    "ds_read_b128 a[204:207], v48 offset:49920\n\t"
    "v_mfma_f32_16x16x32_bf16 v[116:119], a[4:7], v[16:19], v[116:119]\n\t"
    "v_exp_f32 v9, v9\n\t"
    "v_mfma_f32_16x16x32_bf16 v[120:123], a[8:11], v[16:19], v[120:123]\n\t"
    "ds_read_b64_tr_b16 a[126:127], v50 offset:62216\n\t"
    "ds_read_b128 a[208:211], v48 offset:53248\n\t"
    "v_mfma_f32_16x16x32_bf16 v[124:127], a[12:15], v[16:19], v[124:127]\n\t"
    "v_exp_f32 v10, v10\n\t"
    "v_mfma_f32_16x16x32_bf16 v[128:131], a[16:19], v[16:19], v[128:131]\n\t"
    "ds_read_b128 a[212:215], v48 offset:53504\n\t"
    "ds_read_b64_tr_b16 v[224:225], v50 offset:49152\n\t"
    "v_mfma_f32_16x16x32_bf16 v[132:135], a[20:23], v[16:19], v[132:135]\n\t"
    "v_exp_f32 v11, v11\n\t"
    "v_mfma_f32_16x16x32_bf16 v[136:139], a[24:27], v[16:19], v[136:139]\n\t"
    "ds_read_b128 a[216:219], v48 offset:53760\n\t"
    "ds_read_b64_tr_b16 v[226:227], v50 offset:53248\n\t"
    "v_mfma_f32_16x16x32_bf16 v[140:143], a[28:31], v[16:19], v[140:143]\n\t"
    "s_waitcnt lgkmcnt(8)\n\t"
    "v_exp_f32 v12, v12\n\t"
    "v_mfma_f32_16x16x32_bf16 v[0:3], a[192:195], v[176:179], v[24:27]\n\t"
    "ds_read_b128 a[220:223], v48 offset:54016\n\t"
    "ds_read_b64_tr_b16 v[228:229], v50 offset:49160\n\t"
    "v_mfma_f32_16x16x32_bf16 v[0:3], a[196:199], v[180:183], v[0:3]\n\t"
    "v_exp_f32 v13, v13\n\t"
    "v_mfma_f32_16x16x32_bf16 v[0:3], a[200:203], v[184:187], v[0:3]\n\t"
    "s_waitcnt lgkmcnt(6)\n\t"
    "ds_read_b64_tr_b16 v[230:231], v50 offset:53256\n\t"
    "v_cvt_pk_bf16_f32 v20, v8, v9\n\t"
    "v_mfma_f32_16x16x32_bf16 v[0:3], a[204:207], v[188:191], v[0:3]\n\t"
    "v_exp_f32 v14, v14\n\t"
    "v_mfma_f32_16x16x32_bf16 v[4:7], a[208:211], v[176:179], v[24:27]\n\t"
    "s_waitcnt lgkmcnt(5)\n\t"
    "ds_read_b64_tr_b16 v[232:233], v50 offset:49408\n\t"
    "v_cvt_pk_bf16_f32 v21, v10, v11\n\t"
    "v_mfma_f32_16x16x32_bf16 v[4:7], a[212:215], v[180:183], v[4:7]\n\t"
    "s_waitcnt lgkmcnt(5)\n\t"
    "v_exp_f32 v15, v15\n\t"
    "v_mfma_f32_16x16x32_bf16 v[4:7], a[216:219], v[184:187], v[4:7]\n\t"
    "s_waitcnt lgkmcnt(3)\n\t"
    "ds_read_b64_tr_b16 v[234:235], v50 offset:53504\n\t"
    "v_cvt_pk_bf16_f32 v22, v12, v13\n\t"
    "v_cvt_pk_bf16_f32 v23, v14, v15\n\t"
    "v_mfma_f32_16x16x32_bf16 v[4:7], a[220:223], v[188:191], v[4:7]\n\t"
    "v_exp_f32 v0, v0\n\t"
    "v_mfma_f32_16x16x32_bf16 v[36:39], a[248:251], v[20:23], v[36:39]\n\t"
    "ds_read_b64_tr_b16 v[236:237], v50 offset:49416\n\t"
    "v_mfma_f32_16x16x32_bf16 v[80:83], a[96:99], v[20:23], v[80:83]\n\t"
    "v_exp_f32 v1, v1\n\t"
    "v_mfma_f32_16x16x32_bf16 v[84:87], a[100:103], v[20:23], v[84:87]\n\t"
    "ds_read_b64_tr_b16 v[238:239], v50 offset:53512\n\t"
    "v_cvt_pk_bf16_f32 v16, v0, v1\n\t"
    "v_mfma_f32_16x16x32_bf16 v[88:91], a[104:107], v[20:23], v[88:91]\n\t"
    "v_exp_f32 v2, v2\n\t"
    "v_mfma_f32_16x16x32_bf16 v[92:95], a[108:111], v[20:23], v[92:95]\n\t"
    "ds_read_b64_tr_b16 v[240:241], v50 offset:49664\n\t"
    "v_mfma_f32_16x16x32_bf16 v[96:99], a[112:115], v[20:23], v[96:99]\n\t"
    "v_exp_f32 v3, v3\n\t"
    "v_mfma_f32_16x16x32_bf16 v[100:103], a[116:119], v[20:23], v[100:103]\n\t"
    "ds_read_b64_tr_b16 v[242:243], v50 offset:53760\n\t"
    "v_cvt_pk_bf16_f32 v17, v2, v3\n\t"
    "v_mfma_f32_16x16x32_bf16 v[104:107], a[120:123], v[20:23], v[104:107]\n\t"
    "v_exp_f32 v4, v4\n\t"
    "v_mfma_f32_16x16x32_bf16 v[108:111], a[124:127], v[20:23], v[108:111]\n\t"
    "ds_read_b64_tr_b16 v[244:245], v50 offset:49672\n\t"
    "v_mfma_f32_16x16x32_bf16 v[8:11], a[64:67], v[192:195], v[28:31]\n\t"
    "v_exp_f32 v5, v5\n\t"
    "v_mfma_f32_16x16x32_bf16 v[8:11], a[68:71], v[196:199], v[8:11]\n\t"
    "ds_read_b64_tr_b16 v[246:247], v50 offset:53768\n\t"
    "v_cvt_pk_bf16_f32 v18, v4, v5\n\t"
    "v_mfma_f32_16x16x32_bf16 v[8:11], a[72:75], v[200:203], v[8:11]\n\t"
    "v_exp_f32 v6, v6\n\t"
    "v_mfma_f32_16x16x32_bf16 v[8:11], a[76:79], v[204:207], v[8:11]\n\t"
    "ds_read_b64_tr_b16 v[248:249], v50 offset:49920\n\t"
    "v_mfma_f32_16x16x32_bf16 v[12:15], a[80:83], v[192:195], v[28:31]\n\t"
    "v_exp_f32 v7, v7\n\t"
    "v_mfma_f32_16x16x32_bf16 v[12:15], a[84:87], v[196:199], v[12:15]\n\t"
    "ds_read_b64_tr_b16 v[250:251], v50 offset:54016\n\t"
    "v_cvt_pk_bf16_f32 v19, v6, v7\n\t"
    "v_mfma_f32_16x16x32_bf16 v[12:15], a[88:91], v[200:203], v[12:15]\n\t"
    "ds_read_b128 a[32:35], v48 offset:0\n\t"
    "v_mfma_f32_16x16x32_bf16 v[12:15], a[92:95], v[204:207], v[12:15]\n\t"
    "PAD10_%=:\n\ts_cbranch_vccnz PADRET_%=\n\t"
    "ds_read_b64_tr_b16 v[252:253], v50 offset:49928\n\t"
    "v_mfma_f32_16x16x32_bf16 v[36:39], a[248:251], v[16:19], v[36:39]\n\t"
    "v_exp_f32 v8, v8\n\t"
    "v_mfma_f32_16x16x32_bf16 v[80:83], v[224:227], v[16:19], v[80:83]\n\t"
    "s_waitcnt lgkmcnt(4)\n\t"
    "ds_read_b64_tr_b16 v[254:255], v50 offset:54024\n\t"
    "v_mfma_f32_16x16x32_bf16 v[84:87], v[228:231], v[16:19], v[84:87]\n\t"
    "v_exp_f32 v9, v9\n\t"
    "v_mfma_f32_16x16x32_bf16 v[88:91], v[232:235], v[16:19], v[88:91]\n\t"
    "ds_read_b128 a[36:39], v48 offset:256\n\t"
    "v_cvt_pk_bf16_f32 v20, v8, v9\n\t"
    "v_mfma_f32_16x16x32_bf16 v[92:95], v[236:239], v[16:19], v[92:95]\n\t"
    "ds_read_b128 a[40:43], v48 offset:512\n\t"
    "v_mfma_f32_16x16x32_bf16 v[96:99], v[240:243], v[16:19], v[96:99]\n\t"
    "ds_read_b128 a[44:47], v48 offset:768\n\t"
    "v_mfma_f32_16x16x32_bf16 v[100:103], v[244:247], v[16:19], v[100:103]\n\t"
    "s_waitcnt lgkmcnt(5)\n\t"
    "ds_read_b128 a[48:51], v48 offset:4096\n\t"
    "v_mfma_f32_16x16x32_bf16 v[104:107], v[248:251], v[16:19], v[104:107]\n\t"
    "s_waitcnt lgkmcnt(4)\n\t"
    "ds_read_b128 a[52:55], v48 offset:4352\n\t"
    "v_mfma_f32_16x16x32_bf16 v[108:111], v[252:255], v[16:19], v[108:111]\n\t"
    "ds_read_b128 a[56:59], v48 offset:4608\n\t"
    "v_mfma_f32_16x16x32_bf16 v[0:3], a[32:35], v[176:179], v[24:27]\n\t"
    "s_waitcnt lgkmcnt(5)\n\t"
    "ds_read_b128 a[60:63], v48 offset:4864\n\t"
    "v_mfma_f32_16x16x32_bf16 v[0:3], a[36:39], v[180:183], v[0:3]\n\t"
    "s_waitcnt lgkmcnt(5)\n\t"
    "v_exp_f32 v10, v10\n\t"
    "v_mfma_f32_16x16x32_bf16 v[0:3], a[40:43], v[184:187], v[0:3]\n\t"
    "s_waitcnt lgkmcnt(4)\n\t"
    "v_exp_f32 v11, v11\n\t"
    "v_mfma_f32_16x16x32_bf16 v[0:3], a[44:47], v[188:191], v[0:3]\n\t"
    "s_waitcnt lgkmcnt(3)\n\t"
    "v_exp_f32 v12, v12\n\t"
    "v_mfma_f32_16x16x32_bf16 v[4:7], a[48:51], v[176:179], v[24:27]\n\t"
    "s_waitcnt lgkmcnt(2)\n\t"
    "v_exp_f32 v13, v13\n\t"
    "v_mfma_f32_16x16x32_bf16 v[4:7], a[52:55], v[180:183], v[4:7]\n\t"
    "s_waitcnt lgkmcnt(1)\n\t"
    "v_exp_f32 v14, v14\n\t"
    "v_exp_f32 v15, v15\n\t"
    "v_mfma_f32_16x16x32_bf16 v[4:7], a[56:59], v[184:187], v[4:7]\n\t"
    "s_waitcnt lgkmcnt(0)\n\t"
    "v_cvt_pk_bf16_f32 v21, v10, v11\n\t"
    "v_cvt_pk_bf16_f32 v22, v12, v13\n\t"
    "v_cvt_pk_bf16_f32 v23, v14, v15\n\t"
    "v_mfma_f32_16x16x32_bf16 v[4:7], a[60:63], v[188:191], v[4:7]\n\t"
    "v_exp_f32 v0, v0\n\t"
    "v_mfma_f32_16x16x32_bf16 v[40:43], a[248:251], v[20:23], v[40:43]\n\t"
    "v_exp_f32 v1, v1\n\t"
    "v_mfma_f32_16x16x32_bf16 v[112:115], a[96:99], v[20:23], v[112:115]\n\t"
    "ds_read_b64_tr_b16 a[0:1], v50 offset:0\n\t"
    "v_cvt_pk_bf16_f32 v16, v0, v1\n\t"
    "v_mfma_f32_16x16x32_bf16 v[116:119], a[100:103], v[20:23], v[116:119]\n\t"
    "v_exp_f32 v2, v2\n\t"
    "v_mfma_f32_16x16x32_bf16 v[120:123], a[104:107], v[20:23], v[120:123]\n\t"
    "v_exp_f32 v3, v3\n\t"
    "v_mfma_f32_16x16x32_bf16 v[124:127], a[108:111], v[20:23], v[124:127]\n\t"
    "ds_read_b64_tr_b16 a[2:3], v50 offset:4096\n\t"
    "v_cvt_pk_bf16_f32 v17, v2, v3\n\t"
    "v_mfma_f32_16x16x32_bf16 v[128:131], a[112:115], v[20:23], v[128:131]\n\t"
    "ds_read_b64_tr_b16 a[4:5], v50 offset:8\n\t"
    "v_mfma_f32_16x16x32_bf16 v[132:135], a[116:119], v[20:23], v[132:135]\n\t"
    "ds_read_b64_tr_b16 a[6:7], v50 offset:4104\n\t"
    "v_mfma_f32_16x16x32_bf16 v[136:139], a[120:123], v[20:23], v[136:139]\n\t"
    "ds_read_b64_tr_b16 a[8:9], v50 offset:256\n\t"
    "v_mfma_f32_16x16x32_bf16 v[140:143], a[124:127], v[20:23], v[140:143]\n\t"
    "ds_read_b64_tr_b16 a[10:11], v50 offset:4352\n\t"
    "v_mfma_f32_16x16x32_bf16 v[8:11], a[192:195], v[192:195], v[28:31]\n\t"
    "ds_read_b64_tr_b16 a[12:13], v50 offset:264\n\t"
    "v_mfma_f32_16x16x32_bf16 v[8:11], a[196:199], v[196:199], v[8:11]\n\t"
    "ds_read_b64_tr_b16 a[14:15], v50 offset:4360\n\t"
    "v_mfma_f32_16x16x32_bf16 v[8:11], a[200:203], v[200:203], v[8:11]\n\t"
    "ds_read_b64_tr_b16 a[16:17], v50 offset:512\n\t"
    "v_mfma_f32_16x16x32_bf16 v[8:11], a[204:207], v[204:207], v[8:11]\n\t"
    "ds_read_b64_tr_b16 a[18:19], v50 offset:4608\n\t"
    "v_mfma_f32_16x16x32_bf16 v[12:15], a[208:211], v[192:195], v[28:31]\n\t"
    "ds_read_b64_tr_b16 a[20:21], v50 offset:520\n\t"
    "v_mfma_f32_16x16x32_bf16 v[12:15], a[212:215], v[196:199], v[12:15]\n\t"
    "ds_read_b64_tr_b16 a[22:23], v50 offset:4616\n\t"
    "v_exp_f32 v4, v4\n\t"
    "v_exp_f32 v5, v5\n\t"
    "v_exp_f32 v6, v6\n\t"
    "v_exp_f32 v7, v7\n\t"
    "v_mfma_f32_16x16x32_bf16 v[12:15], a[216:219], v[200:203], v[12:15]\n\t"
    "ds_read_b64_tr_b16 a[24:25], v50 offset:768\n\t"
    "v_cvt_pk_bf16_f32 v18, v4, v5\n\t"
    "v_cvt_pk_bf16_f32 v19, v6, v7\n\t"
    "v_mfma_f32_16x16x32_bf16 v[12:15], a[220:223], v[204:207], v[12:15]\n\t"
    "ds_read_b64_tr_b16 a[26:27], v50 offset:4864\n\t"
    "v_mfma_f32_16x16x32_bf16 v[36:39], a[248:251], v[16:19], v[36:39]\n\t"
    "s_waitcnt lgkmcnt(7)\n\t"
    "ds_read_b64_tr_b16 a[28:29], v50 offset:776\n\t"
    "v_mfma_f32_16x16x32_bf16 v[80:83], a[0:3], v[16:19], v[80:83]\n\t"
    "ds_read_b64_tr_b16 a[30:31], v50 offset:4872\n\t"
    "v_mfma_f32_16x16x32_bf16 v[84:87], a[4:7], v[16:19], v[84:87]\n\t"
    "v_exp_f32 v8, v8\n\t"
    "v_mfma_f32_16x16x32_bf16 v[88:91], a[8:11], v[16:19], v[88:91]\n\t"
    "s_waitcnt lgkmcnt(6)\n\t"
    "v_exp_f32 v9, v9\n\t"
    "v_mfma_f32_16x16x32_bf16 v[92:95], a[12:15], v[16:19], v[92:95]\n\t"
    "v_exp_f32 v10, v10\n\t"
    "v_mfma_f32_16x16x32_bf16 v[96:99], a[16:19], v[16:19], v[96:99]\n\t"
    "s_waitcnt lgkmcnt(4)\n\t"
    "v_exp_f32 v11, v11\n\t"
    "v_mfma_f32_16x16x32_bf16 v[100:103], a[20:23], v[16:19], v[100:103]\n\t"
    "s_waitcnt lgkmcnt(2)\n\t"
    "v_exp_f32 v12, v12\n\t"
    "v_mfma_f32_16x16x32_bf16 v[104:107], a[24:27], v[16:19], v[104:107]\n\t"
    "s_waitcnt lgkmcnt(0)\n\t"
    "v_exp_f32 v13, v13\n\t"
    "v_mfma_f32_16x16x32_bf16 v[108:111], a[28:31], v[16:19], v[108:111]\n\t"
    "v_exp_f32 v14, v14\n\t"
    "v_mfma_f32_16x16x32_bf16 v[0:3], a[32:35], v[192:195], v[28:31]\n\t"
    "v_exp_f32 v15, v15\n\t"
    "v_mfma_f32_16x16x32_bf16 v[0:3], a[36:39], v[196:199], v[0:3]\n\t"
    "v_cvt_pk_bf16_f32 v20, v8, v9\n\t"
    "v_cvt_pk_bf16_f32 v21, v10, v11\n\t"
    "v_mfma_f32_16x16x32_bf16 v[0:3], a[40:43], v[200:203], v[0:3]\n\t"
    "v_cvt_pk_bf16_f32 v22, v12, v13\n\t"
    "v_cvt_pk_bf16_f32 v23, v14, v15\n\t"
    "v_mfma_f32_16x16x32_bf16 v[0:3], a[44:47], v[204:207], v[0:3]\n\t"
    "v_rcp_f32 v62, v36\n\t"
    "v_mfma_f32_16x16x32_bf16 v[4:7], a[48:51], v[192:195], v[28:31]\n\t"
    "v_mul_f32 v80, v62, v80\n\t"
    "v_mul_f32 v81, v62, v81\n\t"
    "v_mfma_f32_16x16x32_bf16 v[4:7], a[52:55], v[196:199], v[4:7]\n\t"
    "v_mul_f32 v82, v62, v82\n\t"
    "v_mul_f32 v83, v62, v83\n\t"
    "v_mfma_f32_16x16x32_bf16 v[4:7], a[56:59], v[200:203], v[4:7]\n\t"
    "v_mul_f32 v84, v62, v84\n\t"
    "v_mul_f32 v85, v62, v85\n\t"
    "v_mfma_f32_16x16x32_bf16 v[4:7], a[60:63], v[204:207], v[4:7]\n\t"
    "v_mul_f32 v86, v62, v86\n\t"
    "v_mul_f32 v87, v62, v87\n\t"
    "v_mfma_f32_16x16x32_bf16 v[40:43], a[248:251], v[20:23], v[40:43]\n\t"
    "v_cvt_pk_bf16_f32 v80, v80, v81\n\t"
    "v_cvt_pk_bf16_f32 v81, v82, v83\n\t"
    "v_mfma_f32_16x16x32_bf16 v[112:115], v[224:227], v[20:23], v[112:115]\n\t"
    "v_cvt_pk_bf16_f32 v82, v84, v85\n\t"
    "v_cvt_pk_bf16_f32 v83, v86, v87\n\t"
    "v_mfma_f32_16x16x32_bf16 v[116:119], v[228:231], v[20:23], v[116:119]\n\t"
    "buffer_store_dwordx4 v[80:83], v54, s[44:47], s58 offen offset:0 sc1\n\t"
    "v_mfma_f32_16x16x32_bf16 v[120:123], v[232:235], v[20:23], v[120:123]\n\t"
    "v_mul_f32 v88, v62, v88\n\t"
    "v_mul_f32 v89, v62, v89\n\t"
    "v_mfma_f32_16x16x32_bf16 v[124:127], v[236:239], v[20:23], v[124:127]\n\t"
    "v_mul_f32 v90, v62, v90\n\t"
    "v_mul_f32 v91, v62, v91\n\t"
    "v_mfma_f32_16x16x32_bf16 v[128:131], v[240:243], v[20:23], v[128:131]\n\t"
    "v_mul_f32 v92, v62, v92\n\t"
    "v_mul_f32 v93, v62, v93\n\t"
    "v_mfma_f32_16x16x32_bf16 v[132:135], v[244:247], v[20:23], v[132:135]\n\t"
    "v_mul_f32 v94, v62, v94\n\t"
    "v_mul_f32 v95, v62, v95\n\t"
    "v_mfma_f32_16x16x32_bf16 v[136:139], v[248:251], v[20:23], v[136:139]\n\t"
    "v_cvt_pk_bf16_f32 v88, v88, v89\n\t"
    "v_cvt_pk_bf16_f32 v89, v90, v91\n\t"
    "v_mfma_f32_16x16x32_bf16 v[140:143], v[252:255], v[20:23], v[140:143]\n\t"
    "v_cvt_pk_bf16_f32 v90, v92, v93\n\t"
    "v_cvt_pk_bf16_f32 v91, v94, v95\n\t"
    "v_mfma_f32_16x16x32_bf16 v[8:11], a[64:67], v[208:211], v[32:35]\n\t"
    "buffer_store_dwordx4 v[88:91], v54, s[44:47], s58 offen offset:64 sc1\n\t"
    "v_mfma_f32_16x16x32_bf16 v[8:11], a[68:71], v[212:215], v[8:11]\n\t"
    "v_exp_f32 v0, v0\n\t"
    "v_mfma_f32_16x16x32_bf16 v[8:11], a[72:75], v[216:219], v[8:11]\n\t"
    "v_exp_f32 v1, v1\n\t"
    "v_mfma_f32_16x16x32_bf16 v[8:11], a[76:79], v[220:223], v[8:11]\n\t"
    "v_exp_f32 v2, v2\n\t"
    "v_mfma_f32_16x16x32_bf16 v[12:15], a[80:83], v[208:211], v[32:35]\n\t"
    "v_exp_f32 v3, v3\n\t"
    "v_mfma_f32_16x16x32_bf16 v[12:15], a[84:87], v[212:215], v[12:15]\n\t"
    "v_exp_f32 v4, v4\n\t"
    "v_exp_f32 v5, v5\n\t"
    "v_exp_f32 v6, v6\n\t"
    "v_exp_f32 v7, v7\n\t"
    "v_mfma_f32_16x16x32_bf16 v[12:15], a[88:91], v[216:219], v[12:15]\n\t"
    "v_cvt_pk_bf16_f32 v16, v0, v1\n\t"
    "v_cvt_pk_bf16_f32 v17, v2, v3\n\t"
    "v_cvt_pk_bf16_f32 v18, v4, v5\n\t"
    "v_cvt_pk_bf16_f32 v19, v6, v7\n\t"
    "v_mfma_f32_16x16x32_bf16 v[12:15], a[92:95], v[220:223], v[12:15]\n\t"
    "v_mul_f32 v96, v62, v96\n\t"
    "v_mul_f32 v97, v62, v97\n\t"
    "v_mul_f32 v98, v62, v98\n\t"
    "v_mul_f32 v99, v62, v99\n\t"
    "v_mul_f32 v100, v62, v100\n\t"
    "v_mul_f32 v101, v62, v101\n\t"
    "v_mul_f32 v102, v62, v102\n\t"
    "v_mul_f32 v103, v62, v103\n\t"
    "v_cvt_pk_bf16_f32 v96, v96, v97\n\t"
    "v_cvt_pk_bf16_f32 v97, v98, v99\n\t"
    "v_cvt_pk_bf16_f32 v98, v100, v101\n\t"
    "v_cvt_pk_bf16_f32 v99, v102, v103\n\t"
    "v_mfma_f32_16x16x32_bf16 v[40:43], a[248:251], v[16:19], v[40:43]\n\t"
    "buffer_store_dwordx4 v[96:99], v54, s[44:47], s58 offen offset:128 sc1\n\t"
    "v_mfma_f32_16x16x32_bf16 v[112:115], a[0:3], v[16:19], v[112:115]\n\t"
    "v_mul_f32 v104, v62, v104\n\t"
    "v_mul_f32 v105, v62, v105\n\t"
    "v_mfma_f32_16x16x32_bf16 v[116:119], a[4:7], v[16:19], v[116:119]\n\t"
    "v_mul_f32 v106, v62, v106\n\t"
    "v_mul_f32 v107, v62, v107\n\t"
    "v_mfma_f32_16x16x32_bf16 v[120:123], a[8:11], v[16:19], v[120:123]\n\t"
    "v_mul_f32 v108, v62, v108\n\t"
    "v_mul_f32 v109, v62, v109\n\t"
    "v_mfma_f32_16x16x32_bf16 v[124:127], a[12:15], v[16:19], v[124:127]\n\t"
    "v_mul_f32 v110, v62, v110\n\t"
    "v_mul_f32 v111, v62, v111\n\t"
    "v_mfma_f32_16x16x32_bf16 v[128:131], a[16:19], v[16:19], v[128:131]\n\t"
    "v_cvt_pk_bf16_f32 v104, v104, v105\n\t"
    "v_cvt_pk_bf16_f32 v105, v106, v107\n\t"
    "v_cvt_pk_bf16_f32 v106, v108, v109\n\t"
    "v_cvt_pk_bf16_f32 v107, v110, v111\n\t"
    "v_mfma_f32_16x16x32_bf16 v[132:135], a[20:23], v[16:19], v[132:135]\n\t"
    "buffer_store_dwordx4 v[104:107], v54, s[44:47], s58 offen offset:192 sc1\n\t"
    "v_mfma_f32_16x16x32_bf16 v[136:139], a[24:27], v[16:19], v[136:139]\n\t"
    "v_exp_f32 v8, v8\n\t"
    "v_mfma_f32_16x16x32_bf16 v[140:143], a[28:31], v[16:19], v[140:143]\n\t"
    "v_exp_f32 v9, v9\n\t"
    "v_mfma_f32_16x16x32_bf16 v[0:3], a[192:195], v[208:211], v[32:35]\n\t"
    "v_exp_f32 v10, v10\n\t"
    "v_mfma_f32_16x16x32_bf16 v[0:3], a[196:199], v[212:215], v[0:3]\n\t"
    "v_exp_f32 v11, v11\n\t"
    "v_mfma_f32_16x16x32_bf16 v[0:3], a[200:203], v[216:219], v[0:3]\n\t"
    "v_exp_f32 v12, v12\n\t"
    "v_mfma_f32_16x16x32_bf16 v[0:3], a[204:207], v[220:223], v[0:3]\n\t"
    "v_exp_f32 v13, v13\n\t"
    "v_mfma_f32_16x16x32_bf16 v[4:7], a[208:211], v[208:211], v[32:35]\n\t"
    "v_exp_f32 v14, v14\n\t"
    "v_mfma_f32_16x16x32_bf16 v[4:7], a[212:215], v[212:215], v[4:7]\n\t"
    "v_exp_f32 v15, v15\n\t"
    "v_mfma_f32_16x16x32_bf16 v[4:7], a[216:219], v[216:219], v[4:7]\n\t"
    "v_cvt_pk_bf16_f32 v20, v8, v9\n\t"
    "v_cvt_pk_bf16_f32 v21, v10, v11\n\t"
    "v_cvt_pk_bf16_f32 v22, v12, v13\n\t"
    "v_cvt_pk_bf16_f32 v23, v14, v15\n\t"
    "v_mfma_f32_16x16x32_bf16 v[4:7], a[220:223], v[220:223], v[4:7]\n\t"
    "v_rcp_f32 v63, v40\n\t"
    "v_mfma_f32_16x16x32_bf16 v[44:47], a[248:251], v[20:23], v[44:47]\n\t"
    "v_mul_f32 v112, v63, v112\n\t"
    "v_mul_f32 v113, v63, v113\n\t"
    "v_mfma_f32_16x16x32_bf16 v[144:147], a[96:99], v[20:23], v[144:147]\n\t"
    "v_mul_f32 v114, v63, v114\n\t"
    "v_mul_f32 v115, v63, v115\n\t"
    "v_mfma_f32_16x16x32_bf16 v[148:151], a[100:103], v[20:23], v[148:151]\n\t"
    "v_mul_f32 v116, v63, v116\n\t"
    "v_mul_f32 v117, v63, v117\n\t"
    "v_mfma_f32_16x16x32_bf16 v[152:155], a[104:107], v[20:23], v[152:155]\n\t"
    "v_mul_f32 v118, v63, v118\n\t"
    "v_mul_f32 v119, v63, v119\n\t"
    "v_mfma_f32_16x16x32_bf16 v[156:159], a[108:111], v[20:23], v[156:159]\n\t"
    "v_cvt_pk_bf16_f32 v112, v112, v113\n\t"
    "v_cvt_pk_bf16_f32 v113, v114, v115\n\t"
    "v_cvt_pk_bf16_f32 v114, v116, v117\n\t"
    "v_cvt_pk_bf16_f32 v115, v118, v119\n\t"
    "v_mfma_f32_16x16x32_bf16 v[160:163], a[112:115], v[20:23], v[160:163]\n\t"
    "buffer_store_dwordx4 v[112:115], v54, s[44:47], s59 offen offset:0 sc1\n\t"
    "v_mfma_f32_16x16x32_bf16 v[164:167], a[116:119], v[20:23], v[164:167]\n\t"
    "v_mul_f32 v120, v63, v120\n\t"
    "v_mul_f32 v121, v63, v121\n\t"
    "v_mfma_f32_16x16x32_bf16 v[168:171], a[120:123], v[20:23], v[168:171]\n\t"
    "v_mul_f32 v122, v63, v122\n\t"
    "v_mul_f32 v123, v63, v123\n\t"
    "v_mfma_f32_16x16x32_bf16 v[172:175], a[124:127], v[20:23], v[172:175]\n\t"
    "v_mul_f32 v124, v63, v124\n\t"
    "v_mul_f32 v125, v63, v125\n\t"
    "v_mfma_f32_16x16x32_bf16 v[8:11], a[32:35], v[208:211], v[32:35]\n\t"
    "v_mul_f32 v126, v63, v126\n\t"
    "v_mul_f32 v127, v63, v127\n\t"
    "v_mfma_f32_16x16x32_bf16 v[8:11], a[36:39], v[212:215], v[8:11]\n\t"
    "v_cvt_pk_bf16_f32 v120, v120, v121\n\t"
    "v_cvt_pk_bf16_f32 v121, v122, v123\n\t"
    "v_cvt_pk_bf16_f32 v122, v124, v125\n\t"
    "v_cvt_pk_bf16_f32 v123, v126, v127\n\t"
    "v_mfma_f32_16x16x32_bf16 v[8:11], a[40:43], v[216:219], v[8:11]\n\t"
    "buffer_store_dwordx4 v[120:123], v54, s[44:47], s59 offen offset:64 sc1\n\t"
    "v_mfma_f32_16x16x32_bf16 v[8:11], a[44:47], v[220:223], v[8:11]\n\t"
    "v_exp_f32 v0, v0\n\t"
    "v_mfma_f32_16x16x32_bf16 v[12:15], a[48:51], v[208:211], v[32:35]\n\t"
    "v_exp_f32 v1, v1\n\t"
    "v_mfma_f32_16x16x32_bf16 v[12:15], a[52:55], v[212:215], v[12:15]\n\t"
    "v_exp_f32 v2, v2\n\t"
    "v_exp_f32 v3, v3\n\t"
    "v_exp_f32 v4, v4\n\t"
    "v_exp_f32 v5, v5\n\t"
    "v_exp_f32 v6, v6\n\t"
    "v_exp_f32 v7, v7\n\t"
    "v_mfma_f32_16x16x32_bf16 v[12:15], a[56:59], v[216:219], v[12:15]\n\t"
    "v_cvt_pk_bf16_f32 v16, v0, v1\n\t"
    "v_cvt_pk_bf16_f32 v17, v2, v3\n\t"
    "v_cvt_pk_bf16_f32 v18, v4, v5\n\t"
    "v_cvt_pk_bf16_f32 v19, v6, v7\n\t"
    "v_mfma_f32_16x16x32_bf16 v[12:15], a[60:63], v[220:223], v[12:15]\n\t"
    "v_mul_f32 v128, v63, v128\n\t"
    "v_mul_f32 v129, v63, v129\n\t"
    "v_mul_f32 v130, v63, v130\n\t"
    "v_mul_f32 v131, v63, v131\n\t"
    "v_mul_f32 v132, v63, v132\n\t"
    "v_mul_f32 v133, v63, v133\n\t"
    "v_mul_f32 v134, v63, v134\n\t"
    "v_mul_f32 v135, v63, v135\n\t"
    "v_cvt_pk_bf16_f32 v128, v128, v129\n\t"
    "v_cvt_pk_bf16_f32 v129, v130, v131\n\t"
    "v_cvt_pk_bf16_f32 v130, v132, v133\n\t"
    "v_cvt_pk_bf16_f32 v131, v134, v135\n\t"
    "v_mfma_f32_16x16x32_bf16 v[44:47], a[248:251], v[16:19], v[44:47]\n\t"
    "buffer_store_dwordx4 v[128:131], v54, s[44:47], s59 offen offset:128 sc1\n\t"
    "v_mfma_f32_16x16x32_bf16 v[144:147], v[224:227], v[16:19], v[144:147]\n\t"
    "v_mul_f32 v136, v63, v136\n\t"
    "v_mul_f32 v137, v63, v137\n\t"
    "v_mfma_f32_16x16x32_bf16 v[148:151], v[228:231], v[16:19], v[148:151]\n\t"
    "v_mul_f32 v138, v63, v138\n\t"
    "v_mul_f32 v139, v63, v139\n\t"
    "v_mfma_f32_16x16x32_bf16 v[152:155], v[232:235], v[16:19], v[152:155]\n\t"
    "v_mul_f32 v140, v63, v140\n\t"
    "v_mul_f32 v141, v63, v141\n\t"
    "v_mfma_f32_16x16x32_bf16 v[156:159], v[236:239], v[16:19], v[156:159]\n\t"
    "v_mul_f32 v142, v63, v142\n\t"
    "v_mul_f32 v143, v63, v143\n\t"
    "v_mfma_f32_16x16x32_bf16 v[160:163], v[240:243], v[16:19], v[160:163]\n\t"
    "v_cvt_pk_bf16_f32 v136, v136, v137\n\t"
    "v_cvt_pk_bf16_f32 v137, v138, v139\n\t"
    "v_cvt_pk_bf16_f32 v138, v140, v141\n\t"
    "v_cvt_pk_bf16_f32 v139, v142, v143\n\t"
    "v_mfma_f32_16x16x32_bf16 v[164:167], v[244:247], v[16:19], v[164:167]\n\t"
    "buffer_store_dwordx4 v[136:139], v54, s[44:47], s59 offen offset:192 sc1\n\t"
    "v_exp_f32 v8, v8\n\t"
    "v_exp_f32 v9, v9\n\t"
    "v_exp_f32 v10, v10\n\t"
    "v_exp_f32 v11, v11\n\t"
    "v_exp_f32 v12, v12\n\t"
    "v_exp_f32 v13, v13\n\t"
    "v_exp_f32 v14, v14\n\t"
    "v_exp_f32 v15, v15\n\t"
    "v_mfma_f32_16x16x32_bf16 v[168:171], v[248:251], v[16:19], v[168:171]\n\t"
    "v_cvt_pk_bf16_f32 v20, v8, v9\n\t"
    "v_cvt_pk_bf16_f32 v21, v10, v11\n\t"
    "v_cvt_pk_bf16_f32 v22, v12, v13\n\t"
    "v_cvt_pk_bf16_f32 v23, v14, v15\n\t"
    "v_mfma_f32_16x16x32_bf16 v[172:175], v[252:255], v[16:19], v[172:175]\n\t"
    "v_cmp_nle_f32_e64 s[92:93], v36, s50\n\t"
    "v_cmp_nle_f32_e64 s[18:19], v62, 4.0\n\t"
    "v_mfma_f32_16x16x32_bf16 v[44:47], a[248:251], v[20:23], v[44:47]\n\t"
    "s_nop 0\n\t"
    "s_or_b64 s[92:93], s[92:93], s[18:19]\n\t"
    "v_cmp_nle_f32_e64 s[94:95], v40, s50\n\t"
    "v_cmp_nle_f32_e64 s[20:21], v63, 4.0\n\t"
    "v_mfma_f32_16x16x32_bf16 v[144:147], a[0:3], v[20:23], v[144:147]\n\t"
    "s_nop 0\n\t"
    "s_or_b64 s[94:95], s[94:95], s[20:21]\n\t"
    "v_mfma_f32_16x16x32_bf16 v[148:151], a[4:7], v[20:23], v[148:151]\n\t"
    "v_mfma_f32_16x16x32_bf16 v[152:155], a[8:11], v[20:23], v[152:155]\n\t"
    "v_mfma_f32_16x16x32_bf16 v[156:159], a[12:15], v[20:23], v[156:159]\n\t"
    "v_rcp_f32 v62, v44\n\t"
    "v_mfma_f32_16x16x32_bf16 v[160:163], a[16:19], v[20:23], v[160:163]\n\t"
    "v_cmp_nle_f32_e64 s[96:97], v44, s50\n\t"
    "v_cmp_nle_f32_e64 s[22:23], v62, 4.0\n\t"
    "v_mfma_f32_16x16x32_bf16 v[164:167], a[20:23], v[20:23], v[164:167]\n\t"
    "s_nop 0\n\t"
    "s_or_b64 s[96:97], s[96:97], s[22:23]\n\t"
    "v_mul_f32 v144, v62, v144\n\t"
    "v_mul_f32 v145, v62, v145\n\t"
    "v_mfma_f32_16x16x32_bf16 v[168:171], a[24:27], v[20:23], v[168:171]\n\t"
    "v_mul_f32 v146, v62, v146\n\t"
    "v_mul_f32 v147, v62, v147\n\t"
    "v_mfma_f32_16x16x32_bf16 v[172:175], a[28:31], v[20:23], v[172:175]\n\t"
    "v_mul_f32 v148, v62, v148\n\t"
    "v_mul_f32 v149, v62, v149\n\t"
    "v_mul_f32 v150, v62, v150\n\t"
    "v_mul_f32 v151, v62, v151\n\t"
    "v_cvt_pk_bf16_f32 v144, v144, v145\n\t"
    "v_cvt_pk_bf16_f32 v145, v146, v147\n\t"
    "v_cvt_pk_bf16_f32 v146, v148, v149\n\t"
    "v_cvt_pk_bf16_f32 v147, v150, v151\n\t"
    "v_pk_mul_f32 v[152:153], v[152:153], v[62:63] op_sel_hi:[1,0]\n\t"
    "v_pk_mul_f32 v[154:155], v[154:155], v[62:63] op_sel_hi:[1,0]\n\t"
    "v_pk_mul_f32 v[156:157], v[156:157], v[62:63] op_sel_hi:[1,0]\n\t"
    "v_pk_mul_f32 v[158:159], v[158:159], v[62:63] op_sel_hi:[1,0]\n\t"
    "v_cvt_pk_bf16_f32 v152, v152, v153\n\t"
    "v_cvt_pk_bf16_f32 v153, v154, v155\n\t"
    "v_cvt_pk_bf16_f32 v154, v156, v157\n\t"
    "v_cvt_pk_bf16_f32 v155, v158, v159\n\t"
    "v_pk_mul_f32 v[160:161], v[160:161], v[62:63] op_sel_hi:[1,0]\n\t"
    "v_pk_mul_f32 v[162:163], v[162:163], v[62:63] op_sel_hi:[1,0]\n\t"
    "v_cvt_pk_bf16_f32 v160, v160, v161\n\t"
    "v_cvt_pk_bf16_f32 v161, v162, v163\n\t"
    "buffer_store_dwordx4 v[144:147], v54, s[44:47], s60 offen offset:0 sc1\n\t"
    "buffer_store_dwordx4 v[152:155], v54, s[44:47], s60 offen offset:64 sc1\n\t"
    "v_pk_mul_f32 v[164:165], v[164:165], v[62:63] op_sel_hi:[1,0]\n\t"
    "v_pk_mul_f32 v[166:167], v[166:167], v[62:63] op_sel_hi:[1,0]\n\t"
    "v_cvt_pk_bf16_f32 v162, v164, v165\n\t"
    "v_cvt_pk_bf16_f32 v163, v166, v167\n\t"
    "buffer_store_dwordx4 v[160:163], v54, s[44:47], s60 offen offset:128 sc1\n\t"
    "v_pk_mul_f32 v[168:169], v[168:169], v[62:63] op_sel_hi:[1,0]\n\t"
    "v_pk_mul_f32 v[170:171], v[170:171], v[62:63] op_sel_hi:[1,0]\n\t"
    "v_cvt_pk_bf16_f32 v168, v168, v169\n\t"
    "v_cvt_pk_bf16_f32 v169, v170, v171\n\t"
    "v_pk_mul_f32 v[172:173], v[172:173], v[62:63] op_sel_hi:[1,0]\n\t"
    "v_pk_mul_f32 v[174:175], v[174:175], v[62:63] op_sel_hi:[1,0]\n\t"
    "v_cvt_pk_bf16_f32 v170, v172, v173\n\t"
    "v_cvt_pk_bf16_f32 v171, v174, v175\n\t"
    "buffer_store_dwordx4 v[168:171], v54, s[44:47], s60 offen offset:192 sc1\n\t"
    "s_mov_b64 s[62:63], s[80:81]\n\t"
    "s_or_b64 s[62:63], s[62:63], s[82:83]\n\t"
    "s_or_b64 s[62:63], s[62:63], s[84:85]\n\t"
    "s_or_b64 s[62:63], s[62:63], s[86:87]\n\t"
    "s_or_b64 s[62:63], s[62:63], s[88:89]\n\t"
    "s_or_b64 s[62:63], s[62:63], s[90:91]\n\t"
    "s_or_b64 s[62:63], s[62:63], s[92:93]\n\t"
    "s_or_b64 s[62:63], s[62:63], s[94:95]\n\t"
    "s_or_b64 s[62:63], s[62:63], s[96:97]\n\t"
    "s_cbranch_scc1 SLOW_%=\n\ts_endpgm\nSLOW_%=:\n\t"
    "s_mov_b32 %0, 0\n\t"
    "s_cmp_lg_u64 s[80:81], 0\n\ts_cselect_b32 s98, 1, 0\n\ts_or_b32 %0, %0, s98\n\t"
    "s_cmp_lg_u64 s[82:83], 0\n\ts_cselect_b32 s98, 2, 0\n\ts_or_b32 %0, %0, s98\n\t"
    "s_cmp_lg_u64 s[84:85], 0\n\ts_cselect_b32 s98, 4, 0\n\ts_or_b32 %0, %0, s98\n\t"
    "s_cmp_lg_u64 s[86:87], 0\n\ts_cselect_b32 s98, 8, 0\n\ts_or_b32 %0, %0, s98\n\t"
    "s_cmp_lg_u64 s[88:89], 0\n\ts_cselect_b32 s98, 16, 0\n\ts_or_b32 %0, %0, s98\n\t"
    "s_cmp_lg_u64 s[90:91], 0\n\ts_cselect_b32 s98, 32, 0\n\ts_or_b32 %0, %0, s98\n\t"
    "s_cmp_lg_u64 s[92:93], 0\n\ts_cselect_b32 s98, 64, 0\n\ts_or_b32 %0, %0, s98\n\t"
    "s_cmp_lg_u64 s[94:95], 0\n\ts_cselect_b32 s98, 128, 0\n\ts_or_b32 %0, %0, s98\n\t"
    "s_cmp_lg_u64 s[96:97], 0\n\ts_cselect_b32 s98, 256, 0\n\ts_or_b32 %0, %0, s98\n\t"
    "s_mov_b32 m0, s61\n\t"
    "s_waitcnt vmcnt(0) lgkmcnt(0)\n\t"
    : "=&s"(bad) : "s"(__builtin_amdgcn_kernarg_segment_ptr()), "s"(bh), "s"(wid), "s"(ldsb), "s"(blockIdx.x)
    : "memory", "vcc", "scc", "v0","v1","v2","v3","v4","v5","v6","v7","v8","v9","v10","v11","v12","v13","v14","v15","v16","v17","v18","v19","v20","v21","v22","v23","v24","v25","v26","v27","v28","v29","v30","v31","v32","v33","v34","v35","v36","v37","v38","v39","v40","v41","v42","v43","v44","v45","v46","v47","v48","v49","v50","v51","v52","v53","v54","v55","v56","v57","v58","v59","v60","v61","v62","v63","v64","v65","v66","v67","v68","v69","v70","v71","v72","v73","v74","v75","v76","v77","v78","v79","v80","v81","v82","v83","v84","v85","v86","v87","v88","v89","v90","v91","v92","v93","v94","v95","v96","v97","v98","v99","v100","v101","v102","v103","v104","v105","v106","v107","v108","v109","v110","v111","v112","v113","v114","v115","v116","v117","v118","v119","v120","v121","v122","v123","v124","v125","v126","v127","v128","v129","v130","v131","v132","v133","v134","v135","v136","v137","v138","v139","v140","v141","v142","v143","v144","v145","v146","v147","v148","v149","v150","v151","v152","v153","v154","v155","v156","v157","v158","v159","v160","v161","v162","v163","v164","v165","v166","v167","v168","v169","v170","v171","v172","v173","v174","v175","v176","v177","v178","v179","v180","v181","v182","v183","v184","v185","v186","v187","v188","v189","v190","v191","v192","v193","v194","v195","v196","v197","v198","v199","v200","v201","v202","v203","v204","v205","v206","v207","v208","v209","v210","v211","v212","v213","v214","v215","v216","v217","v218","v219","v220","v221","v222","v223","v224","v225","v226","v227","v228","v229","v230","v231","v232","v233","v234","v235","v236","v237","v238","v239","v240","v241","v242","v243","v244","v245","v246","v247","v248","v249","v250","v251","v252","v253","v254","v255","a0","a1","a2","a3","a4","a5","a6","a7","a8","a9","a10","a11","a12","a13","a14","a15","a16","a17","a18","a19","a20","a21","a22","a23","a24","a25","a26","a27","a28","a29","a30","a31","a32","a33","a34","a35","a36","a37","a38","a39","a40","a41","a42","a43","a44","a45","a46","a47","a48","a49","a50","a51","a52","a53","a54","a55","a56","a57","a58","a59","a60","a61","a62","a63","a64","a65","a66","a67","a68","a69","a70","a71","a72","a73","a74","a75","a76","a77","a78","a79","a80","a81","a82","a83","a84","a85","a86","a87","a88","a89","a90","a91","a92","a93","a94","a95","a96","a97","a98","a99","a100","a101","a102","a103","a104","a105","a106","a107","a108","a109","a110","a111","a112","a113","a114","a115","a116","a117","a118","a119","a120","a121","a122","a123","a124","a125","a126","a127","a128","a129","a130","a131","a132","a133","a134","a135","a136","a137","a138","a139","a140","a141","a142","a143","a144","a145","a146","a147","a148","a149","a150","a151","a152","a153","a154","a155","a156","a157","a158","a159","a160","a161","a162","a163","a164","a165","a166","a167","a168","a169","a170","a171","a172","a173","a174","a175","a176","a177","a178","a179","a180","a181","a182","a183","a184","a185","a186","a187","a188","a189","a190","a191","a192","a193","a194","a195","a196","a197","a198","a199","a200","a201","a202","a203","a204","a205","a206","a207","a208","a209","a210","a211","a212","a213","a214","a215","a216","a217","a218","a219","a220","a221","a222","a223","a224","a225","a226","a227","a228","a229","a230","a231","a232","a233","a234","a235","a236","a237","a238","a239","a240","a241","a242","a243","a244","a245","a246","a247","a248","a249","a250","a251","a252","a253","a254","a255","s14","s15","s16","s17","s18","s19","s20","s21","s22","s23","s24","s25","s26","s27","s28","s29","s30","s31","s36","s37","s38","s39","s40","s41","s42","s43","s44","s45","s46","s47","s48","s49","s50","s51","s52","s53","s54","s55","s56","s57","s58","s59","s60","s61","s62","s63","s64","s65","s66","s67","s68","s69","s70","s71","s72","s73","s74","s75","s76","s77","s78","s79","s80","s81","s82","s83","s84","s85","s86","s87","s88","s89","s90","s91","s92","s93","s94","s95","s96","s97","s98","s99");
  unsigned lane; asm volatile("v_mbcnt_lo_u32_b32 %0, -1, 0\n\tv_mbcnt_hi_u32_b32 %0, -1, %0" : "=v"(lane));
  {
    unsigned short* const volatile __attribute__((address_space(4)))* ka = (unsigned short* const volatile __attribute__((address_space(4)))*)__builtin_amdgcn_kernarg_segment_ptr();
    Q = ka[0]; K = ka[1]; V = ka[2]; O = ka[3];
  }
  const unsigned short* Kb = K + (size_t)bh * (NK * D); const unsigned short* Vb = V + (size_t)bh * (NK * D);
  for (int t = 0; t < 9; ++t) {
    if (!((bad >> t) & 1)) continue;
    const int row0 = t < 6 ? 16 * 6 * wid + 16 * t : 64 * 6 + 16 * (9 - 6) * wid + 16 * (t - 6);
    const size_t off = ((size_t)bh * NQ + row0) * D;
    slow_tile(Q + off, Kb, Vb, O + off, lds + ((wid >> 1) * 81920 + ((wid & 1) ? 16384 : 8192)), (int)lane);
  }
}

extern "C" void kernel_launch(void* const* d_in, const int* in_sizes, int n_in, void* d_out, int out_size, void* d_ws, size_t ws_size, hipStream_t stream) {
  gqa_attn_kernel<<<256, 256, 0, stream>>>((const unsigned short*)d_in[0], (const unsigned short*)d_in[1], (const unsigned short*)d_in[2], (unsigned short*)d_out
     );
}
```
